```python
import torch

def _softmax(x, axis=-1):
    return torch.softmax(x, dim=axis)
def _gelu(x):
    return torch.nn.functional.gelu(x, approximate="tanh")
def _silu(x):
    return torch.nn.functional.silu(x)
def _sigmoid(x):
    return torch.sigmoid(x)
def _layernorm(x, axis=-1, eps=1e-5):
    m = x.mean(dim=axis, keepdim=True)
    v = x.var(dim=axis, keepdim=True, unbiased=False)
    return (x - m) / torch.sqrt(v + eps)
def _scatter_add(tgt, idx, src, axis=0):
    return tgt.clone().index_add_(axis, idx, src)
def _segment_sum(src, seg, n):
    out = torch.zeros((n,) + src.shape[1:], dtype=src.dtype)
    return out.index_add_(0, seg, src)
def _pad(x, pw):
    flat = tuple(v for pair in reversed(pw) for v in pair)
    return torch.nn.functional.pad(x, flat)

kernel_name = 'interleave_layers__gqa_block_cached__dense_activation_silu__decode'

def setup_inputs():

    _w = (lambda a: (lambda t: t.to(torch.bfloat16) if t.is_floating_point() else t)(torch.as_tensor(a)))
    def _A_setup():
        B, Sq, D = 8, 1, 512
        Hq, Hkv, Sk, Dh = 32, 32, 4096, 128
        assert Hq % Hkv == 0, (Hq, Hkv)
        return {
            "x": _w((lambda *s: torch.randn(*s, dtype=torch.bfloat16) * 0.1)(B, Sq, D)),
            "k_cache": _w((lambda *s: torch.randn(*s, dtype=torch.bfloat16) * 0.8)(B, Hkv, Sk, Dh)),
            "v_cache": _w((lambda *s: torch.randn(*s, dtype=torch.bfloat16) * 1.6)(B, Hkv, Sk, Dh)),
            "wq": _w((lambda *s: torch.randn(*s, dtype=torch.bfloat16) * 0.8)(D, Hq * Dh) * (D ** -0.5)),
            "wo": _w((lambda *s: torch.randn(*s, dtype=torch.bfloat16) * 1.6)(Hq * Dh, D) * ((Hq * Dh) ** -0.5)),
        }
    _A_in = _A_setup()

    def _B_setup():
        B, S, D, F = 8, 1, 512, 1376
        return {
            "x": _w((lambda *s: torch.randn(*s, dtype=torch.bfloat16) * 0.1)(B, S, D)),
            "w1": _w((lambda *s: torch.randn(*s, dtype=torch.bfloat16) * 0.4)(D, F) * (D ** -0.5)),
            "w2": _w((lambda *s: torch.randn(*s, dtype=torch.bfloat16) * 0.4)(F, D) * (F ** -0.5)),
        }
    _B_in = _B_setup()

    return {
        "A_x": _A_in["x"],
        "A_k_cache": _A_in["k_cache"],
        "A_v_cache": _A_in["v_cache"],
        "A_wq": _A_in["wq"],
        "A_wo": _A_in["wo"],
        "B_x": _B_in["x"],
        "B_w1": _B_in["w1"],
        "B_w2": _B_in["w2"],
    }

def reference(A_x, A_k_cache, A_v_cache, A_wq, A_wo, B_x, B_w1, B_w2):
    def _A_ref(x, k_cache, v_cache, wq, wo):
        B, Sq, D = x.shape
        Hq, Dh = 32, 128
        Hkv = k_cache.shape[1]
        rep = Hq // Hkv
        q = (lambda x, *d: x.permute(*d))((x @ wq).reshape(B, Sq, Hq, Dh), 0, 2, 1, 3)
        k = (lambda x, n, axis: torch.repeat_interleave(x, n, dim=axis))(k_cache, rep, axis=1)
        v = (lambda x, n, axis: torch.repeat_interleave(x, n, dim=axis))(v_cache, rep, axis=1)
        scores = torch.einsum("bhqd,bhkd->bhqk", q, k) * (Dh ** -0.5)
        p = _softmax(scores, axis=-1)
        o = torch.einsum("bhqk,bhkd->bhqd", p, v)
        return (lambda x, *d: x.permute(*d))(o, 0, 2, 1, 3).reshape(B, Sq, Hq * Dh) @ wo

    def _B_ref(x, w1, w2):
        h = torch.einsum("bsd,df->bsf", x, w1)
        h = _silu(h)
        out = torch.einsum("bsf,fd->bsd", h, w2)
        return out

    x = A_x
    x = x + _A_ref(_layernorm(x, axis=-1), A_k_cache, A_v_cache, A_wq, A_wo)
    x = x + _B_ref(_layernorm(x, axis=-1), B_w1, B_w2)
    x = x + _A_ref(_layernorm(x, axis=-1), A_k_cache, A_v_cache, A_wq, A_wo)
    x = x + _B_ref(_layernorm(x, axis=-1), B_w1, B_w2)
    return x
```

```cpp
#include <hip/hip_runtime.h>
#include <hip/hip_bf16.h>
#include <stdint.h>

typedef __bf16 bf16x8 __attribute__((ext_vector_type(8)));
typedef float f32x4 __attribute__((ext_vector_type(4)));
typedef unsigned u32x4 __attribute__((ext_vector_type(4)));
typedef unsigned short u16;

constexpr int B = 8, D = 512, H = 32, SK = 4096, DH = 128, F = 1376;
constexpr int NSPL = 16;

__device__ __forceinline__ float bf2f(u16 v) { return __uint_as_float((unsigned)v << 16); }
__device__ __forceinline__ u16 f2bf(float f) { return __builtin_bit_cast(u16, (__bf16)f); }
__device__ __forceinline__ float rbf(float f) { return bf2f(f2bf(f)); }


template <int ACT>
__global__ __launch_bounds__(256) void ln_gemv(const float* __restrict__ x, const u16* __restrict__ W, float* __restrict__ out, int N) {
  __shared__ float ln[B][D];
  __shared__ float red[4][B][128];
  const int tid = threadIdx.x, lane = tid & 63, wave = tid >> 6;
  for (int b = wave; b < B; b += 4) {
    float v[8]; float s = 0.f;
    for (int i = 0; i < 8; ++i) { v[i] = x[b * D + lane + 64 * i]; s += v[i]; }
    for (int o = 32; o; o >>= 1) s += __shfl_xor(s, o);
    const float m = s * (1.f / D);
    float q = 0.f;
    for (int i = 0; i < 8; ++i) { float d = v[i] - m; q += d * d; }
    for (int o = 32; o; o >>= 1) q += __shfl_xor(q, o);
    const float rs = 1.f / sqrtf(q * (1.f / D) + 1e-5f);
    for (int i = 0; i < 8; ++i) ln[b][lane + 64 * i] = rbf((v[i] - m) * rs);
  }
  __syncthreads();
  const int n0 = blockIdx.x * 128 + lane * 2;
  float acc[B][2];
  for (int b = 0; b < B; ++b) acc[b][0] = acc[b][1] = 0.f;
  if (n0 < N) {
#pragma unroll 8
    for (int k = wave * 128; k < wave * 128 + 128; ++k) {
      const unsigned w = *(const unsigned*)(W + (size_t)k * N + n0);
      const float w0 = __uint_as_float(w << 16), w1 = __uint_as_float(w & 0xffff0000u);
#pragma unroll
      for (int b = 0; b < B; ++b) { acc[b][0] += ln[b][k] * w0; acc[b][1] += ln[b][k] * w1; }
    }
  }
  for (int b = 0; b < B; ++b) { red[wave][b][lane * 2] = acc[b][0]; red[wave][b][lane * 2 + 1] = acc[b][1]; }
  __syncthreads();
  for (int i = tid; i < B * 128; i += 256) {
    const int b = i >> 7, c = i & 127, n = blockIdx.x * 128 + c;
    if (n < N) {
      float s = rbf(red[0][b][c] + red[1][b][c] + red[2][b][c] + red[3][b][c]);
      if (ACT) s = rbf(s / (1.f + __expf(-s)));
      out[b * N + n] = s;
    }
  }
}

__global__ __launch_bounds__(256) void res_gemv(const float* __restrict__ in, const u16* __restrict__ W, const float* __restrict__ xin, float* __restrict__ xout, u16* __restrict__ final_out, int K) {
  __shared__ float red[32][B][16];
  const int tid = threadIdx.x;
  const int cp = tid & 7, kg = tid >> 3;
  const int n0 = blockIdx.x * 16 + cp * 2;
  float acc[B][2];
  for (int b = 0; b < B; ++b) acc[b][0] = acc[b][1] = 0.f;
#pragma unroll 4
  for (int k = kg; k < K; k += 32) {
    const unsigned w = *(const unsigned*)(W + (size_t)k * D + n0);
    const float w0 = __uint_as_float(w << 16), w1 = __uint_as_float(w & 0xffff0000u);
#pragma unroll
    for (int b = 0; b < B; ++b) { const float a = in[b * K + k]; acc[b][0] += a * w0; acc[b][1] += a * w1; }
  }
  for (int b = 0; b < B; ++b) { red[kg][b][cp * 2] = acc[b][0]; red[kg][b][cp * 2 + 1] = acc[b][1]; }
  __syncthreads();
  if (tid < B * 16) {
    const int b = tid >> 4, c = tid & 15, n = blockIdx.x * 16 + c;
    float s = 0.f;
    for (int g = 0; g < 32; ++g) s += red[g][b][c];
    const float r = rbf(xin[b * D + n] + rbf(s));
    xout[b * D + n] = r;
    if (final_out) final_out[b * D + n] = f2bf(r);
  }
}

__global__ void cvt_x(const u16* __restrict__ x, float* __restrict__ xf) { const int i = blockIdx.x * 256 + threadIdx.x; xf[i] = bf2f(x[i]); }

template <int CTRL> __device__ __forceinline__ unsigned dppu(unsigned v) { return (unsigned)__builtin_amdgcn_update_dpp(0, (int)v, CTRL, 0xf, 0xf, true); }
template <int CTRL> __device__ __forceinline__ float dppf(float v) { return __uint_as_float(dppu<CTRL>(__float_as_uint(v))); }

__global__ __launch_bounds__(256) void attn_part(const float* __restrict__ q, const u16* __restrict__ Kc, const u16* __restrict__ Vc, float* __restrict__ part) {
  const int lane = threadIdx.x & 63, wave = threadIdx.x >> 6;
  const int wid = blockIdx.x * 4 + wave;
  const int bh = wid / NSPL, sp = wid % NSPL;
  const int b = bh / H, h = bh % H;
  const int g = lane >> 4, c = lane & 15;
  u32x4 qc;
  {
    const float* qp = q + b * (H * DH) + h * DH + 8 * (4 * g + (c & 3));
    for (int j = 0; j < 4; ++j) qc[j] = (unsigned)f2bf(qp[2 * j]) | ((unsigned)f2bf(qp[2 * j + 1]) << 16);
  }
  u32x4 bq[4];
  for (int i = 0; i < 4; ++i) bq[i] = ((c >> 2) == i) ? qc : u32x4{0, 0, 0, 0};
  const char* kb = (const char*)Kc + ((size_t)bh * SK + sp * 256) * 256 + ((c >> 2) * 256 + g * 64 + (c & 3) * 16);
  const char* vb = (const char*)Vc + ((size_t)bh * SK + sp * 256) * 256 + lane * 16;
  float sc[16];
#pragma unroll
  for (int t = 0; t < 16; ++t) {
    f32x4 d = {0, 0, 0, 0};
#pragma unroll
    for (int i = 0; i < 4; ++i) {
      const u32x4 kk = __builtin_nontemporal_load((const u32x4*)(kb + (t * 4 + i) * 1024));
      d = __builtin_amdgcn_mfma_f32_16x16x32_bf16(__builtin_bit_cast(bf16x8, kk), __builtin_bit_cast(bf16x8, bq[i]), d, 0, 0, 0);
    }
    float v = (c & 2) ? ((c & 1) ? d[3] : d[2]) : ((c & 1) ? d[1] : d[0]);
    v += dppf<0xB1>(v);
    v += dppf<0x4E>(v);
    sc[t] = v * (0.08838834764831845f * 1.4426950408889634f);
  }
  float m = sc[0];
#pragma unroll
  for (int t = 1; t < 16; ++t) m = fmaxf(m, sc[t]);
  for (int o = 32; o; o >>= 1) m = fmaxf(m, __shfl_xor(m, o));
  u32x4 msk;
  for (int w = 0; w < 4; ++w) msk[w] = ((c >> 1) == w) ? ((c & 1) ? 0xffff0000u : 0x0000ffffu) : 0u;
  float l = 0.f;
  f32x4 acc = {0, 0, 0, 0};
#pragma unroll
  for (int t = 0; t < 16; ++t) {
    const float p = exp2f(sc[t] - m);
    const unsigned pb = f2bf(p);
    l += bf2f(pb);
    const unsigned pp = pb | (pb << 16);
#define PVQ(I) { const unsigned pbr = dppu<0x150 + 4 * I>(pp); u32x4 bb; for (int w = 0; w < 4; ++w) bb[w] = pbr & msk[w]; \
      const u32x4 vv = __builtin_nontemporal_load((const u32x4*)(vb + (t * 4 + I) * 1024)); \
      acc = __builtin_amdgcn_mfma_f32_16x16x32_bf16(__builtin_bit_cast(bf16x8, vv), __builtin_bit_cast(bf16x8, bb), acc, 0, 0, 0); }
    PVQ(0) PVQ(1) PVQ(2) PVQ(3)
  }
  for (int o = 32; o; o >>= 1) l += __shfl_xor(l, o);
  l *= 0.25f;
  float* pr = part + (size_t)wid * 132;
  if (c < 8) for (int r = 0; r < 4; ++r) pr[4 + 32 * g + 8 * r + c] = acc[r];
  if (lane == 0) { pr[0] = m; pr[1] = l; }
}

__global__ __launch_bounds__(128) void attn_comb(const float* __restrict__ part, float* __restrict__ o) {
  const int bh = blockIdx.x, d = threadIdx.x;
  const float* pr = part + (size_t)bh * NSPL * 132;
  float M = -INFINITY;
  for (int s = 0; s < NSPL; ++s) M = fmaxf(M, pr[s * 132]);
  float L = 0.f, a = 0.f;
  for (int s = 0; s < NSPL; ++s) { const float w = exp2f(pr[s * 132] - M); L += w * pr[s * 132 + 1]; a += w * pr[s * 132 + 4 + d]; }
  o[bh * DH + d] = rbf(a / L);
}

extern "C" void kernel_launch(void* const* d_in, const int* in_sizes, int n_in, void* d_out, int out_size, void* d_ws, size_t ws_size, hipStream_t stream) {
  const u16* x = (const u16*)d_in[0]; const u16* Kc = (const u16*)d_in[1]; const u16* Vc = (const u16*)d_in[2];
  const u16* wq = (const u16*)d_in[3]; const u16* wo = (const u16*)d_in[4]; const u16* w1 = (const u16*)d_in[6]; const u16* w2 = (const u16*)d_in[7];
  float* ws = (float*)d_ws;
  float* xa = ws; float* xb = ws + 4096; float* q = ws + 8192; float* o = q + 32768; float* hh = o + 32768; float* part = hh + 16384;
  cvt_x<<<16, 256, 0, stream>>>(x, xa);
  for (int layer = 0; layer < 2; ++layer) {
    ln_gemv<0><<<32, 256, 0, stream>>>(xa, wq, q, 4096);
    attn_part<<<1024, 256, 0, stream>>>(q, Kc, Vc, part);
    attn_comb<<<256, 128, 0, stream>>>(part, o);
    res_gemv<<<32, 256, 0, stream>>>(o, wo, xa, xb, nullptr, 4096);
    ln_gemv<1><<<11, 256, 0, stream>>>(xb, w1, hh, F);
    res_gemv<<<32, 256, 0, stream>>>(hh, w2, xb, xa, layer == 1 ? (u16*)d_out : nullptr, F);
  }
}
```

```cpp
#include <hip/hip_runtime.h>
#include <hip/hip_bf16.h>
#include <stdint.h>

typedef __bf16 bf16x8 __attribute__((ext_vector_type(8)));
typedef float f32x4 __attribute__((ext_vector_type(4)));
typedef unsigned u32x4 __attribute__((ext_vector_type(4)));
typedef unsigned u32x2 __attribute__((ext_vector_type(2)));
typedef short s16x4 __attribute__((ext_vector_type(4)));
typedef unsigned short u16;
#define LAS __attribute__((address_space(3)))
#define GAS __attribute__((address_space(1)))

constexpr int NB = 8, DM = 512, NH = 32, SK = 4096, DH = 128, FF = 1376;
constexpr int NCHAIN = 32, NSUB = 7;
constexpr int WS_TMO = 0;
constexpr int WS_Q = 256;
constexpr int WS_X1 = WS_Q + 16 * 32 * 512;
constexpr int WS_X2 = WS_X1 + 9 * 32768;
constexpr int WS_PART = WS_X2 + 9 * 32768;
constexpr int WS_END = WS_PART + 16 * 32 * 4096;
constexpr int WS_DBG = WS_END;
constexpr unsigned SPIN_CAP = 4000000u;
constexpr int AUX_SC1 = 16;
constexpr float SC2 = 0.08838834764831845f * 1.4426950408889634f;

struct Params { const u16 *x, *K, *V, *wq, *wo, *w1, *w2; u16* out; unsigned char* ws; };

__device__ __forceinline__ float bf2f(unsigned v) { return __uint_as_float(v << 16); }
__device__ __forceinline__ unsigned f2bf(float f) { return (unsigned)__builtin_bit_cast(u16, (__bf16)f); }
__device__ __forceinline__ float rbf(float f) { return bf2f(f2bf(f)); }
__device__ __forceinline__ f32x4 mfma(u32x4 a, u32x4 b, f32x4 c) { return __builtin_amdgcn_mfma_f32_16x16x32_bf16(__builtin_bit_cast(bf16x8, a), __builtin_bit_cast(bf16x8, b), c, 0, 0, 0); }
template <int CTRL> __device__ __forceinline__ unsigned dppu(unsigned v) { return (unsigned)__builtin_amdgcn_update_dpp(0, (int)v, CTRL, 0xf, 0xf, true); }
template <int CTRL> __device__ __forceinline__ float dppf(float v) { return __uint_as_float(dppu<CTRL>(__float_as_uint(v))); }
__device__ __forceinline__ float wave_sum(float v) { for (int o = 32; o; o >>= 1) v += __shfl_xor(v, o); return v; }
__device__ __forceinline__ float wave_max(float v) { for (int o = 32; o; o >>= 1) v = fmaxf(v, __shfl_xor(v, o)); return v; }
__device__ __forceinline__ unsigned tagf(float f) { return __float_as_uint(f) | 1u; }
#define MEMBAR() asm volatile("" ::: "memory")
typedef __amdgpu_buffer_rsrc_t rsrc_t;
__device__ __forceinline__ u32x4 ld128(rsrc_t rs, int off) { return __builtin_amdgcn_raw_buffer_load_b128(rs, off, 0, AUX_SC1); }
__device__ __forceinline__ u32x2 ld64(rsrc_t rs, int off) { return __builtin_amdgcn_raw_buffer_load_b64(rs, off, 0, AUX_SC1); }
__device__ __forceinline__ unsigned ld32(rsrc_t rs, int off) { return __builtin_amdgcn_raw_buffer_load_b32(rs, off, 0, AUX_SC1); }
__device__ __forceinline__ void st32(rsrc_t rs, int off, unsigned v) { __builtin_amdgcn_raw_buffer_store_b32(v, rs, off, 0, AUX_SC1); }
__device__ __forceinline__ bool spin_fail(unsigned& spins, rsrc_t rs) {
  ++spins;
  if (spins > SPIN_CAP) { st32(rs, WS_TMO, 1u); return true; }
  if ((spins & 1023u) == 0u) { if (__builtin_amdgcn_readfirstlane(ld32(rs, WS_TMO)) != 0u) return true; }
  return false;
}
#define STAMP(idx) do {} while (0)

constexpr int L_WO = 0;
constexpr int L_STG = 131072;
constexpr int L_XIN = L_STG;
constexpr int L_H = L_STG + 8192;
constexpr int L_RED = L_STG + 9216;
constexpr int L_CHAIN_END = L_STG + 24576;

template <int NT> __device__ __forceinline__ void tr_frags(LAS char* stg, int lane, u32x4 (&out)[2][NT]) {
  const int g = lane >> 4, q = (lane & 15) >> 2, pp = lane & 3;
#pragma unroll
  for (int ks = 0; ks < 2; ++ks)
#pragma unroll
    for (int tl = 0; tl < NT; ++tl) {
      const s16x4 lo = __builtin_amdgcn_ds_read_tr16_b64_v4i16((LAS s16x4*)(stg + (32 * ks + 8 * g + q) * (NT * 32) + tl * 32 + 8 * pp));
      const s16x4 hi = __builtin_amdgcn_ds_read_tr16_b64_v4i16((LAS s16x4*)(stg + (32 * ks + 8 * g + 4 + q) * (NT * 32) + tl * 32 + 8 * pp));
      const u32x2 a = __builtin_bit_cast(u32x2, lo), b = __builtin_bit_cast(u32x2, hi);
      out[ks][tl] = u32x4{a.x, a.y, b.x, b.y};
    }
}
template <int NKS, typename SINK> __device__ __forceinline__ void load_w64(const u16* base, int pitch_elems, int nvalid, LAS char* stg, int lane, SINK sink) {
  u32x4 raw[NKS * 2];
#pragma unroll
  for (int i = 0; i < NKS * 2; ++i) {
    const int row = (lane >> 2) + 16 * i;
    const int rc = row < nvalid ? row : 0;
    const u32x4 v = *(const u32x4*)(base + (size_t)rc * pitch_elems + (lane & 3) * 8);
    raw[i] = row < nvalid ? v : u32x4{0, 0, 0, 0};
  }
#pragma unroll
  for (int c2 = 0; c2 < NKS / 2; ++c2) {
#pragma unroll
    for (int i = 0; i < 4; ++i) *(LAS u32x4*)(stg + ((lane >> 2) + 16 * i) * 64 + (lane & 3) * 16) = raw[c2 * 4 + i];
    __builtin_amdgcn_fence(__ATOMIC_RELEASE, "wavefront"); asm volatile("s_waitcnt lgkmcnt(0)" ::: "memory");
    u32x4 fr[2][2];
    tr_frags<2>(stg, lane, fr);
    sink(c2 * 2, fr[0][0], fr[0][1]); sink(c2 * 2 + 1, fr[1][0], fr[1][1]);
    asm volatile("s_waitcnt lgkmcnt(0)" ::: "memory");
  }
}

__device__ __forceinline__ void chain_role(const Params& p, LAS char* L) {
  const int tid = threadIdx.x, lane = tid & 63, w = __builtin_amdgcn_readfirstlane(tid >> 6);
  const int c = blockIdx.x, rg = c >> 2, cg = c & 3;
  const int g = lane >> 4, c16 = lane & 15;
  const rsrc_t rs = __builtin_amdgcn_make_buffer_rsrc(p.ws, 0, WS_END, 0x00020000);
  LAS char* stg = L + L_STG + w * 6144;
  u32x4 wqf[16][2], w1f[16][3], w2f[6][2];
  STAMP(0);
  load_w64<16>(p.wq + 128 * c + 32 * w, NH * DH, 512, stg, lane, [&](int ks, u32x4 a, u32x4 b) { wqf[ks][0] = a; wqf[ks][1] = b; });
  __syncthreads();
  for (int b = w; b < NB; b += 4) {
    const u32x4 xv = *(const u32x4*)(p.x + b * DM + lane * 8);
    float v[8];
#pragma unroll
    for (int j = 0; j < 4; ++j) { v[2 * j] = bf2f(xv[j] & 0xffffu); v[2 * j + 1] = bf2f(xv[j] >> 16); }
    float s = 0.f;
#pragma unroll
    for (int j = 0; j < 8; ++j) s += v[j];
    const float m = wave_sum(s) * (1.f / DM);
    float q = 0.f;
#pragma unroll
    for (int j = 0; j < 8; ++j) { const float d = v[j] - m; q += d * d; }
    const float rstd = 1.f / sqrtf(wave_sum(q) * (1.f / DM) + 1e-5f);
    u32x4 o;
#pragma unroll
    for (int j = 0; j < 4; ++j) o[j] = f2bf((v[2 * j] - m) * rstd) | (f2bf((v[2 * j + 1] - m) * rstd) << 16);
    *(LAS u32x4*)(L + L_XIN + b * 1024 + lane * 16) = o;
  }
  __syncthreads();
  {
    f32x4 a0 = {0, 0, 0, 0}, a1 = {0, 0, 0, 0};
#pragma unroll
    for (int ks = 0; ks < 16; ++ks) {
      const u32x4 af = *(LAS u32x4*)(L + L_XIN + (c16 & 7) * 1024 + ks * 64 + g * 16);
      a0 = mfma(af, wqf[ks][0], a0); a1 = mfma(af, wqf[ks][1], a1);
    }
    const f32x4 a = (g >> 1) ? a1 : a0;
#pragma unroll
    for (int i = 0; i < 4; ++i) {
      const int b = 4 * (g & 1) + i;
      st32(rs, WS_Q + ((2 * b) * 32 + c) * 512 + (32 * w + 16 * (g >> 1) + c16) * 4, (f2bf(a[i]) << 16) | 1u);
    }
  }
  STAMP(1);
  __syncthreads();
  load_w64<16>(p.wo + (size_t)(512 * rg) * DM + 128 * cg + 32 * w, DM, 512, stg, lane, [&](int ks, u32x4 a, u32x4 b) {
    *(LAS u32x4*)(L + L_WO + ((w * 16 + ks) * 2 + 0) * 1024 + lane * 16) = a; *(LAS u32x4*)(L + L_WO + ((w * 16 + ks) * 2 + 1) * 1024 + lane * 16) = b; });
  load_w64<6>(p.w2 + (size_t)(172 * rg) * DM + 128 * cg + 32 * w, DM, 172, stg, lane, [&](int ks, u32x4 a, u32x4 b) { w2f[ks][0] = a; w2f[ks][1] = b; });
#pragma unroll
  for (int c2 = 0; c2 < 8; ++c2) {
    u32x2 raw[12];
#pragma unroll
    for (int i = 0; i < 12; ++i) {
      const int idx = lane + 64 * i, row = idx / 12, pc = idx % 12;
      const int lc = 48 * w + 4 * pc;
      const bool ok = lc < 172;
      const u32x2 v = *(const u32x2*)(p.w1 + (size_t)(64 * c2 + row) * FF + 172 * rg + (ok ? lc : 0));
      raw[i] = ok ? v : u32x2{0, 0};
    }
#pragma unroll
    for (int i = 0; i < 12; ++i) { const int idx = lane + 64 * i; *(LAS u32x2*)(stg + idx * 8) = raw[i]; }
    asm volatile("s_waitcnt lgkmcnt(0)" ::: "memory");
    u32x4 fr[2][3];
    tr_frags<3>(stg, lane, fr);
#pragma unroll
    for (int t = 0; t < 3; ++t) { w1f[2 * c2][t] = fr[0][t]; w1f[2 * c2 + 1][t] = fr[1][t]; }
    asm volatile("s_waitcnt lgkmcnt(0)" ::: "memory");
  }
  __syncthreads();
  STAMP(2);
  const int grow = w >> 1, gcol = 4 * (tid & 127);
  float xprev[4] = {0, 0, 0, 0};
  LAS float* red = (LAS float*)(L + L_RED);
  LAS float* xsave = (LAS float*)(L + L_RED + 256);
  for (int r = 0; r <= NB; ++r) {
    const bool hasA = r < NB, hasB = r >= 1;
#pragma unroll
    for (int row = 1; row >= 0; --row) {
      unsigned o2 = 0;
      if (row == 0 ? hasA : hasB) {
        const int inst = row == 0 ? 2 * r : 2 * (r - 1) + 1;
        const int base = WS_PART + (inst * 32 + 4 * rg + w) * 4096;
        unsigned spins = 0;
        for (;;) {
          MEMBAR();
          const unsigned ml = ld32(rs, base + 3584 + (lane < 14 ? lane : 0) * 4);
          if (__any(ml & 1u)) break;
          if (spin_fail(spins, rs)) return;
          __builtin_amdgcn_s_sleep(2);
        }
        u32x2 a[7]; unsigned ml;
        for (;;) {
          MEMBAR();
          ml = ld32(rs, base + 3584 + (lane < 14 ? lane : 0) * 4);
#pragma unroll
          for (int s = 0; s < 7; ++s) a[s] = ld64(rs, base + s * 512 + lane * 8);
          unsigned ok = ml;
#pragma unroll
          for (int s = 0; s < 7; ++s) ok &= a[s].x & a[s].y;
          if (__all(ok & 1u)) break;
          if (spin_fail(spins, rs)) return;
        }
        float M = -INFINITY;
#pragma unroll
        for (int s = 0; s < 7; ++s) M = fmaxf(M, __uint_as_float(__builtin_amdgcn_readlane(ml, 2 * s)));
        float Ls = 0.f, o0 = 0.f, o1 = 0.f;
#pragma unroll
        for (int s = 0; s < 7; ++s) {
          const float e = exp2f(__uint_as_float(__builtin_amdgcn_readlane(ml, 2 * s)) - M);
          Ls += e * __uint_as_float(__builtin_amdgcn_readlane(ml, 2 * s + 1));
          o0 += e * __uint_as_float(a[s].x); o1 += e * __uint_as_float(a[s].y);
        }
        const float inv = 1.f / Ls;
        o2 = f2bf(o0 * inv) | (f2bf(o1 * inv) << 16);
      }
      *(LAS unsigned*)(L + L_XIN + row * 1024 + w * 256 + lane * 4) = o2;
    }
    STAMP(8 + r * 8 + 0);
    __syncthreads();
    {
      f32x4 a0 = {0, 0, 0, 0}, a1 = {0, 0, 0, 0};
#pragma unroll
      for (int ks = 0; ks < 16; ++ks) {
        const u32x4 af = *(LAS u32x4*)(L + L_XIN + (c16 & 1) * 1024 + ks * 64 + g * 16);
        const u32x4 b0 = *(LAS u32x4*)(L + L_WO + ((w * 16 + ks) * 2 + 0) * 1024 + lane * 16);
        const u32x4 b1 = *(LAS u32x4*)(L + L_WO + ((w * 16 + ks) * 2 + 1) * 1024 + lane * 16);
        a0 = mfma(af, b0, a0); a1 = mfma(af, b1, a1);
      }
      const f32x4 a = (g >> 1) ? a1 : a0;
      const float v = (g & 1) ? a[1] : a[0];
      st32(rs, WS_X1 + ((r * 2 + (g & 1)) * 8 + rg) * 2048 + (128 * cg + 32 * w + 16 * (g >> 1) + c16) * 4, tagf(v));
    }
    STAMP(8 + r * 8 + 1);
    float x1[4];
    const bool act = grow == 0 ? hasA : hasB;
    {
      float res[4] = {0, 0, 0, 0};
      if (grow == 0) { if (hasA) { const u32x2 xv = *(const u32x2*)(p.x + r * DM + gcol); res[0] = bf2f(xv.x & 0xffffu); res[1] = bf2f(xv.x >> 16); res[2] = bf2f(xv.y & 0xffffu); res[3] = bf2f(xv.y >> 16); } }
      else { const f32x4 xv = *(LAS f32x4*)(xsave + gcol); res[0] = xv[0]; res[1] = xv[1]; res[2] = xv[2]; res[3] = xv[3]; }
      float s[4] = {0, 0, 0, 0};
      if (act) {
        unsigned spins = 0;
        u32x4 a[8];
        for (;;) {
          MEMBAR();
#pragma unroll
          for (int k = 0; k < 8; ++k) a[k] = ld128(rs, WS_X1 + ((r * 2 + grow) * 8 + k) * 2048 + gcol * 4);
          unsigned ok = 1u;
#pragma unroll
          for (int k = 0; k < 8; ++k) ok &= a[k].x & a[k].y & a[k].z & a[k].w;
          if (__all(ok & 1u)) break;
          if (spin_fail(spins, rs)) return;
        }
#pragma unroll
        for (int k = 0; k < 8; ++k)
#pragma unroll
          for (int j = 0; j < 4; ++j) s[j] += __uint_as_float(a[k][j]);
      }
#pragma unroll
      for (int j = 0; j < 4; ++j) x1[j] = act ? rbf(res[j] + rbf(s[j])) : 0.f;
    }
    STAMP(8 + r * 8 + 2);
    auto layernorm_to_xin = [&](const float (&xx)[4]) {
      const float s = wave_sum(xx[0] + xx[1] + xx[2] + xx[3]);
      if (lane == 0) red[w] = s;
      __syncthreads();
      const float m = (red[w & 2] + red[(w & 2) + 1]) * (1.f / DM);
      float q = 0.f;
#pragma unroll
      for (int j = 0; j < 4; ++j) { const float d = xx[j] - m; q += d * d; }
      q = wave_sum(q);
      if (lane == 0) red[4 + w] = q;
      __syncthreads();
      const float rstd = 1.f / sqrtf((red[4 + (w & 2)] + red[4 + (w & 2) + 1]) * (1.f / DM) + 1e-5f);
      u32x2 o; o.x = f2bf((xx[0] - m) * rstd) | (f2bf((xx[1] - m) * rstd) << 16); o.y = f2bf((xx[2] - m) * rstd) | (f2bf((xx[3] - m) * rstd) << 16);
      *(LAS u32x2*)(L + L_XIN + grow * 1024 + gcol * 2) = o;
      __syncthreads();
    };
    layernorm_to_xin(x1);
    {
      f32x4 a0 = {0, 0, 0, 0}, a1 = {0, 0, 0, 0}, a2 = {0, 0, 0, 0};
#pragma unroll
      for (int ks = 0; ks < 16; ++ks) {
        const u32x4 af = *(LAS u32x4*)(L + L_XIN + (c16 & 1) * 1024 + ks * 64 + g * 16);
        a0 = mfma(af, w1f[ks][0], a0); a1 = mfma(af, w1f[ks][1], a1); a2 = mfma(af, w1f[ks][2], a2);
      }
      const f32x4 a = g == 0 ? a0 : (g == 1 ? a1 : a2);
      if (g < 3) {
#pragma unroll
        for (int row = 0; row < 2; ++row) {
          const float hv = rbf(a[row]);
          const float sv = hv / (1.f + __expf(-hv));
          *(LAS u16*)(L + L_H + row * 384 + (48 * w + 16 * g + c16) * 2) = (u16)f2bf(sv);
        }
      }
    }
    __syncthreads();
    {
      f32x4 a0 = {0, 0, 0, 0}, a1 = {0, 0, 0, 0};
#pragma unroll
      for (int ks = 0; ks < 6; ++ks) {
        const u32x4 af = *(LAS u32x4*)(L + L_H + (c16 & 1) * 384 + ks * 64 + g * 16);
        a0 = mfma(af, w2f[ks][0], a0); a1 = mfma(af, w2f[ks][1], a1);
      }
      const f32x4 a = (g >> 1) ? a1 : a0;
      const float v = (g & 1) ? a[1] : a[0];
      st32(rs, WS_X2 + ((r * 2 + (g & 1)) * 8 + rg) * 2048 + (128 * cg + 32 * w + 16 * (g >> 1) + c16) * 4, tagf(v));
    }
    STAMP(8 + r * 8 + 3);
    float x2[4] = {0, 0, 0, 0};
    {
      const bool mine = grow == 0 ? true : ((gcol >> 4) == c);
      const bool wact = act && (grow == 0 || __any(mine));
      if (wact) {
        unsigned spins = 0;
        u32x4 a[8];
        const int colc = mine ? gcol : 16 * c;
        for (;;) {
          MEMBAR();
#pragma unroll
          for (int k = 0; k < 8; ++k) a[k] = ld128(rs, WS_X2 + ((r * 2 + grow) * 8 + k) * 2048 + colc * 4);
          unsigned ok = 1u;
#pragma unroll
          for (int k = 0; k < 8; ++k) ok &= a[k].x & a[k].y & a[k].z & a[k].w;
          if (__all(ok & 1u)) break;
          if (spin_fail(spins, rs)) return;
        }
        float s[4] = {0, 0, 0, 0};
#pragma unroll
        for (int k = 0; k < 8; ++k)
#pragma unroll
          for (int j = 0; j < 4; ++j) s[j] += __uint_as_float(a[k][j]);
#pragma unroll
        for (int j = 0; j < 4; ++j) x2[j] = rbf(x1[j] + rbf(s[j]));
        if (grow == 1 && mine) {
          u32x2 o; o.x = f2bf(x2[0]) | (f2bf(x2[1]) << 16); o.y = f2bf(x2[2]) | (f2bf(x2[3]) << 16);
          *(u32x2*)(p.out + (r - 1) * DM + gcol) = o;
        }
      }
    }
    STAMP(8 + r * 8 + 4);
    if (hasA) {
      if (grow == 0) *(LAS f32x4*)(xsave + gcol) = f32x4{x2[0], x2[1], x2[2], x2[3]};
      layernorm_to_xin(x2);
      f32x4 a0 = {0, 0, 0, 0}, a1 = {0, 0, 0, 0};
#pragma unroll
      for (int ks = 0; ks < 16; ++ks) {
        const u32x4 af = *(LAS u32x4*)(L + L_XIN + ks * 64 + g * 16);
        a0 = mfma(af, wqf[ks][0], a0); a1 = mfma(af, wqf[ks][1], a1);
      }
      if (g < 2) {
        const float v = g ? a1[0] : a0[0];
        st32(rs, WS_Q + ((2 * r + 1) * 32 + c) * 512 + (32 * w + 16 * g + c16) * 4, (f2bf(v) << 16) | 1u);
      }
      __syncthreads();
    }
    STAMP(8 + r * 8 + 5);
  }
}

__device__ __forceinline__ void stream_role(const Params& p, LAS char* L) {
  const int tid = threadIdx.x, lane = tid & 63, w = __builtin_amdgcn_readfirstlane(tid >> 6);
  const int s = blockIdx.x - NCHAIN, h = s / NSUB, sub = s % NSUB;
  const int wv = sub * 4 + w;
  const int q0 = wv < 12 ? wv * 36 : 432 + (wv - 12) * 37, nq = wv < 12 ? 36 : 37;
  const int g = lane >> 4, c = lane & 15;
  const rsrc_t rs = __builtin_amdgcn_make_buffer_rsrc(p.ws, 0, WS_END, 0x00020000);
  u32x4 msk;
#pragma unroll
  for (int i = 0; i < 4; ++i) msk[i] = ((c >> 1) == i) ? ((c & 1) ? 0xffff0000u : 0x0000ffffu) : 0u;
  for (int job = 0; job < 16; ++job) {
    int b, layer;
    if (job == 0) { b = 0; layer = 0; } else if (job == 15) { b = 7; layer = 1; } else if (job & 1) { b = (job + 1) >> 1; layer = 0; } else { b = (job >> 1) - 1; layer = 1; }
    const int inst = 2 * b + layer;
    STAMP(job * 4 + 0);
    u32x4 qa, qb;
    {
      unsigned spins = 0;
      const int qoff = WS_Q + (inst * 32 + h) * 512 + (4 * g + (c & 3)) * 32;
      for (;;) {
        MEMBAR();
        qa = ld128(rs, qoff); qb = ld128(rs, qoff + 16);
        const unsigned ok = qa.x & qa.y & qa.z & qa.w & qb.x & qb.y & qb.z & qb.w;
        if (__all(ok & 1u)) break;
        if (spin_fail(spins, rs)) return;
        __builtin_amdgcn_s_sleep(4);
      }
    }
    STAMP(job * 4 + 1);
    const u32x4 qc = {(qa.x >> 16) | (qa.y & 0xffff0000u), (qa.z >> 16) | (qa.w & 0xffff0000u), (qb.x >> 16) | (qb.y & 0xffff0000u), (qb.z >> 16) | (qb.w & 0xffff0000u)};
    u32x4 bq[4];
#pragma unroll
    for (int i = 0; i < 4; ++i) bq[i] = ((c >> 2) == i) ? qc : u32x4{0, 0, 0, 0};
    const size_t base = ((size_t)(b * NH + h) * SK + q0 * 4) * 256;
    const char* kb = (const char*)p.K + base + ((c >> 2) * 256 + g * 64 + (c & 3) * 16);
    const char* vb = (const char*)p.V + base + lane * 16;
    float sc[10];
#pragma unroll
    for (int t = 0; t < 10; ++t) {
      f32x4 d = {0, 0, 0, 0};
#pragma unroll
      for (int i = 0; i < (t < 9 ? 4 : 1); ++i) {
        const u32x4 kk = __builtin_nontemporal_load((const u32x4*)(kb + (t * 4 + i) * 1024));
        d = mfma(kk, bq[i], d);
      }
      float v = (c & 2) ? ((c & 1) ? d[3] : d[2]) : ((c & 1) ? d[1] : d[0]);
      v += dppf<0xB1>(v); v += dppf<0x4E>(v);
      sc[t] = v * SC2;
    }
    if (nq == 36 || (c >> 2) != 0) sc[9] = -INFINITY;
    float m = sc[0];
#pragma unroll
    for (int t = 1; t < 10; ++t) m = fmaxf(m, sc[t]);
    m = wave_max(m);
    float l = 0.f;
    f32x4 acc = {0, 0, 0, 0};
#pragma unroll
    for (int t = 0; t < 10; ++t) {
      const unsigned pb = f2bf(exp2f(sc[t] - m));
      l += bf2f(pb);
      const unsigned pp = pb | (pb << 16);
#define PVQ(I) { const unsigned pbr = dppu<0x150 + 4 * I>(pp); u32x4 bb; for (int k = 0; k < 4; ++k) bb[k] = pbr & msk[k]; \
      const u32x4 vv = __builtin_nontemporal_load((const u32x4*)(vb + (t * 4 + I) * 1024)); acc = mfma(vv, bb, acc); }
      PVQ(0) if (t < 9) { PVQ(1) PVQ(2) PVQ(3) }
    }
    l = wave_sum(l) * 0.25f;
    STAMP(job * 4 + 2);
    LAS float* area = (LAS float*)(L + (job & 1) * 4096);
    if (c < 8) {
#pragma unroll
      for (int r = 0; r < 4; ++r) area[w * 132 + 4 + 32 * g + 8 * r + c] = acc[r];
    }
    if (lane == 0) { area[w * 132] = m; area[w * 132 + 1] = l; }
    __syncthreads();
    {
      const float m0 = area[0], m1 = area[132], m2 = area[264], m3 = area[396];
      const float M = fmaxf(fmaxf(m0, m1), fmaxf(m2, m3));
      const float e0 = exp2f(m0 - M), e1 = exp2f(m1 - M), e2 = exp2f(m2 - M), e3 = exp2f(m3 - M);
      const int d = 32 * w + (lane & 31);
      const float a = e0 * area[4 + d] + e1 * area[132 + 4 + d] + e2 * area[264 + 4 + d] + e3 * area[396 + 4 + d];
      const float Ls = e0 * area[1] + e1 * area[133] + e2 * area[265] + e3 * area[397];
      const int pbase = WS_PART + (inst * 32 + h) * 4096;
      if (lane < 32) st32(rs, pbase + sub * 512 + d * 4, tagf(a));
      else if (w == 0 && lane < 34) st32(rs, pbase + 3584 + sub * 8 + (lane - 32) * 4, tagf(lane == 32 ? M : Ls));
    }
    STAMP(job * 4 + 3);
  }
}

__global__ __launch_bounds__(256, 1) void fused_kernel(Params p) {
  extern __shared__ __attribute__((aligned(16))) char lds_raw[];
  LAS char* L = (LAS char*)lds_raw;
  if (blockIdx.x < NCHAIN) chain_role(p, L); else stream_role(p, L);
}

extern "C" void kernel_launch(void* const* d_in, const int* in_sizes, int n_in, void* d_out, int out_size, void* d_ws, size_t ws_size, hipStream_t stream) {
  Params p;
  p.x = (const u16*)d_in[0]; p.K = (const u16*)d_in[1]; p.V = (const u16*)d_in[2]; p.wq = (const u16*)d_in[3]; p.wo = (const u16*)d_in[4];
  p.w1 = (const u16*)d_in[6]; p.w2 = (const u16*)d_in[7]; p.out = (u16*)d_out; p.ws = (unsigned char*)d_ws;
  constexpr int LDS_BYTES = 160 * 1024;
  hipFuncSetAttribute((const void*)fused_kernel, hipFuncAttributeMaxDynamicSharedMemorySize, LDS_BYTES);
  hipMemsetAsync(d_ws, 0, WS_END, stream);
  hipLaunchKernelGGL(fused_kernel, dim3(256), dim3(256), LDS_BYTES, stream, p);
}
```

```cpp
#include <hip/hip_runtime.h>
#include <hip/hip_bf16.h>
#include <stdint.h>

typedef unsigned u32x4 __attribute__((ext_vector_type(4)));
typedef unsigned short u16;
#define LAS __attribute__((address_space(3)))

constexpr int DM = 512, NH = 32, SK = 4096, DH = 128, FF = 1376;
constexpr int NCHAIN = 32, NSUB = 7;
constexpr int WS_EARLY = 6 * 16384;
constexpr int WS_QTOP = 15 * 16384;
constexpr int WS_TMO = 16 * 16384;
constexpr int WS_XE = WS_TMO + 256;
constexpr int WS_PART = WS_XE + 3 * 32768;
constexpr int WS_EARLY_END = WS_PART + 4 * 131072;
constexpr int WS_XL = WS_PART + 17 * 131072 - 3 * 32768;
constexpr int WS_END = WS_XL + 9 * 65536;
constexpr int WS_DBG = WS_END;

struct Params { const u16 *x, *K, *V, *wq, *wo, *w1, *w2; u16* out; unsigned char* ws; };

__device__ __forceinline__ float bf2f(unsigned v) { return __uint_as_float(v << 16); }
__device__ __forceinline__ unsigned f2bf(float f) { return (unsigned)__builtin_bit_cast(u16, (__bf16)f); }
template <int CTRL> __device__ __forceinline__ float dppf(float v) {
  return __uint_as_float((unsigned)__builtin_amdgcn_update_dpp(0, (int)__float_as_uint(v), CTRL, 0xf, 0xf, true));
}

constexpr int L_WO = 0;
constexpr int L_STG = 131072;
__device__ __forceinline__ float wave_sum_fast(float v) {
  v += dppf<0xB1>(v); v += dppf<0x4E>(v); v += dppf<0x141>(v); v += dppf<0x140>(v);
  auto s = __builtin_amdgcn_permlane16_swap(__float_as_uint(v), __float_as_uint(v), false, false);
  v = __uint_as_float(s[0]) + __uint_as_float(s[1]);
  auto t = __builtin_amdgcn_permlane32_swap(__float_as_uint(v), __float_as_uint(v), false, false);
  return __uint_as_float(t[0]) + __uint_as_float(t[1]);
}
__device__ __forceinline__ unsigned long long pk2(unsigned a, unsigned b) { return (unsigned long long)a | ((unsigned long long)b << 32); }

__device__ __forceinline__ void chain_role(const Params& p, LAS char* L) {
  const int tid = threadIdx.x, lane = tid & 63, w = __builtin_amdgcn_readfirstlane(tid >> 6);
  const int c = blockIdx.x, rg = c & 7, cg = c >> 3;
  const int g = lane >> 4, c16 = lane & 15, q = c16 >> 2, pp = lane & 3;
#pragma unroll
  for (int bb = 0; bb < 2; ++bb) {
    const u32x4 xv = *(const u32x4*)(p.x + (w + 4 * bb) * DM + lane * 8);
    float v[8];
#pragma unroll
    for (int j = 0; j < 4; ++j) { v[2 * j] = bf2f(xv[j] & 0xffffu); v[2 * j + 1] = bf2f(xv[j] >> 16); }
    float s = 0.f;
#pragma unroll
    for (int j = 0; j < 8; ++j) s += v[j];
    const float m = wave_sum_fast(s) * (1.f / DM);
    float qq = 0.f;
#pragma unroll
    for (int j = 0; j < 8; ++j) { const float d = v[j] - m; qq += d * d; }
    const float rstd = __builtin_amdgcn_rsqf(wave_sum_fast(qq) * (1.f / DM) + 1e-5f);
    u32x4 o;
#pragma unroll
    for (int j = 0; j < 4; ++j) o[j] = f2bf((v[2 * j] - m) * rstd) | (f2bf((v[2 * j + 1] - m) * rstd) << 16);
    *(LAS u32x4*)(L + L_STG + (w + 4 * bb) * 1024 + lane * 16) = o;
  }
  unsigned woa[4];
#pragma unroll
  for (int t = 0; t < 2; ++t)
#pragma unroll
    for (int tl = 0; tl < 2; ++tl) {
      const int row = 8 * g + 4 * t + q, ch = 2 * (2 * w + tl) + (pp >> 1);
      woa[t * 2 + tl] = L_WO + 256 * row + 16 * (ch ^ (((row & 3) << 2) | ((row >> 2) & 3))) + 8 * (pp & 1);
    }
  const int grow = w >> 1, gcol = 4 * (tid & 127);
  const int ocol = 32 * w + 16 * (g >> 1) + c16;
  const unsigned k0 = lane * 8, k1 = 3584 + (lane < 14 ? lane : 0) * 4, k2 = L_STG + w * 256 + lane * 4, k3 = L_STG + (c16 & 1) * 1024 + g * 16;
  const unsigned k12 = (g & 1) * 16384 + rg * 2048 + (128 * cg + ocol) * 4;
  const unsigned k13 = grow * 16384 + gcol * 4 + 4096, k14 = k13 + 8192;
  const unsigned k15 = L_STG + 2048 + grow * 1024 + ((gcol - 128 * cg) & 511) * 2, k16 = L_STG + 8192 + gcol * 4, k17 = L_STG + 6144 + (48 * w + 16 * g + c16) * 2;
  const unsigned k18 = c * 512 + (32 * w + 16 * (g & 1) + c16) * 4, k19 = L_STG + 2048 + lane * 16;
  const unsigned k20 = gcol * 2, k21 = grow * 16384 + 4096 + ((gcol >> 4) == c ? gcol : 16 * c) * 4;
  const unsigned stg = L_STG + w * 6144;
  const unsigned k22 = L_STG + (c16 & 7) * 1024 + g * 16, k23 = stg + lane * 16, k24 = stg + (8 * g + q) * 64 + 8 * pp, k25 = stg + (8 * g + q) * 96 + 8 * pp;
  const unsigned k26 = (172 * rg + (lane >> 2)) * 1024 + (128 * cg + 32 * w) * 2 + (lane & 3) * 16;
  const unsigned k27 = (128 * w + g) * 1024 + (c16 ^ (g << 2)) * 16;
  const unsigned k28 = WS_QTOP - (4 * (g & 1)) * 16384 + c * 512 + ocol * 4;
  unsigned k29[3];
#pragma unroll
  for (int ii = 0; ii < 3; ++ii) { const int idx = lane + 64 * ii; k29[ii] = (idx / 6) * (FF * 2) + (172 * rg + 48 * w + 8 * (idx % 6)) * 2; }
  const u16* wob = p.wo + (size_t)(512 * rg) * DM + 128 * cg;
  const u16* wqb = p.wq + 128 * c;
  const unsigned info = __builtin_amdgcn_readfirstlane(c | (w << 8));
  unsigned char* dbg = p.ws + WS_DBG + (size_t)(c * 4 + w) * 1024;
  asm volatile(
".Lch_begin:\n"
"v_mov_b64 v[200:201], %8\n"
"v_mov_b64 v[202:203], %9\n"
"v_mov_b64 v[204:205], %10\n"
"v_mov_b64 v[206:207], %11\n"
"v_mov_b64 v[208:209], %12\n"
"v_mov_b64 v[210:211], %13\n"
"v_mov_b64 v[212:213], %14\n"
"v_mov_b64 v[214:215], %15\n"
"v_mov_b64 v[216:217], %16\n"
"v_mov_b64 v[218:219], %17\n"
"v_mov_b64 v[220:221], %18\n"
"v_mov_b64 v[222:223], %19\n"
"v_mov_b64 v[224:225], %20\n"
"v_mov_b64 v[226:227], %21\n"
"v_mov_b64 v[228:229], %22\n"
"v_mov_b64 v[230:231], %23\n"
"v_mov_b32 v0, v200\n"
"v_mov_b32 v1, v201\n"
"v_mov_b32 v2, v202\n"
"v_mov_b32 v3, v203\n"
"v_mov_b32 v4, v204\n"
"v_mov_b32 v5, v205\n"
"v_mov_b32 v6, v206\n"
"v_mov_b32 v7, v207\n"
"v_mov_b32 v8, v208\n"
"v_mov_b32 v9, v209\n"
"v_mov_b32 v10, v210\n"
"v_mov_b32 v11, v211\n"
"v_mov_b32 v12, v212\n"
"v_mov_b32 v13, v213\n"
"v_mov_b32 v14, v214\n"
"v_mov_b32 v15, v215\n"
"v_mov_b32 v16, v216\n"
"v_mov_b32 v17, v217\n"
"v_mov_b32 v18, v218\n"
"v_mov_b32 v19, v219\n"
"v_mov_b32 v142, v220\n"
"v_mov_b32 v143, v221\n"
"v_mov_b32 v108, v222\n"
"v_mov_b32 v109, v223\n"
"v_mov_b32 v110, v224\n"
"v_mov_b32 v111, v225\n"
"v_mov_b32 v112, v226\n"
"v_mov_b32 v113, v227\n"
"v_mov_b32 v114, v228\n"
"v_mov_b32 v116, v229\n"
"v_mov_b32 v117, v230\n"
"v_mov_b32 v118, v231\n"
"v_add_u32 v119, 88064, v229\n"
"v_add_u32 v120, 88064, v230\n"
"v_add_u32 v121, 88064, v231\n"
"s_mov_b64 s[16:17], %0\n"
"s_mov_b64 s[18:19], %1\n"
"s_mov_b64 s[20:21], %2\n"
"s_mov_b64 s[22:23], %3\n"
"s_mov_b64 s[24:25], %4\n"
"s_and_b32 s25, s25, 0xffff\n"
"s_mov_b32 s26, 1409024\n"
"s_mov_b32 s27, 0x20000\n"
"s_mov_b64 s[28:29], %5\n"
"s_and_b32 s29, s29, 0xffff\n"
"s_mov_b32 s30, 1409024\n"
"s_mov_b32 s31, 0x20000\n"
"s_mov_b64 s[74:75], %7\n"
"s_and_b32 s33, %6, 0xff\n"
"s_lshr_b32 s34, %6, 8\n"
"s_and_b32 s35, s33, 7\n"
"s_lshl_b32 s35, s35, 2\n"
"s_add_u32 s35, s35, s34\n"
"s_lshl_b32 s61, s35, 12\n"
"s_add_u32 s61, s61, 360704\n"
"s_lshr_b32 s62, s34, 1\n"
"s_lshl_b32 s69, s33, 1\n"
"s_and_b32 s35, s34, 1\n"
"s_add_u32 s69, s69, s35\n"
"s_lshr_b32 s35, s33, 4\n"
"s_and_b32 s63, s34, 1\n"
"s_cmp_eq_u32 s35, s63\n"
"s_cselect_b32 s63, 1, 0\n"
"s_and_b32 s35, s33, 15\n"
"s_lshl_b32 s35, s35, 2\n"
"s_lshl_b64 s[64:65], 15, s35\n"
"s_cmp_eq_u32 s34, 3\n"
"s_cselect_b32 s50, 0x0fffffff, -1\n"
"s_cselect_b32 s51, 0, 0xffff\n"
"s_mov_b32 s38, 0xaaaaaaaa\n"
"s_mov_b32 s39, 0xaaaaaaaa\n"
"s_mov_b32 s40, 0xffff0000\n"
"s_mov_b32 s41, 0xffff0000\n"
"s_mov_b32 s42, 0x0\n"
"s_mov_b32 s43, 0xffffffff\n"
"s_mov_b32 s44, 0xffff0000\n"
"s_mov_b32 s45, 0x0\n"
"s_mov_b32 s46, 0xffffffff\n"
"s_mov_b32 s47, 0xffff\n"
"s_mov_b32 s48, 0xffffffff\n"
"s_mov_b32 s49, 0x0\n"
"s_mov_b32 s52, 0x2aaa\n"
"s_mov_b32 s53, 0x0\n"
"s_lshl_b32 s70, s34, 15\n"
"s_mov_b32 s68, s70\n"
"s_mov_b64 s[78:79], %24\n"
"v_lshrrev_b32 v128, 10, v113\n"
"v_and_b32 v129, 0x3ff, v113\n"
"v_lshl_or_b32 v128, v128, 13, v129\n"
"v_xor_b32 v127, 48, v113\n"
"v_add_u32 v127, 12288, v127\n"
"v_xor_b32 v131, 48, v128\n"
"v_add_u32 v131, 98304, v131\n"
"v_xor_b32 v126, 32, v113\n"
"v_add_u32 v126, 8192, v126\n"
"v_xor_b32 v130, 32, v128\n"
"v_add_u32 v130, 65536, v130\n"
"v_xor_b32 v125, 16, v113\n"
"v_add_u32 v125, 4096, v125\n"
"v_xor_b32 v129, 16, v128\n"
"v_add_u32 v129, 32768, v129\n"
"v_xor_b32 v124, 0, v113\n"
"s_add_u32 m0, s68, 0\n"
"s_nop 0\n"
"global_load_lds_dwordx4 v128, s[78:79] nt\n"
"s_add_u32 m0, s68, 1024\n"
"s_nop 0\n"
"global_load_lds_dwordx4 v129, s[78:79] nt\n"
"s_add_u32 m0, s68, 2048\n"
"s_nop 0\n"
"global_load_lds_dwordx4 v130, s[78:79] nt\n"
"s_add_u32 m0, s68, 3072\n"
"s_nop 0\n"
"global_load_lds_dwordx4 v131, s[78:79] nt\n"
"s_add_u32 s78, s78, 131072\n"
"s_addc_u32 s79, s79, 0\n"
"s_add_u32 m0, s68, 4096\n"
"s_nop 0\n"
"global_load_lds_dwordx4 v128, s[78:79] nt\n"
"s_add_u32 m0, s68, 5120\n"
"s_nop 0\n"
"global_load_lds_dwordx4 v129, s[78:79] nt\n"
"s_add_u32 m0, s68, 6144\n"
"s_nop 0\n"
"global_load_lds_dwordx4 v130, s[78:79] nt\n"
"s_add_u32 m0, s68, 7168\n"
"s_nop 0\n"
"global_load_lds_dwordx4 v131, s[78:79] nt\n"
"s_add_u32 s78, s78, 131072\n"
"s_addc_u32 s79, s79, 0\n"
"s_add_u32 m0, s68, 8192\n"
"s_nop 0\n"
"global_load_lds_dwordx4 v128, s[78:79] nt\n"
"s_add_u32 m0, s68, 9216\n"
"s_nop 0\n"
"global_load_lds_dwordx4 v129, s[78:79] nt\n"
"s_add_u32 m0, s68, 10240\n"
"s_nop 0\n"
"global_load_lds_dwordx4 v130, s[78:79] nt\n"
"s_add_u32 m0, s68, 11264\n"
"s_nop 0\n"
"global_load_lds_dwordx4 v131, s[78:79] nt\n"
"s_add_u32 s78, s78, 131072\n"
"s_addc_u32 s79, s79, 0\n"
"s_add_u32 m0, s68, 12288\n"
"s_nop 0\n"
"global_load_lds_dwordx4 v128, s[78:79] nt\n"
"s_add_u32 m0, s68, 13312\n"
"s_nop 0\n"
"global_load_lds_dwordx4 v129, s[78:79] nt\n"
"s_add_u32 m0, s68, 14336\n"
"s_nop 0\n"
"global_load_lds_dwordx4 v130, s[78:79] nt\n"
"s_add_u32 m0, s68, 15360\n"
"s_nop 0\n"
"global_load_lds_dwordx4 v131, s[78:79] nt\n"
"s_add_u32 s78, s78, 131072\n"
"s_addc_u32 s79, s79, 0\n"
"s_add_u32 m0, s68, 16384\n"
"s_nop 0\n"
"global_load_lds_dwordx4 v128, s[78:79] nt\n"
"s_add_u32 m0, s68, 17408\n"
"s_nop 0\n"
"global_load_lds_dwordx4 v129, s[78:79] nt\n"
"s_add_u32 m0, s68, 18432\n"
"s_nop 0\n"
"global_load_lds_dwordx4 v130, s[78:79] nt\n"
"s_add_u32 m0, s68, 19456\n"
"s_nop 0\n"
"global_load_lds_dwordx4 v131, s[78:79] nt\n"
"s_add_u32 s78, s78, 131072\n"
"s_addc_u32 s79, s79, 0\n"
"s_add_u32 m0, s68, 20480\n"
"s_nop 0\n"
"global_load_lds_dwordx4 v128, s[78:79] nt\n"
"s_add_u32 m0, s68, 21504\n"
"s_nop 0\n"
"global_load_lds_dwordx4 v129, s[78:79] nt\n"
"s_add_u32 m0, s68, 22528\n"
"s_nop 0\n"
"global_load_lds_dwordx4 v130, s[78:79] nt\n"
"s_add_u32 m0, s68, 23552\n"
"s_nop 0\n"
"global_load_lds_dwordx4 v131, s[78:79] nt\n"
"s_add_u32 s78, s78, 131072\n"
"s_addc_u32 s79, s79, 0\n"
"s_add_u32 m0, s68, 24576\n"
"s_nop 0\n"
"global_load_lds_dwordx4 v128, s[78:79] nt\n"
"s_add_u32 m0, s68, 25600\n"
"s_nop 0\n"
"global_load_lds_dwordx4 v129, s[78:79] nt\n"
"s_add_u32 m0, s68, 26624\n"
"s_nop 0\n"
"global_load_lds_dwordx4 v130, s[78:79] nt\n"
"s_add_u32 m0, s68, 27648\n"
"s_nop 0\n"
"global_load_lds_dwordx4 v131, s[78:79] nt\n"
"s_add_u32 s78, s78, 131072\n"
"s_addc_u32 s79, s79, 0\n"
"s_add_u32 m0, s68, 28672\n"
"s_nop 0\n"
"global_load_lds_dwordx4 v128, s[78:79] nt\n"
"s_add_u32 m0, s68, 29696\n"
"s_nop 0\n"
"global_load_lds_dwordx4 v129, s[78:79] nt\n"
"s_add_u32 m0, s68, 30720\n"
"s_nop 0\n"
"global_load_lds_dwordx4 v130, s[78:79] nt\n"
"s_add_u32 m0, s68, 31744\n"
"s_nop 0\n"
"global_load_lds_dwordx4 v131, s[78:79] nt\n"
"s_mov_b32 s35, 0\n"
"buffer_load_dwordx4 v[208:211], v112, s[28:31], s35 offen nt\n"
"s_add_u32 s35, s35, 16384\n"
"buffer_load_dwordx4 v[212:215], v112, s[28:31], s35 offen nt\n"
"s_add_u32 s35, s35, 16384\n"
"buffer_load_dwordx4 v[216:219], v112, s[28:31], s35 offen nt\n"
"s_add_u32 s35, s35, 16384\n"
"buffer_load_dwordx4 v[220:223], v112, s[28:31], s35 offen nt\n"
"s_add_u32 s35, s35, 16384\n"
"buffer_load_dwordx4 v[224:227], v112, s[28:31], s35 offen nt\n"
"s_add_u32 s35, s35, 16384\n"
"buffer_load_dwordx4 v[228:231], v112, s[28:31], s35 offen nt\n"
"s_add_u32 s35, s35, 16384\n"
"buffer_load_dwordx4 v[232:235], v112, s[28:31], s35 offen nt\n"
"s_add_u32 s35, s35, 16384\n"
"buffer_load_dwordx4 v[236:239], v112, s[28:31], s35 offen nt\n"
"s_add_u32 s35, s35, 16384\n"
"buffer_load_dwordx4 v[240:243], v112, s[28:31], s35 offen nt\n"
"s_add_u32 s35, s35, 16384\n"
"buffer_load_dwordx4 v[244:247], v112, s[28:31], s35 offen nt\n"
"s_add_u32 s35, s35, 16384\n"
"buffer_load_dwordx4 v[248:251], v112, s[28:31], s35 offen nt\n"
"s_add_u32 s35, s35, 16384\n"
"buffer_load_dwordx4 v[252:255], v112, s[28:31], s35 offen nt\n"
"s_add_u32 s35, s35, 16384\n"
"s_waitcnt vmcnt(12) lgkmcnt(0)\n"
"s_barrier\n"
"ds_read_b128 v[44:47], v108 offset:0\n"
"ds_read_b128 v[48:51], v108 offset:64\n"
"ds_read_b128 v[52:55], v108 offset:128\n"
"ds_read_b128 v[56:59], v108 offset:192\n"
"ds_read_b128 v[60:63], v108 offset:256\n"
"ds_read_b128 v[64:67], v108 offset:320\n"
"ds_read_b128 v[68:71], v108 offset:384\n"
"ds_read_b128 v[72:75], v108 offset:448\n"
"ds_read_b128 v[76:79], v108 offset:512\n"
"ds_read_b128 v[80:83], v108 offset:576\n"
"ds_read_b128 v[84:87], v108 offset:640\n"
"ds_read_b128 v[88:91], v108 offset:704\n"
"ds_read_b128 v[92:95], v108 offset:768\n"
"ds_read_b128 v[96:99], v108 offset:832\n"
"ds_read_b128 v[100:103], v108 offset:896\n"
"ds_read_b128 v[104:107], v108 offset:960\n"
"ds_read_b64_tr_b16 a[0:1], v4 offset:0\n"
"ds_read_b64_tr_b16 a[2:3], v6 offset:0\n"
"ds_read_b64_tr_b16 a[4:5], v5 offset:0\n"
"ds_read_b64_tr_b16 a[6:7], v7 offset:0\n"
"ds_read_b64_tr_b16 a[8:9], v4 offset:8192\n"
"ds_read_b64_tr_b16 a[10:11], v6 offset:8192\n"
"ds_read_b64_tr_b16 a[12:13], v5 offset:8192\n"
"ds_read_b64_tr_b16 a[14:15], v7 offset:8192\n"
"ds_read_b64_tr_b16 a[16:17], v4 offset:16384\n"
"ds_read_b64_tr_b16 a[18:19], v6 offset:16384\n"
"ds_read_b64_tr_b16 a[20:21], v5 offset:16384\n"
"ds_read_b64_tr_b16 a[22:23], v7 offset:16384\n"
"ds_read_b64_tr_b16 a[24:25], v4 offset:24576\n"
"ds_read_b64_tr_b16 a[26:27], v6 offset:24576\n"
"ds_read_b64_tr_b16 a[28:29], v5 offset:24576\n"
"ds_read_b64_tr_b16 a[30:31], v7 offset:24576\n"
"ds_read_b64_tr_b16 a[32:33], v4 offset:32768\n"
"ds_read_b64_tr_b16 a[34:35], v6 offset:32768\n"
"ds_read_b64_tr_b16 a[36:37], v5 offset:32768\n"
"ds_read_b64_tr_b16 a[38:39], v7 offset:32768\n"
"ds_read_b64_tr_b16 a[40:41], v4 offset:40960\n"
"ds_read_b64_tr_b16 a[42:43], v6 offset:40960\n"
"ds_read_b64_tr_b16 a[44:45], v5 offset:40960\n"
"ds_read_b64_tr_b16 a[46:47], v7 offset:40960\n"
"ds_read_b64_tr_b16 a[48:49], v4 offset:49152\n"
"ds_read_b64_tr_b16 a[50:51], v6 offset:49152\n"
"ds_read_b64_tr_b16 a[52:53], v5 offset:49152\n"
"ds_read_b64_tr_b16 a[54:55], v7 offset:49152\n"
"ds_read_b64_tr_b16 a[56:57], v4 offset:57344\n"
"ds_read_b64_tr_b16 a[58:59], v6 offset:57344\n"
"ds_read_b64_tr_b16 a[60:61], v5 offset:57344\n"
"ds_read_b64_tr_b16 a[62:63], v7 offset:57344\n"
"ds_read_b64_tr_b16 a[64:65], v8 offset:0\n"
"ds_read_b64_tr_b16 a[66:67], v10 offset:0\n"
"ds_read_b64_tr_b16 a[68:69], v9 offset:0\n"
"ds_read_b64_tr_b16 a[70:71], v11 offset:0\n"
"ds_read_b64_tr_b16 a[72:73], v8 offset:8192\n"
"ds_read_b64_tr_b16 a[74:75], v10 offset:8192\n"
"ds_read_b64_tr_b16 a[76:77], v9 offset:8192\n"
"ds_read_b64_tr_b16 a[78:79], v11 offset:8192\n"
"ds_read_b64_tr_b16 a[80:81], v8 offset:16384\n"
"ds_read_b64_tr_b16 a[82:83], v10 offset:16384\n"
"ds_read_b64_tr_b16 a[84:85], v9 offset:16384\n"
"ds_read_b64_tr_b16 a[86:87], v11 offset:16384\n"
"ds_read_b64_tr_b16 a[88:89], v8 offset:24576\n"
"ds_read_b64_tr_b16 a[90:91], v10 offset:24576\n"
"ds_read_b64_tr_b16 a[92:93], v9 offset:24576\n"
"ds_read_b64_tr_b16 a[94:95], v11 offset:24576\n"
"ds_read_b64_tr_b16 a[96:97], v8 offset:32768\n"
"ds_read_b64_tr_b16 a[98:99], v10 offset:32768\n"
"ds_read_b64_tr_b16 a[100:101], v9 offset:32768\n"
"ds_read_b64_tr_b16 a[102:103], v11 offset:32768\n"
"ds_read_b64_tr_b16 a[104:105], v8 offset:40960\n"
"ds_read_b64_tr_b16 a[106:107], v10 offset:40960\n"
"ds_read_b64_tr_b16 a[108:109], v9 offset:40960\n"
"ds_read_b64_tr_b16 a[110:111], v11 offset:40960\n"
"ds_read_b64_tr_b16 a[112:113], v8 offset:49152\n"
"ds_read_b64_tr_b16 a[114:115], v10 offset:49152\n"
"ds_read_b64_tr_b16 a[116:117], v9 offset:49152\n"
"ds_read_b64_tr_b16 a[118:119], v11 offset:49152\n"
"ds_read_b64_tr_b16 a[120:121], v8 offset:57344\n"
"ds_read_b64_tr_b16 a[122:123], v10 offset:57344\n"
"ds_read_b64_tr_b16 a[124:125], v9 offset:57344\n"
"ds_read_b64_tr_b16 a[126:127], v11 offset:57344\n"
"s_waitcnt lgkmcnt(0)\n"
"s_barrier\n"
"v_mfma_f32_16x16x32_bf16 v[24:27], v[44:47], a[0:3], 0\n"
"v_mfma_f32_16x16x32_bf16 v[28:31], v[44:47], a[4:7], 0\n"
"v_mfma_f32_16x16x32_bf16 v[24:27], v[48:51], a[8:11], v[24:27]\n"
"v_mfma_f32_16x16x32_bf16 v[28:31], v[48:51], a[12:15], v[28:31]\n"
"v_mfma_f32_16x16x32_bf16 v[24:27], v[52:55], a[16:19], v[24:27]\n"
"v_mfma_f32_16x16x32_bf16 v[28:31], v[52:55], a[20:23], v[28:31]\n"
"v_mfma_f32_16x16x32_bf16 v[24:27], v[56:59], a[24:27], v[24:27]\n"
"v_mfma_f32_16x16x32_bf16 v[28:31], v[56:59], a[28:31], v[28:31]\n"
"v_mfma_f32_16x16x32_bf16 v[24:27], v[60:63], a[32:35], v[24:27]\n"
"v_mfma_f32_16x16x32_bf16 v[28:31], v[60:63], a[36:39], v[28:31]\n"
"v_mfma_f32_16x16x32_bf16 v[24:27], v[64:67], a[40:43], v[24:27]\n"
"v_mfma_f32_16x16x32_bf16 v[28:31], v[64:67], a[44:47], v[28:31]\n"
"v_mfma_f32_16x16x32_bf16 v[24:27], v[68:71], a[48:51], v[24:27]\n"
"v_mfma_f32_16x16x32_bf16 v[28:31], v[68:71], a[52:55], v[28:31]\n"
"v_mfma_f32_16x16x32_bf16 v[24:27], v[72:75], a[56:59], v[24:27]\n"
"v_mfma_f32_16x16x32_bf16 v[28:31], v[72:75], a[60:63], v[28:31]\n"
"v_mfma_f32_16x16x32_bf16 v[24:27], v[76:79], a[64:67], v[24:27]\n"
"v_mfma_f32_16x16x32_bf16 v[28:31], v[76:79], a[68:71], v[28:31]\n"
"v_mfma_f32_16x16x32_bf16 v[24:27], v[80:83], a[72:75], v[24:27]\n"
"v_mfma_f32_16x16x32_bf16 v[28:31], v[80:83], a[76:79], v[28:31]\n"
"v_mfma_f32_16x16x32_bf16 v[24:27], v[84:87], a[80:83], v[24:27]\n"
"v_mfma_f32_16x16x32_bf16 v[28:31], v[84:87], a[84:87], v[28:31]\n"
"v_mfma_f32_16x16x32_bf16 v[24:27], v[88:91], a[88:91], v[24:27]\n"
"v_mfma_f32_16x16x32_bf16 v[28:31], v[88:91], a[92:95], v[28:31]\n"
"v_mfma_f32_16x16x32_bf16 v[24:27], v[92:95], a[96:99], v[24:27]\n"
"v_mfma_f32_16x16x32_bf16 v[28:31], v[92:95], a[100:103], v[28:31]\n"
"v_mfma_f32_16x16x32_bf16 v[24:27], v[96:99], a[104:107], v[24:27]\n"
"v_mfma_f32_16x16x32_bf16 v[28:31], v[96:99], a[108:111], v[28:31]\n"
"v_mfma_f32_16x16x32_bf16 v[24:27], v[100:103], a[112:115], v[24:27]\n"
"v_mfma_f32_16x16x32_bf16 v[28:31], v[100:103], a[116:119], v[28:31]\n"
"v_mfma_f32_16x16x32_bf16 v[24:27], v[104:107], a[120:123], v[24:27]\n"
"v_mfma_f32_16x16x32_bf16 v[28:31], v[104:107], a[124:127], v[28:31]\n"
"s_nop 7\n"
"v_cndmask_b32_e64 v36, v24, v28, s[42:43]\n"
"v_cvt_pk_bf16_f32 v36, v36, v36\n"
"v_lshlrev_b32 v36, 16, v36\n"
"v_or_b32 v36, 1, v36\n"
"global_store_dword v114, v36, s[16:17] sc1\n"
"v_cndmask_b32_e64 v37, v25, v29, s[42:43]\n"
"v_cvt_pk_bf16_f32 v37, v37, v37\n"
"v_lshlrev_b32 v37, 16, v37\n"
"v_or_b32 v37, 1, v37\n"
"v_subrev_u32 v114, 16384, v114\n"
"global_store_dword v114, v37, s[16:17] sc1\n"
"v_cndmask_b32_e64 v38, v26, v30, s[42:43]\n"
"v_cvt_pk_bf16_f32 v38, v38, v38\n"
"v_lshlrev_b32 v38, 16, v38\n"
"v_or_b32 v38, 1, v38\n"
"v_subrev_u32 v114, 16384, v114\n"
"global_store_dword v114, v38, s[16:17] sc1\n"
"v_cndmask_b32_e64 v39, v27, v31, s[42:43]\n"
"v_cvt_pk_bf16_f32 v39, v39, v39\n"
"v_lshlrev_b32 v39, 16, v39\n"
"v_or_b32 v39, 1, v39\n"
"v_subrev_u32 v114, 16384, v114\n"
"global_store_dword v114, v39, s[16:17] sc1\n"
"s_add_u32 m0, s68, 0\n"
"s_nop 0\n"
"global_load_lds_dwordx4 v124, s[22:23] nt\n"
"s_add_u32 m0, s68, 1024\n"
"s_nop 0\n"
"global_load_lds_dwordx4 v125, s[22:23] nt\n"
"s_add_u32 m0, s68, 2048\n"
"s_nop 0\n"
"global_load_lds_dwordx4 v126, s[22:23] nt\n"
"s_add_u32 m0, s68, 3072\n"
"s_nop 0\n"
"global_load_lds_dwordx4 v127, s[22:23] nt\n"
"s_add_u32 s22, s22, 16384\n"
"s_addc_u32 s23, s23, 0\n"
"s_add_u32 m0, s68, 4096\n"
"s_nop 0\n"
"global_load_lds_dwordx4 v124, s[22:23] nt\n"
"s_add_u32 m0, s68, 5120\n"
"s_nop 0\n"
"global_load_lds_dwordx4 v125, s[22:23] nt\n"
"s_add_u32 m0, s68, 6144\n"
"s_nop 0\n"
"global_load_lds_dwordx4 v126, s[22:23] nt\n"
"s_add_u32 m0, s68, 7168\n"
"s_nop 0\n"
"global_load_lds_dwordx4 v127, s[22:23] nt\n"
"s_add_u32 s22, s22, 16384\n"
"s_addc_u32 s23, s23, 0\n"
"s_add_u32 m0, s68, 8192\n"
"s_nop 0\n"
"global_load_lds_dwordx4 v124, s[22:23] nt\n"
"s_add_u32 m0, s68, 9216\n"
"s_nop 0\n"
"global_load_lds_dwordx4 v125, s[22:23] nt\n"
"s_add_u32 m0, s68, 10240\n"
"s_nop 0\n"
"global_load_lds_dwordx4 v126, s[22:23] nt\n"
"s_add_u32 m0, s68, 11264\n"
"s_nop 0\n"
"global_load_lds_dwordx4 v127, s[22:23] nt\n"
"s_add_u32 s22, s22, 16384\n"
"s_addc_u32 s23, s23, 0\n"
"s_add_u32 m0, s68, 12288\n"
"s_nop 0\n"
"global_load_lds_dwordx4 v124, s[22:23] nt\n"
"s_add_u32 m0, s68, 13312\n"
"s_nop 0\n"
"global_load_lds_dwordx4 v125, s[22:23] nt\n"
"s_add_u32 m0, s68, 14336\n"
"s_nop 0\n"
"global_load_lds_dwordx4 v126, s[22:23] nt\n"
"s_add_u32 m0, s68, 15360\n"
"s_nop 0\n"
"global_load_lds_dwordx4 v127, s[22:23] nt\n"
"s_add_u32 s22, s22, 16384\n"
"s_addc_u32 s23, s23, 0\n"
"s_add_u32 m0, s68, 16384\n"
"s_nop 0\n"
"global_load_lds_dwordx4 v124, s[22:23] nt\n"
"s_add_u32 m0, s68, 17408\n"
"s_nop 0\n"
"global_load_lds_dwordx4 v125, s[22:23] nt\n"
"s_add_u32 m0, s68, 18432\n"
"s_nop 0\n"
"global_load_lds_dwordx4 v126, s[22:23] nt\n"
"s_add_u32 m0, s68, 19456\n"
"s_nop 0\n"
"global_load_lds_dwordx4 v127, s[22:23] nt\n"
"s_add_u32 s22, s22, 16384\n"
"s_addc_u32 s23, s23, 0\n"
"s_add_u32 m0, s68, 20480\n"
"s_nop 0\n"
"global_load_lds_dwordx4 v124, s[22:23] nt\n"
"s_add_u32 m0, s68, 21504\n"
"s_nop 0\n"
"global_load_lds_dwordx4 v125, s[22:23] nt\n"
"s_add_u32 m0, s68, 22528\n"
"s_nop 0\n"
"global_load_lds_dwordx4 v126, s[22:23] nt\n"
"s_add_u32 m0, s68, 23552\n"
"s_nop 0\n"
"global_load_lds_dwordx4 v127, s[22:23] nt\n"
"s_add_u32 s22, s22, 16384\n"
"s_addc_u32 s23, s23, 0\n"
"s_add_u32 m0, s68, 24576\n"
"s_nop 0\n"
"global_load_lds_dwordx4 v124, s[22:23] nt\n"
"s_add_u32 m0, s68, 25600\n"
"s_nop 0\n"
"global_load_lds_dwordx4 v125, s[22:23] nt\n"
"s_add_u32 m0, s68, 26624\n"
"s_nop 0\n"
"global_load_lds_dwordx4 v126, s[22:23] nt\n"
"s_add_u32 m0, s68, 27648\n"
"s_nop 0\n"
"global_load_lds_dwordx4 v127, s[22:23] nt\n"
"s_add_u32 s22, s22, 16384\n"
"s_addc_u32 s23, s23, 0\n"
"s_add_u32 m0, s68, 28672\n"
"s_nop 0\n"
"global_load_lds_dwordx4 v124, s[22:23] nt\n"
"s_add_u32 m0, s68, 29696\n"
"s_nop 0\n"
"global_load_lds_dwordx4 v125, s[22:23] nt\n"
"s_add_u32 m0, s68, 30720\n"
"s_nop 0\n"
"global_load_lds_dwordx4 v126, s[22:23] nt\n"
"s_add_u32 m0, s68, 31744\n"
"s_nop 0\n"
"global_load_lds_dwordx4 v127, s[22:23] nt\n"
"s_lshr_b32 s34, s33, 3\n"
"s_lshl_b32 s34, s34, 1\n"
"s_add_u32 s35, s34, 0\n"
"s_and_b32 s35, s35, 7\n"
"s_mul_i32 s35, s35, 176128\n"
"buffer_load_dwordx4 a[128:131], v116, s[24:27], s35 offen\n"
"buffer_load_dwordx4 a[132:135], v117, s[24:27], s35 offen\n"
"buffer_load_dwordx4 a[136:139], v118, s[24:27], s35 offen\n"
"buffer_load_dwordx4 a[140:143], v119, s[24:27], s35 offen\n"
"buffer_load_dwordx4 a[144:147], v120, s[24:27], s35 offen\n"
"buffer_load_dwordx4 a[148:151], v121, s[24:27], s35 offen\n"
"s_add_u32 s35, s34, 1\n"
"s_and_b32 s35, s35, 7\n"
"s_mul_i32 s35, s35, 176128\n"
"buffer_load_dwordx4 a[152:155], v116, s[24:27], s35 offen\n"
"buffer_load_dwordx4 a[156:159], v117, s[24:27], s35 offen\n"
"buffer_load_dwordx4 a[160:163], v118, s[24:27], s35 offen\n"
"buffer_load_dwordx4 a[164:167], v119, s[24:27], s35 offen\n"
"buffer_load_dwordx4 a[168:171], v120, s[24:27], s35 offen\n"
"buffer_load_dwordx4 a[172:175], v121, s[24:27], s35 offen\n"
"s_add_u32 s35, s34, 2\n"
"s_and_b32 s35, s35, 7\n"
"s_mul_i32 s35, s35, 176128\n"
"buffer_load_dwordx4 a[176:179], v116, s[24:27], s35 offen\n"
"buffer_load_dwordx4 a[180:183], v117, s[24:27], s35 offen\n"
"buffer_load_dwordx4 a[184:187], v118, s[24:27], s35 offen\n"
"buffer_load_dwordx4 a[188:191], v119, s[24:27], s35 offen\n"
"buffer_load_dwordx4 a[192:195], v120, s[24:27], s35 offen\n"
"buffer_load_dwordx4 a[196:199], v121, s[24:27], s35 offen\n"
"s_add_u32 s35, s34, 3\n"
"s_and_b32 s35, s35, 7\n"
"s_mul_i32 s35, s35, 176128\n"
"buffer_load_dwordx4 a[200:203], v116, s[24:27], s35 offen\n"
"buffer_load_dwordx4 a[204:207], v117, s[24:27], s35 offen\n"
"buffer_load_dwordx4 a[208:211], v118, s[24:27], s35 offen\n"
"buffer_load_dwordx4 a[212:215], v119, s[24:27], s35 offen\n"
"buffer_load_dwordx4 a[216:219], v120, s[24:27], s35 offen\n"
"buffer_load_dwordx4 a[220:223], v121, s[24:27], s35 offen\n"
"s_add_u32 s35, s34, 4\n"
"s_and_b32 s35, s35, 7\n"
"s_mul_i32 s35, s35, 176128\n"
"buffer_load_dwordx4 a[224:227], v116, s[24:27], s35 offen\n"
"buffer_load_dwordx4 a[228:231], v117, s[24:27], s35 offen\n"
"buffer_load_dwordx4 a[232:235], v118, s[24:27], s35 offen\n"
"buffer_load_dwordx4 a[236:239], v119, s[24:27], s35 offen\n"
"buffer_load_dwordx4 a[240:243], v120, s[24:27], s35 offen\n"
"buffer_load_dwordx4 a[244:247], v121, s[24:27], s35 offen\n"
"s_add_u32 s35, s34, 5\n"
"s_and_b32 s35, s35, 7\n"
"s_mul_i32 s35, s35, 176128\n"
"buffer_load_dwordx4 a[248:251], v116, s[24:27], s35 offen\n"
"buffer_load_dwordx4 a[252:255], v117, s[24:27], s35 offen\n"
"buffer_load_dwordx4 v[144:147], v118, s[24:27], s35 offen\n"
"buffer_load_dwordx4 v[148:151], v119, s[24:27], s35 offen\n"
"buffer_load_dwordx4 v[152:155], v120, s[24:27], s35 offen\n"
"buffer_load_dwordx4 v[156:159], v121, s[24:27], s35 offen\n"
"s_add_u32 s35, s34, 6\n"
"s_and_b32 s35, s35, 7\n"
"s_mul_i32 s35, s35, 176128\n"
"buffer_load_dwordx4 v[160:163], v116, s[24:27], s35 offen\n"
"buffer_load_dwordx4 v[164:167], v117, s[24:27], s35 offen\n"
"buffer_load_dwordx4 v[168:171], v118, s[24:27], s35 offen\n"
"buffer_load_dwordx4 v[172:175], v119, s[24:27], s35 offen\n"
"buffer_load_dwordx4 v[176:179], v120, s[24:27], s35 offen\n"
"buffer_load_dwordx4 v[180:183], v121, s[24:27], s35 offen\n"
"s_add_u32 s35, s34, 7\n"
"s_and_b32 s35, s35, 7\n"
"s_mul_i32 s35, s35, 176128\n"
"buffer_load_dwordx4 v[184:187], v116, s[24:27], s35 offen\n"
"buffer_load_dwordx4 v[188:191], v117, s[24:27], s35 offen\n"
"buffer_load_dwordx4 v[192:195], v118, s[24:27], s35 offen\n"
"buffer_load_dwordx4 v[196:199], v119, s[24:27], s35 offen\n"
"buffer_load_dwordx4 v[200:203], v120, s[24:27], s35 offen\n"
"buffer_load_dwordx4 v[204:207], v121, s[24:27], s35 offen\n"
"v_mov_b32 v44, 0x3f803f80\n"
"v_mov_b32 v45, 0x3f803f80\n"
"v_mov_b32 v46, 0x3f803f80\n"
"v_mov_b32 v47, 0x3f803f80\n"
"s_nop 1\n"
"v_mfma_f32_16x16x32_bf16 v[24:27], v[44:47], a[0:3], 0\n"
"v_mfma_f32_16x16x32_bf16 v[28:31], v[44:47], a[4:7], 0\n"
"v_mfma_f32_16x16x32_bf16 v[24:27], v[44:47], a[8:11], v[24:27]\n"
"v_mfma_f32_16x16x32_bf16 v[28:31], v[44:47], a[12:15], v[28:31]\n"
"v_mfma_f32_16x16x32_bf16 v[24:27], v[44:47], a[16:19], v[24:27]\n"
"v_mfma_f32_16x16x32_bf16 v[28:31], v[44:47], a[20:23], v[28:31]\n"
"v_mfma_f32_16x16x32_bf16 v[24:27], v[44:47], a[24:27], v[24:27]\n"
"v_mfma_f32_16x16x32_bf16 v[28:31], v[44:47], a[28:31], v[28:31]\n"
"v_mfma_f32_16x16x32_bf16 v[24:27], v[44:47], a[32:35], v[24:27]\n"
"v_mfma_f32_16x16x32_bf16 v[28:31], v[44:47], a[36:39], v[28:31]\n"
"v_mfma_f32_16x16x32_bf16 v[24:27], v[44:47], a[40:43], v[24:27]\n"
"v_mfma_f32_16x16x32_bf16 v[28:31], v[44:47], a[44:47], v[28:31]\n"
"v_mfma_f32_16x16x32_bf16 v[24:27], v[44:47], a[48:51], v[24:27]\n"
"v_mfma_f32_16x16x32_bf16 v[28:31], v[44:47], a[52:55], v[28:31]\n"
"v_mfma_f32_16x16x32_bf16 v[24:27], v[44:47], a[56:59], v[24:27]\n"
"v_mfma_f32_16x16x32_bf16 v[28:31], v[44:47], a[60:63], v[28:31]\n"
"v_mfma_f32_16x16x32_bf16 v[24:27], v[44:47], a[64:67], v[24:27]\n"
"v_mfma_f32_16x16x32_bf16 v[28:31], v[44:47], a[68:71], v[28:31]\n"
"v_mfma_f32_16x16x32_bf16 v[24:27], v[44:47], a[72:75], v[24:27]\n"
"v_mfma_f32_16x16x32_bf16 v[28:31], v[44:47], a[76:79], v[28:31]\n"
"v_mfma_f32_16x16x32_bf16 v[24:27], v[44:47], a[80:83], v[24:27]\n"
"v_mfma_f32_16x16x32_bf16 v[28:31], v[44:47], a[84:87], v[28:31]\n"
"v_mfma_f32_16x16x32_bf16 v[24:27], v[44:47], a[88:91], v[24:27]\n"
"v_mfma_f32_16x16x32_bf16 v[28:31], v[44:47], a[92:95], v[28:31]\n"
"v_mfma_f32_16x16x32_bf16 v[24:27], v[44:47], a[96:99], v[24:27]\n"
"v_mfma_f32_16x16x32_bf16 v[28:31], v[44:47], a[100:103], v[28:31]\n"
"v_mfma_f32_16x16x32_bf16 v[24:27], v[44:47], a[104:107], v[24:27]\n"
"v_mfma_f32_16x16x32_bf16 v[28:31], v[44:47], a[108:111], v[28:31]\n"
"v_mfma_f32_16x16x32_bf16 v[24:27], v[44:47], a[112:115], v[24:27]\n"
"v_mfma_f32_16x16x32_bf16 v[28:31], v[44:47], a[116:119], v[28:31]\n"
"v_mfma_f32_16x16x32_bf16 v[24:27], v[44:47], a[120:123], v[24:27]\n"
"v_mfma_f32_16x16x32_bf16 v[28:31], v[44:47], a[124:127], v[28:31]\n"
"s_nop 7\n"
"v_cndmask_b32_e64 v141, v24, v28, s[40:41]\n"
"s_waitcnt vmcnt(48)\n"
"ds_write_b128 v109, v[208:211] offset:0\n"
"ds_write_b128 v109, v[212:215] offset:1024\n"
"ds_write_b128 v109, v[216:219] offset:2048\n"
"ds_write_b128 v109, v[220:223] offset:3072\n"
"s_waitcnt lgkmcnt(0)\n"
"ds_read_b64_tr_b16 v[208:209], v110 offset:0\n"
"ds_read_b64_tr_b16 v[210:211], v110 offset:256\n"
"ds_read_b64_tr_b16 v[212:213], v110 offset:32\n"
"ds_read_b64_tr_b16 v[214:215], v110 offset:288\n"
"ds_read_b64_tr_b16 v[216:217], v110 offset:2048\n"
"ds_read_b64_tr_b16 v[218:219], v110 offset:2304\n"
"ds_read_b64_tr_b16 v[220:221], v110 offset:2080\n"
"ds_read_b64_tr_b16 v[222:223], v110 offset:2336\n"
"s_waitcnt lgkmcnt(0)\n"
"ds_write_b128 v109, v[224:227] offset:0\n"
"ds_write_b128 v109, v[228:231] offset:1024\n"
"ds_write_b128 v109, v[232:235] offset:2048\n"
"ds_write_b128 v109, v[236:239] offset:3072\n"
"s_waitcnt lgkmcnt(0)\n"
"ds_read_b64_tr_b16 v[224:225], v110 offset:0\n"
"ds_read_b64_tr_b16 v[226:227], v110 offset:256\n"
"ds_read_b64_tr_b16 v[228:229], v110 offset:32\n"
"ds_read_b64_tr_b16 v[230:231], v110 offset:288\n"
"ds_read_b64_tr_b16 v[232:233], v110 offset:2048\n"
"ds_read_b64_tr_b16 v[234:235], v110 offset:2304\n"
"ds_read_b64_tr_b16 v[236:237], v110 offset:2080\n"
"ds_read_b64_tr_b16 v[238:239], v110 offset:2336\n"
"s_waitcnt lgkmcnt(0)\n"
"ds_write_b128 v109, v[240:243] offset:0\n"
"ds_write_b128 v109, v[244:247] offset:1024\n"
"ds_write_b128 v109, v[248:251] offset:2048\n"
"ds_write_b128 v109, v[252:255] offset:3072\n"
"s_waitcnt lgkmcnt(0)\n"
"ds_read_b64_tr_b16 v[240:241], v110 offset:0\n"
"ds_read_b64_tr_b16 v[242:243], v110 offset:256\n"
"ds_read_b64_tr_b16 v[244:245], v110 offset:32\n"
"ds_read_b64_tr_b16 v[246:247], v110 offset:288\n"
"ds_read_b64_tr_b16 v[248:249], v110 offset:2048\n"
"ds_read_b64_tr_b16 v[250:251], v110 offset:2304\n"
"ds_read_b64_tr_b16 v[252:253], v110 offset:2080\n"
"ds_read_b64_tr_b16 v[254:255], v110 offset:2336\n"
"s_waitcnt lgkmcnt(0)\n"
"s_waitcnt vmcnt(42)\n"
"ds_write_b128 v109, a[128:131] offset:0\n"
"ds_write_b128 v109, a[132:135] offset:1024\n"
"ds_write_b128 v109, a[136:139] offset:2048\n"
"ds_write_b128 v109, a[140:143] offset:3072\n"
"ds_write_b128 v109, a[144:147] offset:4096\n"
"ds_write_b128 v109, a[148:151] offset:5120\n"
"s_waitcnt lgkmcnt(0)\n"
"ds_read_b64_tr_b16 a[128:129], v111 offset:0\n"
"ds_read_b64_tr_b16 a[130:131], v111 offset:384\n"
"ds_read_b64_tr_b16 a[132:133], v111 offset:32\n"
"ds_read_b64_tr_b16 a[134:135], v111 offset:416\n"
"ds_read_b64_tr_b16 a[136:137], v111 offset:64\n"
"ds_read_b64_tr_b16 a[138:139], v111 offset:448\n"
"ds_read_b64_tr_b16 a[140:141], v111 offset:3072\n"
"ds_read_b64_tr_b16 a[142:143], v111 offset:3456\n"
"ds_read_b64_tr_b16 a[144:145], v111 offset:3104\n"
"ds_read_b64_tr_b16 a[146:147], v111 offset:3488\n"
"ds_read_b64_tr_b16 a[148:149], v111 offset:3136\n"
"ds_read_b64_tr_b16 a[150:151], v111 offset:3520\n"
"s_waitcnt lgkmcnt(0)\n"
"s_waitcnt vmcnt(36)\n"
"ds_write_b128 v109, a[152:155] offset:0\n"
"ds_write_b128 v109, a[156:159] offset:1024\n"
"ds_write_b128 v109, a[160:163] offset:2048\n"
"ds_write_b128 v109, a[164:167] offset:3072\n"
"ds_write_b128 v109, a[168:171] offset:4096\n"
"ds_write_b128 v109, a[172:175] offset:5120\n"
"s_waitcnt lgkmcnt(0)\n"
"ds_read_b64_tr_b16 a[152:153], v111 offset:0\n"
"ds_read_b64_tr_b16 a[154:155], v111 offset:384\n"
"ds_read_b64_tr_b16 a[156:157], v111 offset:32\n"
"ds_read_b64_tr_b16 a[158:159], v111 offset:416\n"
"ds_read_b64_tr_b16 a[160:161], v111 offset:64\n"
"ds_read_b64_tr_b16 a[162:163], v111 offset:448\n"
"ds_read_b64_tr_b16 a[164:165], v111 offset:3072\n"
"ds_read_b64_tr_b16 a[166:167], v111 offset:3456\n"
"ds_read_b64_tr_b16 a[168:169], v111 offset:3104\n"
"ds_read_b64_tr_b16 a[170:171], v111 offset:3488\n"
"ds_read_b64_tr_b16 a[172:173], v111 offset:3136\n"
"ds_read_b64_tr_b16 a[174:175], v111 offset:3520\n"
"s_waitcnt lgkmcnt(0)\n"
"s_waitcnt vmcnt(30)\n"
"ds_write_b128 v109, a[176:179] offset:0\n"
"ds_write_b128 v109, a[180:183] offset:1024\n"
"ds_write_b128 v109, a[184:187] offset:2048\n"
"ds_write_b128 v109, a[188:191] offset:3072\n"
"ds_write_b128 v109, a[192:195] offset:4096\n"
"ds_write_b128 v109, a[196:199] offset:5120\n"
"s_waitcnt lgkmcnt(0)\n"
"ds_read_b64_tr_b16 a[176:177], v111 offset:0\n"
"ds_read_b64_tr_b16 a[178:179], v111 offset:384\n"
"ds_read_b64_tr_b16 a[180:181], v111 offset:32\n"
"ds_read_b64_tr_b16 a[182:183], v111 offset:416\n"
"ds_read_b64_tr_b16 a[184:185], v111 offset:64\n"
"ds_read_b64_tr_b16 a[186:187], v111 offset:448\n"
"ds_read_b64_tr_b16 a[188:189], v111 offset:3072\n"
"ds_read_b64_tr_b16 a[190:191], v111 offset:3456\n"
"ds_read_b64_tr_b16 a[192:193], v111 offset:3104\n"
"ds_read_b64_tr_b16 a[194:195], v111 offset:3488\n"
"ds_read_b64_tr_b16 a[196:197], v111 offset:3136\n"
"ds_read_b64_tr_b16 a[198:199], v111 offset:3520\n"
"s_waitcnt lgkmcnt(0)\n"
"s_waitcnt vmcnt(24)\n"
"ds_write_b128 v109, a[200:203] offset:0\n"
"ds_write_b128 v109, a[204:207] offset:1024\n"
"ds_write_b128 v109, a[208:211] offset:2048\n"
"ds_write_b128 v109, a[212:215] offset:3072\n"
"ds_write_b128 v109, a[216:219] offset:4096\n"
"ds_write_b128 v109, a[220:223] offset:5120\n"
"s_waitcnt lgkmcnt(0)\n"
"ds_read_b64_tr_b16 a[200:201], v111 offset:0\n"
"ds_read_b64_tr_b16 a[202:203], v111 offset:384\n"
"ds_read_b64_tr_b16 a[204:205], v111 offset:32\n"
"ds_read_b64_tr_b16 a[206:207], v111 offset:416\n"
"ds_read_b64_tr_b16 a[208:209], v111 offset:64\n"
"ds_read_b64_tr_b16 a[210:211], v111 offset:448\n"
"ds_read_b64_tr_b16 a[212:213], v111 offset:3072\n"
"ds_read_b64_tr_b16 a[214:215], v111 offset:3456\n"
"ds_read_b64_tr_b16 a[216:217], v111 offset:3104\n"
"ds_read_b64_tr_b16 a[218:219], v111 offset:3488\n"
"ds_read_b64_tr_b16 a[220:221], v111 offset:3136\n"
"ds_read_b64_tr_b16 a[222:223], v111 offset:3520\n"
"s_waitcnt lgkmcnt(0)\n"
"s_waitcnt vmcnt(18)\n"
"ds_write_b128 v109, a[224:227] offset:0\n"
"ds_write_b128 v109, a[228:231] offset:1024\n"
"ds_write_b128 v109, a[232:235] offset:2048\n"
"ds_write_b128 v109, a[236:239] offset:3072\n"
"ds_write_b128 v109, a[240:243] offset:4096\n"
"ds_write_b128 v109, a[244:247] offset:5120\n"
"s_waitcnt lgkmcnt(0)\n"
"ds_read_b64_tr_b16 a[224:225], v111 offset:0\n"
"ds_read_b64_tr_b16 a[226:227], v111 offset:384\n"
"ds_read_b64_tr_b16 a[228:229], v111 offset:32\n"
"ds_read_b64_tr_b16 a[230:231], v111 offset:416\n"
"ds_read_b64_tr_b16 a[232:233], v111 offset:64\n"
"ds_read_b64_tr_b16 a[234:235], v111 offset:448\n"
"ds_read_b64_tr_b16 a[236:237], v111 offset:3072\n"
"ds_read_b64_tr_b16 a[238:239], v111 offset:3456\n"
"ds_read_b64_tr_b16 a[240:241], v111 offset:3104\n"
"ds_read_b64_tr_b16 a[242:243], v111 offset:3488\n"
"ds_read_b64_tr_b16 a[244:245], v111 offset:3136\n"
"ds_read_b64_tr_b16 a[246:247], v111 offset:3520\n"
"s_waitcnt lgkmcnt(0)\n"
"s_waitcnt vmcnt(12)\n"
"ds_write_b128 v109, a[248:251] offset:0\n"
"ds_write_b128 v109, a[252:255] offset:1024\n"
"ds_write_b128 v109, v[144:147] offset:2048\n"
"ds_write_b128 v109, v[148:151] offset:3072\n"
"ds_write_b128 v109, v[152:155] offset:4096\n"
"ds_write_b128 v109, v[156:159] offset:5120\n"
"s_waitcnt lgkmcnt(0)\n"
"ds_read_b64_tr_b16 a[248:249], v111 offset:0\n"
"ds_read_b64_tr_b16 a[250:251], v111 offset:384\n"
"ds_read_b64_tr_b16 a[252:253], v111 offset:32\n"
"ds_read_b64_tr_b16 a[254:255], v111 offset:416\n"
"ds_read_b64_tr_b16 v[144:145], v111 offset:64\n"
"ds_read_b64_tr_b16 v[146:147], v111 offset:448\n"
"ds_read_b64_tr_b16 v[148:149], v111 offset:3072\n"
"ds_read_b64_tr_b16 v[150:151], v111 offset:3456\n"
"ds_read_b64_tr_b16 v[152:153], v111 offset:3104\n"
"ds_read_b64_tr_b16 v[154:155], v111 offset:3488\n"
"ds_read_b64_tr_b16 v[156:157], v111 offset:3136\n"
"ds_read_b64_tr_b16 v[158:159], v111 offset:3520\n"
"s_waitcnt lgkmcnt(0)\n"
"s_waitcnt vmcnt(6)\n"
"ds_write_b128 v109, v[160:163] offset:0\n"
"ds_write_b128 v109, v[164:167] offset:1024\n"
"ds_write_b128 v109, v[168:171] offset:2048\n"
"ds_write_b128 v109, v[172:175] offset:3072\n"
"ds_write_b128 v109, v[176:179] offset:4096\n"
"ds_write_b128 v109, v[180:183] offset:5120\n"
"s_waitcnt lgkmcnt(0)\n"
"ds_read_b64_tr_b16 v[160:161], v111 offset:0\n"
"ds_read_b64_tr_b16 v[162:163], v111 offset:384\n"
"ds_read_b64_tr_b16 v[164:165], v111 offset:32\n"
"ds_read_b64_tr_b16 v[166:167], v111 offset:416\n"
"ds_read_b64_tr_b16 v[168:169], v111 offset:64\n"
"ds_read_b64_tr_b16 v[170:171], v111 offset:448\n"
"ds_read_b64_tr_b16 v[172:173], v111 offset:3072\n"
"ds_read_b64_tr_b16 v[174:175], v111 offset:3456\n"
"ds_read_b64_tr_b16 v[176:177], v111 offset:3104\n"
"ds_read_b64_tr_b16 v[178:179], v111 offset:3488\n"
"ds_read_b64_tr_b16 v[180:181], v111 offset:3136\n"
"ds_read_b64_tr_b16 v[182:183], v111 offset:3520\n"
"s_waitcnt lgkmcnt(0)\n"
"s_waitcnt vmcnt(0)\n"
"ds_write_b128 v109, v[184:187] offset:0\n"
"ds_write_b128 v109, v[188:191] offset:1024\n"
"ds_write_b128 v109, v[192:195] offset:2048\n"
"ds_write_b128 v109, v[196:199] offset:3072\n"
"ds_write_b128 v109, v[200:203] offset:4096\n"
"ds_write_b128 v109, v[204:207] offset:5120\n"
"s_waitcnt lgkmcnt(0)\n"
"ds_read_b64_tr_b16 v[184:185], v111 offset:0\n"
"ds_read_b64_tr_b16 v[186:187], v111 offset:384\n"
"ds_read_b64_tr_b16 v[188:189], v111 offset:32\n"
"ds_read_b64_tr_b16 v[190:191], v111 offset:416\n"
"ds_read_b64_tr_b16 v[192:193], v111 offset:64\n"
"ds_read_b64_tr_b16 v[194:195], v111 offset:448\n"
"ds_read_b64_tr_b16 v[196:197], v111 offset:3072\n"
"ds_read_b64_tr_b16 v[198:199], v111 offset:3456\n"
"ds_read_b64_tr_b16 v[200:201], v111 offset:3104\n"
"ds_read_b64_tr_b16 v[202:203], v111 offset:3488\n"
"ds_read_b64_tr_b16 v[204:205], v111 offset:3136\n"
"ds_read_b64_tr_b16 v[206:207], v111 offset:3520\n"
"s_waitcnt lgkmcnt(0)\n"
"v_mfma_f32_16x16x32_bf16 v[24:27], v[44:47], a[128:131], 0\n"
"v_mfma_f32_16x16x32_bf16 v[28:31], v[44:47], a[132:135], 0\n"
"v_mfma_f32_16x16x32_bf16 v[32:35], v[44:47], a[136:139], 0\n"
"v_mfma_f32_16x16x32_bf16 v[24:27], v[44:47], a[140:143], v[24:27]\n"
"v_mfma_f32_16x16x32_bf16 v[28:31], v[44:47], a[144:147], v[28:31]\n"
"v_mfma_f32_16x16x32_bf16 v[32:35], v[44:47], a[148:151], v[32:35]\n"
"v_mfma_f32_16x16x32_bf16 v[24:27], v[44:47], a[152:155], v[24:27]\n"
"v_mfma_f32_16x16x32_bf16 v[28:31], v[44:47], a[156:159], v[28:31]\n"
"v_mfma_f32_16x16x32_bf16 v[32:35], v[44:47], a[160:163], v[32:35]\n"
"v_mfma_f32_16x16x32_bf16 v[24:27], v[44:47], a[164:167], v[24:27]\n"
"v_mfma_f32_16x16x32_bf16 v[28:31], v[44:47], a[168:171], v[28:31]\n"
"v_mfma_f32_16x16x32_bf16 v[32:35], v[44:47], a[172:175], v[32:35]\n"
"v_mfma_f32_16x16x32_bf16 v[24:27], v[44:47], a[176:179], v[24:27]\n"
"v_mfma_f32_16x16x32_bf16 v[28:31], v[44:47], a[180:183], v[28:31]\n"
"v_mfma_f32_16x16x32_bf16 v[32:35], v[44:47], a[184:187], v[32:35]\n"
"v_mfma_f32_16x16x32_bf16 v[24:27], v[44:47], a[188:191], v[24:27]\n"
"v_mfma_f32_16x16x32_bf16 v[28:31], v[44:47], a[192:195], v[28:31]\n"
"v_mfma_f32_16x16x32_bf16 v[32:35], v[44:47], a[196:199], v[32:35]\n"
"v_mfma_f32_16x16x32_bf16 v[24:27], v[44:47], a[200:203], v[24:27]\n"
"v_mfma_f32_16x16x32_bf16 v[28:31], v[44:47], a[204:207], v[28:31]\n"
"v_mfma_f32_16x16x32_bf16 v[32:35], v[44:47], a[208:211], v[32:35]\n"
"v_mfma_f32_16x16x32_bf16 v[24:27], v[44:47], a[212:215], v[24:27]\n"
"v_mfma_f32_16x16x32_bf16 v[28:31], v[44:47], a[216:219], v[28:31]\n"
"v_mfma_f32_16x16x32_bf16 v[32:35], v[44:47], a[220:223], v[32:35]\n"
"v_mfma_f32_16x16x32_bf16 v[24:27], v[44:47], a[224:227], v[24:27]\n"
"v_mfma_f32_16x16x32_bf16 v[28:31], v[44:47], a[228:231], v[28:31]\n"
"v_mfma_f32_16x16x32_bf16 v[32:35], v[44:47], a[232:235], v[32:35]\n"
"v_mfma_f32_16x16x32_bf16 v[24:27], v[44:47], a[236:239], v[24:27]\n"
"v_mfma_f32_16x16x32_bf16 v[28:31], v[44:47], a[240:243], v[28:31]\n"
"v_mfma_f32_16x16x32_bf16 v[32:35], v[44:47], a[244:247], v[32:35]\n"
"v_mfma_f32_16x16x32_bf16 v[24:27], v[44:47], a[248:251], v[24:27]\n"
"v_mfma_f32_16x16x32_bf16 v[28:31], v[44:47], a[252:255], v[28:31]\n"
"v_mfma_f32_16x16x32_bf16 v[32:35], v[44:47], v[144:147], v[32:35]\n"
"v_mfma_f32_16x16x32_bf16 v[24:27], v[44:47], v[148:151], v[24:27]\n"
"v_mfma_f32_16x16x32_bf16 v[28:31], v[44:47], v[152:155], v[28:31]\n"
"v_mfma_f32_16x16x32_bf16 v[32:35], v[44:47], v[156:159], v[32:35]\n"
"v_mfma_f32_16x16x32_bf16 v[24:27], v[44:47], v[160:163], v[24:27]\n"
"v_mfma_f32_16x16x32_bf16 v[28:31], v[44:47], v[164:167], v[28:31]\n"
"v_mfma_f32_16x16x32_bf16 v[32:35], v[44:47], v[168:171], v[32:35]\n"
"v_mfma_f32_16x16x32_bf16 v[24:27], v[44:47], v[172:175], v[24:27]\n"
"v_mfma_f32_16x16x32_bf16 v[28:31], v[44:47], v[176:179], v[28:31]\n"
"v_mfma_f32_16x16x32_bf16 v[32:35], v[44:47], v[180:183], v[32:35]\n"
"v_mfma_f32_16x16x32_bf16 v[24:27], v[44:47], v[184:187], v[24:27]\n"
"v_mfma_f32_16x16x32_bf16 v[28:31], v[44:47], v[188:191], v[28:31]\n"
"v_mfma_f32_16x16x32_bf16 v[32:35], v[44:47], v[192:195], v[32:35]\n"
"v_mfma_f32_16x16x32_bf16 v[24:27], v[44:47], v[196:199], v[24:27]\n"
"v_mfma_f32_16x16x32_bf16 v[28:31], v[44:47], v[200:203], v[28:31]\n"
"v_mfma_f32_16x16x32_bf16 v[32:35], v[44:47], v[204:207], v[32:35]\n"
"s_nop 6\n"
"v_cndmask_b32_e64 v140, v24, v28, s[44:45]\n"
"v_cndmask_b32_e64 v140, v140, v32, s[42:43]\n"
"s_waitcnt vmcnt(0)\n"
"s_barrier\n"
"v_mov_b32 v36, 0\n"
"ds_write_b32 v2, v36\n"
"ds_write_b32 v2, v36 offset:1024\n"
"v_mov_b32 v20, 0\n"
"v_mov_b32 v21, 0\n"
"v_mov_b32 v22, 0\n"
"v_mov_b32 v23, 0\n"
"s_mov_b32 s32, 0\n"
"s_mov_b32 s66, s61\n"
"s_sub_u32 s67, s61, 262144\n"
"s_waitcnt lgkmcnt(0)\n"
"s_nop 7\n"
"s_nop 3\n"
".Lch_round:\n"
"s_cmp_lt_u32 s32, 8\n"
"s_cselect_b32 s76, 1, 0\n"
"s_cmp_ge_u32 s32, 2\n"
"s_cselect_b32 s77, 1, 0\n"
"ds_read_b64_tr_b16 v[76:77], v4 offset:0\n"
"ds_read_b64_tr_b16 v[78:79], v6 offset:0\n"
"ds_read_b64_tr_b16 v[80:81], v5 offset:0\n"
"ds_read_b64_tr_b16 v[82:83], v7 offset:0\n"
"ds_read_b64_tr_b16 v[84:85], v4 offset:8192\n"
"ds_read_b64_tr_b16 v[86:87], v6 offset:8192\n"
"ds_read_b64_tr_b16 v[88:89], v5 offset:8192\n"
"ds_read_b64_tr_b16 v[90:91], v7 offset:8192\n"
"ds_read_b64_tr_b16 v[92:93], v4 offset:16384\n"
"ds_read_b64_tr_b16 v[94:95], v6 offset:16384\n"
"ds_read_b64_tr_b16 v[96:97], v5 offset:16384\n"
"ds_read_b64_tr_b16 v[98:99], v7 offset:16384\n"
"ds_read_b64_tr_b16 v[100:101], v4 offset:24576\n"
"ds_read_b64_tr_b16 v[102:103], v6 offset:24576\n"
"ds_read_b64_tr_b16 v[104:105], v5 offset:24576\n"
"ds_read_b64_tr_b16 v[106:107], v7 offset:24576\n"
"ds_read_b64_tr_b16 v[108:109], v4 offset:32768\n"
"ds_read_b64_tr_b16 v[110:111], v6 offset:32768\n"
"ds_read_b64_tr_b16 v[112:113], v5 offset:32768\n"
"ds_read_b64_tr_b16 v[114:115], v7 offset:32768\n"
"ds_read_b64_tr_b16 v[116:117], v4 offset:40960\n"
"ds_read_b64_tr_b16 v[118:119], v6 offset:40960\n"
"ds_read_b64_tr_b16 v[120:121], v5 offset:40960\n"
"ds_read_b64_tr_b16 v[122:123], v7 offset:40960\n"
"ds_read_b64_tr_b16 v[124:125], v4 offset:49152\n"
"ds_read_b64_tr_b16 v[126:127], v6 offset:49152\n"
"ds_read_b64_tr_b16 v[128:129], v5 offset:49152\n"
"ds_read_b64_tr_b16 v[130:131], v7 offset:49152\n"
"ds_read_b64_tr_b16 v[132:133], v4 offset:57344\n"
"ds_read_b64_tr_b16 v[134:135], v6 offset:57344\n"
"ds_read_b64_tr_b16 v[136:137], v5 offset:57344\n"
"ds_read_b64_tr_b16 v[138:139], v7 offset:57344\n"
"s_cmp_gt_u32 s76, s62\n"
"s_cbranch_scc0 .Lch_res1_1\n"
"s_lshl_b32 s33, s32, 10\n"
"v_add_u32 v36, s33, v142\n"
"global_load_dwordx2 v[38:39], v36, s[18:19]\n"
"s_waitcnt vmcnt(0)\n"
"v_lshlrev_b32 v20, 16, v38\n"
"v_and_b32 v21, 0xffff0000, v38\n"
"v_lshlrev_b32 v22, 16, v39\n"
"v_and_b32 v23, 0xffff0000, v39\n"
"s_branch .Lch_res2_2\n"
".Lch_res1_1:\n"
"s_cmp_eq_u32 s62, 0\n"
"s_cselect_b32 s33, 1, s32\n"
"s_and_b32 s33, s33, 1\n"
"s_lshl_b32 s33, s33, 11\n"
"v_add_u32 v36, s33, v16\n"
"ds_read_b128 v[20:23], v36\n"
".Lch_res2_2:\n"
"s_waitcnt lgkmcnt(0)\n"
"s_cmp_eq_u32 s77, 0\n"
"s_cbranch_scc1 .Lch_nob_3\n"
"v_add_u32 v36, s67, v0\n"
"v_add_u32 v37, s67, v1\n"
"v_mov_b32 v40, 262144\n"
"s_mov_b32 m0, 143360\n"
"s_mov_b32 s36, 0\n"
"s_mov_b32 s37, 0\n"
"global_load_dword v58, v37, s[16:17] sc1\n"
"global_load_dwordx2 v[44:45], v36, s[16:17] offset:0 sc1\n"
"global_load_dwordx2 v[46:47], v36, s[16:17] offset:512 sc1\n"
"global_load_dwordx2 v[48:49], v36, s[16:17] offset:1024 sc1\n"
"global_load_dwordx2 v[50:51], v36, s[16:17] offset:1536 sc1\n"
"global_load_dwordx2 v[52:53], v36, s[16:17] offset:2048 sc1\n"
"global_load_dwordx2 v[54:55], v36, s[16:17] offset:2560 sc1\n"
"global_load_dwordx2 v[56:57], v36, s[16:17] offset:3072 sc1\n"
".Lch_pl_5:\n"
"s_waitcnt vmcnt(7)\n"
"s_bitcmp1_b32 s37, 0\n"
"s_cbranch_scc1 .Lch_pdm_7\n"
"v_and_b32 v38, 1, v58\n"
"v_cmp_ne_u32 vcc, 0, v38\n"
"s_nop 0\n"
"s_cmp_eq_u64 vcc, -1\n"
"s_cbranch_scc1 .Lch_pv_9\n"
"global_load_dword v58, v37, s[16:17] sc1\n"
"s_branch .Lch_pnx_8\n"
".Lch_pv_9:\n"
"s_bitset1_b32 s37, 0\n"
"s_cmp_eq_u32 s37, 255\n"
"s_cbranch_scc1 .Lch_pd_6\n"
".Lch_pdm_7:\n"
"global_load_lds_dword v40, s[16:17] sc1\n"
".Lch_pnx_8:\n"
"s_waitcnt vmcnt(7)\n"
"s_bitcmp1_b32 s37, 1\n"
"s_cbranch_scc1 .Lch_pdm_10\n"
"v_bitop3_b32 v38, v44, 1, v45 bitop3:0x80\n"
"v_cmp_ne_u32 vcc, 0, v38\n"
"s_nop 0\n"
"s_cmp_eq_u64 vcc, -1\n"
"s_cbranch_scc1 .Lch_pv_12\n"
"global_load_dwordx2 v[44:45], v36, s[16:17] offset:0 sc1\n"
"s_branch .Lch_pnx_11\n"
".Lch_pv_12:\n"
"s_bitset1_b32 s37, 1\n"
"s_cmp_eq_u32 s37, 255\n"
"s_cbranch_scc1 .Lch_pd_6\n"
".Lch_pdm_10:\n"
"global_load_lds_dword v40, s[16:17] sc1\n"
".Lch_pnx_11:\n"
"s_waitcnt vmcnt(7)\n"
"s_bitcmp1_b32 s37, 2\n"
"s_cbranch_scc1 .Lch_pdm_13\n"
"v_bitop3_b32 v38, v46, 1, v47 bitop3:0x80\n"
"v_cmp_ne_u32 vcc, 0, v38\n"
"s_nop 0\n"
"s_cmp_eq_u64 vcc, -1\n"
"s_cbranch_scc1 .Lch_pv_15\n"
"global_load_dwordx2 v[46:47], v36, s[16:17] offset:512 sc1\n"
"s_branch .Lch_pnx_14\n"
".Lch_pv_15:\n"
"s_bitset1_b32 s37, 2\n"
"s_cmp_eq_u32 s37, 255\n"
"s_cbranch_scc1 .Lch_pd_6\n"
".Lch_pdm_13:\n"
"global_load_lds_dword v40, s[16:17] sc1\n"
".Lch_pnx_14:\n"
"s_waitcnt vmcnt(7)\n"
"s_bitcmp1_b32 s37, 3\n"
"s_cbranch_scc1 .Lch_pdm_16\n"
"v_bitop3_b32 v38, v48, 1, v49 bitop3:0x80\n"
"v_cmp_ne_u32 vcc, 0, v38\n"
"s_nop 0\n"
"s_cmp_eq_u64 vcc, -1\n"
"s_cbranch_scc1 .Lch_pv_18\n"
"global_load_dwordx2 v[48:49], v36, s[16:17] offset:1024 sc1\n"
"s_branch .Lch_pnx_17\n"
".Lch_pv_18:\n"
"s_bitset1_b32 s37, 3\n"
"s_cmp_eq_u32 s37, 255\n"
"s_cbranch_scc1 .Lch_pd_6\n"
".Lch_pdm_16:\n"
"global_load_lds_dword v40, s[16:17] sc1\n"
".Lch_pnx_17:\n"
"s_waitcnt vmcnt(7)\n"
"s_bitcmp1_b32 s37, 4\n"
"s_cbranch_scc1 .Lch_pdm_19\n"
"v_bitop3_b32 v38, v50, 1, v51 bitop3:0x80\n"
"v_cmp_ne_u32 vcc, 0, v38\n"
"s_nop 0\n"
"s_cmp_eq_u64 vcc, -1\n"
"s_cbranch_scc1 .Lch_pv_21\n"
"global_load_dwordx2 v[50:51], v36, s[16:17] offset:1536 sc1\n"
"s_branch .Lch_pnx_20\n"
".Lch_pv_21:\n"
"s_bitset1_b32 s37, 4\n"
"s_cmp_eq_u32 s37, 255\n"
"s_cbranch_scc1 .Lch_pd_6\n"
".Lch_pdm_19:\n"
"global_load_lds_dword v40, s[16:17] sc1\n"
".Lch_pnx_20:\n"
"s_waitcnt vmcnt(7)\n"
"s_bitcmp1_b32 s37, 5\n"
"s_cbranch_scc1 .Lch_pdm_22\n"
"v_bitop3_b32 v38, v52, 1, v53 bitop3:0x80\n"
"v_cmp_ne_u32 vcc, 0, v38\n"
"s_nop 0\n"
"s_cmp_eq_u64 vcc, -1\n"
"s_cbranch_scc1 .Lch_pv_24\n"
"global_load_dwordx2 v[52:53], v36, s[16:17] offset:2048 sc1\n"
"s_branch .Lch_pnx_23\n"
".Lch_pv_24:\n"
"s_bitset1_b32 s37, 5\n"
"s_cmp_eq_u32 s37, 255\n"
"s_cbranch_scc1 .Lch_pd_6\n"
".Lch_pdm_22:\n"
"global_load_lds_dword v40, s[16:17] sc1\n"
".Lch_pnx_23:\n"
"s_waitcnt vmcnt(7)\n"
"s_bitcmp1_b32 s37, 6\n"
"s_cbranch_scc1 .Lch_pdm_25\n"
"v_bitop3_b32 v38, v54, 1, v55 bitop3:0x80\n"
"v_cmp_ne_u32 vcc, 0, v38\n"
"s_nop 0\n"
"s_cmp_eq_u64 vcc, -1\n"
"s_cbranch_scc1 .Lch_pv_27\n"
"global_load_dwordx2 v[54:55], v36, s[16:17] offset:2560 sc1\n"
"s_branch .Lch_pnx_26\n"
".Lch_pv_27:\n"
"s_bitset1_b32 s37, 6\n"
"s_cmp_eq_u32 s37, 255\n"
"s_cbranch_scc1 .Lch_pd_6\n"
".Lch_pdm_25:\n"
"global_load_lds_dword v40, s[16:17] sc1\n"
".Lch_pnx_26:\n"
"s_waitcnt vmcnt(7)\n"
"s_bitcmp1_b32 s37, 7\n"
"s_cbranch_scc1 .Lch_pdm_28\n"
"v_bitop3_b32 v38, v56, 1, v57 bitop3:0x80\n"
"v_cmp_ne_u32 vcc, 0, v38\n"
"s_nop 0\n"
"s_cmp_eq_u64 vcc, -1\n"
"s_cbranch_scc1 .Lch_pv_30\n"
"global_load_dwordx2 v[56:57], v36, s[16:17] offset:3072 sc1\n"
"s_branch .Lch_pnx_29\n"
".Lch_pv_30:\n"
"s_bitset1_b32 s37, 7\n"
"s_cmp_eq_u32 s37, 255\n"
"s_cbranch_scc1 .Lch_pd_6\n"
".Lch_pdm_28:\n"
"global_load_lds_dword v40, s[16:17] sc1\n"
".Lch_pnx_29:\n"
"s_add_u32 s36, s36, 1\n"
"s_cmp_lt_u32 s36, 2000000\n"
"s_cbranch_scc1 .Lch_pl_5\n"
"s_branch .Lch_abort\n"
".Lch_pd_6:\n"
"v_mov_b32 v39, 0xff800000\n"
"v_cndmask_b32_e64 v38, v58, v39, s[38:39]\n"
"s_nop 1\n"
"v_max_f32_dpp v38, v38, v38 quad_perm:[1,0,3,2] row_mask:0xf bank_mask:0xf\n"
"s_nop 1\n"
"v_max_f32_dpp v38, v38, v38 quad_perm:[2,3,0,1] row_mask:0xf bank_mask:0xf\n"
"s_nop 1\n"
"v_max_f32_dpp v38, v38, v38 row_half_mirror row_mask:0xf bank_mask:0xf\n"
"s_nop 1\n"
"v_max_f32_dpp v38, v38, v38 row_mirror row_mask:0xf bank_mask:0xf\n"
"v_sub_f32 v38, v58, v38\n"
"v_exp_f32 v38, v38\n"
"s_nop 1\n"
"v_mov_b32_dpp v39, v38 quad_perm:[0,0,2,2] row_mask:0xf bank_mask:0xf\n"
"v_mul_f32 v39, v39, v58\n"
"v_cndmask_b32_e64 v39, 0, v39, s[52:53]\n"
"s_nop 1\n"
"v_add_f32_dpp v39, v39, v39 quad_perm:[1,0,3,2] row_mask:0xf bank_mask:0xf\n"
"s_nop 1\n"
"v_add_f32_dpp v39, v39, v39 quad_perm:[2,3,0,1] row_mask:0xf bank_mask:0xf\n"
"s_nop 1\n"
"v_add_f32_dpp v39, v39, v39 row_half_mirror row_mask:0xf bank_mask:0xf\n"
"s_nop 1\n"
"v_add_f32_dpp v39, v39, v39 row_mirror row_mask:0xf bank_mask:0xf\n"
"v_rcp_f32 v39, v39\n"
"s_nop 0\n"
"v_mul_f32 v38, v38, v39\n"
"s_nop 0\n"
"v_readlane_b32 s54, v38, 0\n"
"v_readlane_b32 s55, v38, 2\n"
"v_readlane_b32 s56, v38, 4\n"
"v_readlane_b32 s57, v38, 6\n"
"v_readlane_b32 s58, v38, 8\n"
"v_readlane_b32 s59, v38, 10\n"
"v_readlane_b32 s60, v38, 12\n"
"v_mul_f32 v39, s54, v44\n"
"v_mul_f32 v40, s54, v45\n"
"v_fmac_f32 v39, s55, v46\n"
"v_fmac_f32 v40, s55, v47\n"
"v_fmac_f32 v39, s56, v48\n"
"v_fmac_f32 v40, s56, v49\n"
"v_fmac_f32 v39, s57, v50\n"
"v_fmac_f32 v40, s57, v51\n"
"v_fmac_f32 v39, s58, v52\n"
"v_fmac_f32 v40, s58, v53\n"
"v_fmac_f32 v39, s59, v54\n"
"v_fmac_f32 v40, s59, v55\n"
"v_fmac_f32 v39, s60, v56\n"
"v_fmac_f32 v40, s60, v57\n"
"v_cvt_pk_bf16_f32 v39, v39, v40\n"
"ds_write_b32 v2, v39 offset:1024\n"
".Lch_nob_3:\n"
"v_add_u32 v36, s66, v0\n"
"v_add_u32 v37, s66, v1\n"
"v_mov_b32 v40, 262144\n"
"s_mov_b32 m0, 143360\n"
"s_mov_b32 s36, 0\n"
"s_mov_b32 s37, 0\n"
"global_load_dword v58, v37, s[16:17] sc1\n"
"global_load_dwordx2 v[44:45], v36, s[16:17] offset:0 sc1\n"
"global_load_dwordx2 v[46:47], v36, s[16:17] offset:512 sc1\n"
"global_load_dwordx2 v[48:49], v36, s[16:17] offset:1024 sc1\n"
"global_load_dwordx2 v[50:51], v36, s[16:17] offset:1536 sc1\n"
"global_load_dwordx2 v[52:53], v36, s[16:17] offset:2048 sc1\n"
"global_load_dwordx2 v[54:55], v36, s[16:17] offset:2560 sc1\n"
"global_load_dwordx2 v[56:57], v36, s[16:17] offset:3072 sc1\n"
".Lch_pl_31:\n"
"s_waitcnt vmcnt(7)\n"
"s_bitcmp1_b32 s37, 0\n"
"s_cbranch_scc1 .Lch_pdm_33\n"
"v_and_b32 v38, 1, v58\n"
"v_cmp_ne_u32 vcc, 0, v38\n"
"s_nop 0\n"
"s_cmp_eq_u64 vcc, -1\n"
"s_cbranch_scc1 .Lch_pv_35\n"
"global_load_dword v58, v37, s[16:17] sc1\n"
"s_branch .Lch_pnx_34\n"
".Lch_pv_35:\n"
"s_bitset1_b32 s37, 0\n"
"s_cmp_eq_u32 s37, 255\n"
"s_cbranch_scc1 .Lch_pd_32\n"
".Lch_pdm_33:\n"
"global_load_lds_dword v40, s[16:17] sc1\n"
".Lch_pnx_34:\n"
"s_waitcnt vmcnt(7)\n"
"s_bitcmp1_b32 s37, 1\n"
"s_cbranch_scc1 .Lch_pdm_36\n"
"v_bitop3_b32 v38, v44, 1, v45 bitop3:0x80\n"
"v_cmp_ne_u32 vcc, 0, v38\n"
"s_nop 0\n"
"s_cmp_eq_u64 vcc, -1\n"
"s_cbranch_scc1 .Lch_pv_38\n"
"global_load_dwordx2 v[44:45], v36, s[16:17] offset:0 sc1\n"
"s_branch .Lch_pnx_37\n"
".Lch_pv_38:\n"
"s_bitset1_b32 s37, 1\n"
"s_cmp_eq_u32 s37, 255\n"
"s_cbranch_scc1 .Lch_pd_32\n"
".Lch_pdm_36:\n"
"global_load_lds_dword v40, s[16:17] sc1\n"
".Lch_pnx_37:\n"
"s_waitcnt vmcnt(7)\n"
"s_bitcmp1_b32 s37, 2\n"
"s_cbranch_scc1 .Lch_pdm_39\n"
"v_bitop3_b32 v38, v46, 1, v47 bitop3:0x80\n"
"v_cmp_ne_u32 vcc, 0, v38\n"
"s_nop 0\n"
"s_cmp_eq_u64 vcc, -1\n"
"s_cbranch_scc1 .Lch_pv_41\n"
"global_load_dwordx2 v[46:47], v36, s[16:17] offset:512 sc1\n"
"s_branch .Lch_pnx_40\n"
".Lch_pv_41:\n"
"s_bitset1_b32 s37, 2\n"
"s_cmp_eq_u32 s37, 255\n"
"s_cbranch_scc1 .Lch_pd_32\n"
".Lch_pdm_39:\n"
"global_load_lds_dword v40, s[16:17] sc1\n"
".Lch_pnx_40:\n"
"s_waitcnt vmcnt(7)\n"
"s_bitcmp1_b32 s37, 3\n"
"s_cbranch_scc1 .Lch_pdm_42\n"
"v_bitop3_b32 v38, v48, 1, v49 bitop3:0x80\n"
"v_cmp_ne_u32 vcc, 0, v38\n"
"s_nop 0\n"
"s_cmp_eq_u64 vcc, -1\n"
"s_cbranch_scc1 .Lch_pv_44\n"
"global_load_dwordx2 v[48:49], v36, s[16:17] offset:1024 sc1\n"
"s_branch .Lch_pnx_43\n"
".Lch_pv_44:\n"
"s_bitset1_b32 s37, 3\n"
"s_cmp_eq_u32 s37, 255\n"
"s_cbranch_scc1 .Lch_pd_32\n"
".Lch_pdm_42:\n"
"global_load_lds_dword v40, s[16:17] sc1\n"
".Lch_pnx_43:\n"
"s_waitcnt vmcnt(7)\n"
"s_bitcmp1_b32 s37, 4\n"
"s_cbranch_scc1 .Lch_pdm_45\n"
"v_bitop3_b32 v38, v50, 1, v51 bitop3:0x80\n"
"v_cmp_ne_u32 vcc, 0, v38\n"
"s_nop 0\n"
"s_cmp_eq_u64 vcc, -1\n"
"s_cbranch_scc1 .Lch_pv_47\n"
"global_load_dwordx2 v[50:51], v36, s[16:17] offset:1536 sc1\n"
"s_branch .Lch_pnx_46\n"
".Lch_pv_47:\n"
"s_bitset1_b32 s37, 4\n"
"s_cmp_eq_u32 s37, 255\n"
"s_cbranch_scc1 .Lch_pd_32\n"
".Lch_pdm_45:\n"
"global_load_lds_dword v40, s[16:17] sc1\n"
".Lch_pnx_46:\n"
"s_waitcnt vmcnt(7)\n"
"s_bitcmp1_b32 s37, 5\n"
"s_cbranch_scc1 .Lch_pdm_48\n"
"v_bitop3_b32 v38, v52, 1, v53 bitop3:0x80\n"
"v_cmp_ne_u32 vcc, 0, v38\n"
"s_nop 0\n"
"s_cmp_eq_u64 vcc, -1\n"
"s_cbranch_scc1 .Lch_pv_50\n"
"global_load_dwordx2 v[52:53], v36, s[16:17] offset:2048 sc1\n"
"s_branch .Lch_pnx_49\n"
".Lch_pv_50:\n"
"s_bitset1_b32 s37, 5\n"
"s_cmp_eq_u32 s37, 255\n"
"s_cbranch_scc1 .Lch_pd_32\n"
".Lch_pdm_48:\n"
"global_load_lds_dword v40, s[16:17] sc1\n"
".Lch_pnx_49:\n"
"s_waitcnt vmcnt(7)\n"
"s_bitcmp1_b32 s37, 6\n"
"s_cbranch_scc1 .Lch_pdm_51\n"
"v_bitop3_b32 v38, v54, 1, v55 bitop3:0x80\n"
"v_cmp_ne_u32 vcc, 0, v38\n"
"s_nop 0\n"
"s_cmp_eq_u64 vcc, -1\n"
"s_cbranch_scc1 .Lch_pv_53\n"
"global_load_dwordx2 v[54:55], v36, s[16:17] offset:2560 sc1\n"
"s_branch .Lch_pnx_52\n"
".Lch_pv_53:\n"
"s_bitset1_b32 s37, 6\n"
"s_cmp_eq_u32 s37, 255\n"
"s_cbranch_scc1 .Lch_pd_32\n"
".Lch_pdm_51:\n"
"global_load_lds_dword v40, s[16:17] sc1\n"
".Lch_pnx_52:\n"
"s_waitcnt vmcnt(7)\n"
"s_bitcmp1_b32 s37, 7\n"
"s_cbranch_scc1 .Lch_pdm_54\n"
"v_bitop3_b32 v38, v56, 1, v57 bitop3:0x80\n"
"v_cmp_ne_u32 vcc, 0, v38\n"
"s_nop 0\n"
"s_cmp_eq_u64 vcc, -1\n"
"s_cbranch_scc1 .Lch_pv_56\n"
"global_load_dwordx2 v[56:57], v36, s[16:17] offset:3072 sc1\n"
"s_branch .Lch_pnx_55\n"
".Lch_pv_56:\n"
"s_bitset1_b32 s37, 7\n"
"s_cmp_eq_u32 s37, 255\n"
"s_cbranch_scc1 .Lch_pd_32\n"
".Lch_pdm_54:\n"
"global_load_lds_dword v40, s[16:17] sc1\n"
".Lch_pnx_55:\n"
"s_add_u32 s36, s36, 1\n"
"s_cmp_lt_u32 s36, 2000000\n"
"s_cbranch_scc1 .Lch_pl_31\n"
"s_branch .Lch_abort\n"
".Lch_pd_32:\n"
"v_mov_b32 v39, 0xff800000\n"
"v_cndmask_b32_e64 v38, v58, v39, s[38:39]\n"
"s_nop 1\n"
"v_max_f32_dpp v38, v38, v38 quad_perm:[1,0,3,2] row_mask:0xf bank_mask:0xf\n"
"s_nop 1\n"
"v_max_f32_dpp v38, v38, v38 quad_perm:[2,3,0,1] row_mask:0xf bank_mask:0xf\n"
"s_nop 1\n"
"v_max_f32_dpp v38, v38, v38 row_half_mirror row_mask:0xf bank_mask:0xf\n"
"s_nop 1\n"
"v_max_f32_dpp v38, v38, v38 row_mirror row_mask:0xf bank_mask:0xf\n"
"v_sub_f32 v38, v58, v38\n"
"v_exp_f32 v38, v38\n"
"s_nop 1\n"
"v_mov_b32_dpp v39, v38 quad_perm:[0,0,2,2] row_mask:0xf bank_mask:0xf\n"
"v_mul_f32 v39, v39, v58\n"
"v_cndmask_b32_e64 v39, 0, v39, s[52:53]\n"
"s_nop 1\n"
"v_add_f32_dpp v39, v39, v39 quad_perm:[1,0,3,2] row_mask:0xf bank_mask:0xf\n"
"s_nop 1\n"
"v_add_f32_dpp v39, v39, v39 quad_perm:[2,3,0,1] row_mask:0xf bank_mask:0xf\n"
"s_nop 1\n"
"v_add_f32_dpp v39, v39, v39 row_half_mirror row_mask:0xf bank_mask:0xf\n"
"s_nop 1\n"
"v_add_f32_dpp v39, v39, v39 row_mirror row_mask:0xf bank_mask:0xf\n"
"v_rcp_f32 v39, v39\n"
"s_nop 0\n"
"v_mul_f32 v38, v38, v39\n"
"s_nop 0\n"
"v_readlane_b32 s54, v38, 0\n"
"v_readlane_b32 s55, v38, 2\n"
"v_readlane_b32 s56, v38, 4\n"
"v_readlane_b32 s57, v38, 6\n"
"v_readlane_b32 s58, v38, 8\n"
"v_readlane_b32 s59, v38, 10\n"
"v_readlane_b32 s60, v38, 12\n"
"v_mul_f32 v39, s54, v44\n"
"v_mul_f32 v40, s54, v45\n"
"v_fmac_f32 v39, s55, v46\n"
"v_fmac_f32 v40, s55, v47\n"
"v_fmac_f32 v39, s56, v48\n"
"v_fmac_f32 v40, s56, v49\n"
"v_fmac_f32 v39, s57, v50\n"
"v_fmac_f32 v40, s57, v51\n"
"v_fmac_f32 v39, s58, v52\n"
"v_fmac_f32 v40, s58, v53\n"
"v_fmac_f32 v39, s59, v54\n"
"v_fmac_f32 v40, s59, v55\n"
"v_fmac_f32 v39, s60, v56\n"
"v_fmac_f32 v40, s60, v57\n"
"v_cvt_pk_bf16_f32 v39, v39, v40\n"
"ds_write_b32 v2, v39 offset:0\n"
"s_waitcnt lgkmcnt(0)\n"
"ds_read_b64_tr_b16 v[44:45], v8 offset:0\n"
"ds_read_b64_tr_b16 v[46:47], v10 offset:0\n"
"ds_read_b64_tr_b16 v[48:49], v9 offset:0\n"
"ds_read_b64_tr_b16 v[50:51], v11 offset:0\n"
"ds_read_b64_tr_b16 v[52:53], v8 offset:8192\n"
"ds_read_b64_tr_b16 v[54:55], v10 offset:8192\n"
"ds_read_b64_tr_b16 v[56:57], v9 offset:8192\n"
"ds_read_b64_tr_b16 v[58:59], v11 offset:8192\n"
"ds_read_b64_tr_b16 v[60:61], v8 offset:16384\n"
"ds_read_b64_tr_b16 v[62:63], v10 offset:16384\n"
"ds_read_b64_tr_b16 v[64:65], v9 offset:16384\n"
"ds_read_b64_tr_b16 v[66:67], v11 offset:16384\n"
"ds_read_b64_tr_b16 v[68:69], v8 offset:24576\n"
"ds_read_b64_tr_b16 v[70:71], v10 offset:24576\n"
"ds_read_b64_tr_b16 v[72:73], v9 offset:24576\n"
"ds_read_b64_tr_b16 v[74:75], v11 offset:24576\n"
"s_barrier\n"
"ds_read_b128 v[32:35], v3 offset:0\n"
"ds_read_b128 v[36:39], v3 offset:64\n"
"ds_read_b128 v[40:43], v3 offset:128\n"
"s_waitcnt lgkmcnt(2)\n"
"v_mfma_f32_16x16x32_bf16 v[24:27], v[32:35], v[76:79], 0\n"
"v_mfma_f32_16x16x32_bf16 v[28:31], v[32:35], v[80:83], 0\n"
"ds_read_b128 v[32:35], v3 offset:192\n"
"ds_read_b128 v[76:79], v3 offset:256\n"
"ds_read_b128 v[80:83], v3 offset:320\n"
"s_waitcnt lgkmcnt(4)\n"
"v_mfma_f32_16x16x32_bf16 v[24:27], v[36:39], v[84:87], v[24:27]\n"
"v_mfma_f32_16x16x32_bf16 v[28:31], v[36:39], v[88:91], v[28:31]\n"
"ds_read_b128 v[36:39], v3 offset:384\n"
"ds_read_b128 v[84:87], v3 offset:448\n"
"ds_read_b128 v[88:91], v3 offset:512\n"
"s_waitcnt lgkmcnt(6)\n"
"v_mfma_f32_16x16x32_bf16 v[24:27], v[40:43], v[92:95], v[24:27]\n"
"v_mfma_f32_16x16x32_bf16 v[28:31], v[40:43], v[96:99], v[28:31]\n"
"ds_read_b128 v[40:43], v3 offset:576\n"
"ds_read_b128 v[92:95], v3 offset:640\n"
"ds_read_b128 v[96:99], v3 offset:704\n"
"s_waitcnt lgkmcnt(8)\n"
"v_mfma_f32_16x16x32_bf16 v[24:27], v[32:35], v[100:103], v[24:27]\n"
"v_mfma_f32_16x16x32_bf16 v[28:31], v[32:35], v[104:107], v[28:31]\n"
"ds_read_b64_tr_b16 v[32:33], v8 offset:32768\n"
"ds_read_b64_tr_b16 v[34:35], v10 offset:32768\n"
"ds_read_b64_tr_b16 v[100:101], v9 offset:32768\n"
"ds_read_b64_tr_b16 v[102:103], v11 offset:32768\n"
"ds_read_b128 v[104:107], v3 offset:768\n"
"s_waitcnt lgkmcnt(12)\n"
"v_mfma_f32_16x16x32_bf16 v[24:27], v[76:79], v[108:111], v[24:27]\n"
"v_mfma_f32_16x16x32_bf16 v[28:31], v[76:79], v[112:115], v[28:31]\n"
"ds_read_b64_tr_b16 v[76:77], v8 offset:40960\n"
"ds_read_b64_tr_b16 v[78:79], v10 offset:40960\n"
"ds_read_b64_tr_b16 v[108:109], v9 offset:40960\n"
"ds_read_b64_tr_b16 v[110:111], v11 offset:40960\n"
"ds_read_b128 v[112:115], v3 offset:832\n"
"s_waitcnt lgkmcnt(15)\n"
"v_mfma_f32_16x16x32_bf16 v[24:27], v[80:83], v[116:119], v[24:27]\n"
"v_mfma_f32_16x16x32_bf16 v[28:31], v[80:83], v[120:123], v[28:31]\n"
"ds_read_b64_tr_b16 v[80:81], v8 offset:49152\n"
"ds_read_b64_tr_b16 v[82:83], v10 offset:49152\n"
"ds_read_b64_tr_b16 v[116:117], v9 offset:49152\n"
"ds_read_b64_tr_b16 v[118:119], v11 offset:49152\n"
"ds_read_b128 v[120:123], v3 offset:896\n"
"s_waitcnt lgkmcnt(15)\n"
"v_mfma_f32_16x16x32_bf16 v[24:27], v[36:39], v[124:127], v[24:27]\n"
"v_mfma_f32_16x16x32_bf16 v[28:31], v[36:39], v[128:131], v[28:31]\n"
"ds_read_b64_tr_b16 v[36:37], v8 offset:57344\n"
"ds_read_b64_tr_b16 v[38:39], v10 offset:57344\n"
"ds_read_b64_tr_b16 v[124:125], v9 offset:57344\n"
"ds_read_b64_tr_b16 v[126:127], v11 offset:57344\n"
"ds_read_b128 v[128:131], v3 offset:960\n"
"s_waitcnt lgkmcnt(15)\n"
"v_mfma_f32_16x16x32_bf16 v[24:27], v[84:87], v[132:135], v[24:27]\n"
"v_mfma_f32_16x16x32_bf16 v[28:31], v[84:87], v[136:139], v[28:31]\n"
"s_waitcnt lgkmcnt(15)\n"
"s_waitcnt lgkmcnt(15)\n"
"v_mfma_f32_16x16x32_bf16 v[24:27], v[88:91], v[44:47], v[24:27]\n"
"v_mfma_f32_16x16x32_bf16 v[28:31], v[88:91], v[48:51], v[28:31]\n"
"s_waitcnt lgkmcnt(15)\n"
"s_waitcnt lgkmcnt(15)\n"
"v_mfma_f32_16x16x32_bf16 v[24:27], v[40:43], v[52:55], v[24:27]\n"
"v_mfma_f32_16x16x32_bf16 v[28:31], v[40:43], v[56:59], v[28:31]\n"
"s_waitcnt lgkmcnt(15)\n"
"s_waitcnt lgkmcnt(15)\n"
"v_mfma_f32_16x16x32_bf16 v[24:27], v[92:95], v[60:63], v[24:27]\n"
"v_mfma_f32_16x16x32_bf16 v[28:31], v[92:95], v[64:67], v[28:31]\n"
"s_waitcnt lgkmcnt(15)\n"
"s_waitcnt lgkmcnt(15)\n"
"v_mfma_f32_16x16x32_bf16 v[24:27], v[96:99], v[68:71], v[24:27]\n"
"v_mfma_f32_16x16x32_bf16 v[28:31], v[96:99], v[72:75], v[28:31]\n"
"s_waitcnt lgkmcnt(15)\n"
"s_waitcnt lgkmcnt(15)\n"
"v_mfma_f32_16x16x32_bf16 v[24:27], v[104:107], v[32:35], v[24:27]\n"
"v_mfma_f32_16x16x32_bf16 v[28:31], v[104:107], v[100:103], v[28:31]\n"
"s_waitcnt lgkmcnt(10)\n"
"s_waitcnt lgkmcnt(11)\n"
"v_mfma_f32_16x16x32_bf16 v[24:27], v[112:115], v[76:79], v[24:27]\n"
"v_mfma_f32_16x16x32_bf16 v[28:31], v[112:115], v[108:111], v[28:31]\n"
"s_waitcnt lgkmcnt(5)\n"
"s_waitcnt lgkmcnt(6)\n"
"v_mfma_f32_16x16x32_bf16 v[24:27], v[120:123], v[80:83], v[24:27]\n"
"v_mfma_f32_16x16x32_bf16 v[28:31], v[120:123], v[116:119], v[28:31]\n"
"s_waitcnt lgkmcnt(0)\n"
"s_waitcnt lgkmcnt(1)\n"
"v_mfma_f32_16x16x32_bf16 v[24:27], v[128:131], v[36:39], v[24:27]\n"
"v_mfma_f32_16x16x32_bf16 v[28:31], v[128:131], v[124:127], v[28:31]\n"
"s_cmp_lt_u32 s32, 2\n"
"s_cselect_b32 s34, 0, 2228224\n"
"s_lshl_b32 s33, s32, 16\n"
"s_add_u32 s33, s33, 262400\n"
"s_add_u32 s33, s33, s34\n"
"v_add_u32 v38, s33, v12\n"
"s_nop 0\n"
"v_cndmask_b32_e64 v36, v24, v25, s[40:41]\n"
"v_cndmask_b32_e64 v37, v28, v29, s[40:41]\n"
"v_cndmask_b32_e64 v36, v36, v37, s[42:43]\n"
"v_or_b32 v36, 1, v36\n"
"global_store_dword v38, v36, s[16:17] sc1\n"
"s_cmp_eq_u32 s62, 0\n"
"s_cselect_b32 s34, 1, s77\n"
"s_cmp_eq_u32 s34, 0\n"
"s_cbranch_scc1 .Lch_clr_58\n"
"s_sleep 10\n"
"v_add_u32 v36, s33, v13\n"
"v_add_u32 v37, s33, v14\n"
"v_mov_b32 v39, 262144\n"
"s_mov_b32 m0, 143360\n"
"s_mov_b32 s36, 0\n"
"s_mov_b32 s37, 0\n"
"global_load_dwordx4 v[44:47], v36, s[16:17] offset:-4096 sc1\n"
"global_load_dwordx4 v[48:51], v36, s[16:17] offset:-2048 sc1\n"
"global_load_dwordx4 v[52:55], v36, s[16:17] offset:0 sc1\n"
"global_load_dwordx4 v[56:59], v36, s[16:17] offset:2048 sc1\n"
"global_load_dwordx4 v[60:63], v37, s[16:17] offset:-4096 sc1\n"
"global_load_dwordx4 v[64:67], v37, s[16:17] offset:-2048 sc1\n"
"global_load_dwordx4 v[68:71], v37, s[16:17] offset:0 sc1\n"
"global_load_dwordx4 v[72:75], v37, s[16:17] offset:2048 sc1\n"
".Lch_pl_59:\n"
"s_waitcnt vmcnt(7)\n"
"s_bitcmp1_b32 s37, 0\n"
"s_cbranch_scc1 .Lch_pdm_61\n"
"v_bitop3_b32 v38, v44, v45, v46 bitop3:0x80\n"
"v_bitop3_b32 v38, v38, 1, v47 bitop3:0x80\n"
"v_cmp_ne_u32 vcc, 0, v38\n"
"s_nop 0\n"
"s_cmp_eq_u64 vcc, -1\n"
"s_cbranch_scc1 .Lch_pv_63\n"
"global_load_dwordx4 v[44:47], v36, s[16:17] offset:-4096 sc1\n"
"s_branch .Lch_pnx_62\n"
".Lch_pv_63:\n"
"s_bitset1_b32 s37, 0\n"
"s_cmp_eq_u32 s37, 255\n"
"s_cbranch_scc1 .Lch_pd_60\n"
".Lch_pdm_61:\n"
"global_load_lds_dword v39, s[16:17] sc1\n"
".Lch_pnx_62:\n"
"s_waitcnt vmcnt(7)\n"
"s_bitcmp1_b32 s37, 1\n"
"s_cbranch_scc1 .Lch_pdm_64\n"
"v_bitop3_b32 v38, v48, v49, v50 bitop3:0x80\n"
"v_bitop3_b32 v38, v38, 1, v51 bitop3:0x80\n"
"v_cmp_ne_u32 vcc, 0, v38\n"
"s_nop 0\n"
"s_cmp_eq_u64 vcc, -1\n"
"s_cbranch_scc1 .Lch_pv_66\n"
"global_load_dwordx4 v[48:51], v36, s[16:17] offset:-2048 sc1\n"
"s_branch .Lch_pnx_65\n"
".Lch_pv_66:\n"
"s_bitset1_b32 s37, 1\n"
"s_cmp_eq_u32 s37, 255\n"
"s_cbranch_scc1 .Lch_pd_60\n"
".Lch_pdm_64:\n"
"global_load_lds_dword v39, s[16:17] sc1\n"
".Lch_pnx_65:\n"
"s_waitcnt vmcnt(7)\n"
"s_bitcmp1_b32 s37, 2\n"
"s_cbranch_scc1 .Lch_pdm_67\n"
"v_bitop3_b32 v38, v52, v53, v54 bitop3:0x80\n"
"v_bitop3_b32 v38, v38, 1, v55 bitop3:0x80\n"
"v_cmp_ne_u32 vcc, 0, v38\n"
"s_nop 0\n"
"s_cmp_eq_u64 vcc, -1\n"
"s_cbranch_scc1 .Lch_pv_69\n"
"global_load_dwordx4 v[52:55], v36, s[16:17] offset:0 sc1\n"
"s_branch .Lch_pnx_68\n"
".Lch_pv_69:\n"
"s_bitset1_b32 s37, 2\n"
"s_cmp_eq_u32 s37, 255\n"
"s_cbranch_scc1 .Lch_pd_60\n"
".Lch_pdm_67:\n"
"global_load_lds_dword v39, s[16:17] sc1\n"
".Lch_pnx_68:\n"
"s_waitcnt vmcnt(7)\n"
"s_bitcmp1_b32 s37, 3\n"
"s_cbranch_scc1 .Lch_pdm_70\n"
"v_bitop3_b32 v38, v56, v57, v58 bitop3:0x80\n"
"v_bitop3_b32 v38, v38, 1, v59 bitop3:0x80\n"
"v_cmp_ne_u32 vcc, 0, v38\n"
"s_nop 0\n"
"s_cmp_eq_u64 vcc, -1\n"
"s_cbranch_scc1 .Lch_pv_72\n"
"global_load_dwordx4 v[56:59], v36, s[16:17] offset:2048 sc1\n"
"s_branch .Lch_pnx_71\n"
".Lch_pv_72:\n"
"s_bitset1_b32 s37, 3\n"
"s_cmp_eq_u32 s37, 255\n"
"s_cbranch_scc1 .Lch_pd_60\n"
".Lch_pdm_70:\n"
"global_load_lds_dword v39, s[16:17] sc1\n"
".Lch_pnx_71:\n"
"s_waitcnt vmcnt(7)\n"
"s_bitcmp1_b32 s37, 4\n"
"s_cbranch_scc1 .Lch_pdm_73\n"
"v_bitop3_b32 v38, v60, v61, v62 bitop3:0x80\n"
"v_bitop3_b32 v38, v38, 1, v63 bitop3:0x80\n"
"v_cmp_ne_u32 vcc, 0, v38\n"
"s_nop 0\n"
"s_cmp_eq_u64 vcc, -1\n"
"s_cbranch_scc1 .Lch_pv_75\n"
"global_load_dwordx4 v[60:63], v37, s[16:17] offset:-4096 sc1\n"
"s_branch .Lch_pnx_74\n"
".Lch_pv_75:\n"
"s_bitset1_b32 s37, 4\n"
"s_cmp_eq_u32 s37, 255\n"
"s_cbranch_scc1 .Lch_pd_60\n"
".Lch_pdm_73:\n"
"global_load_lds_dword v39, s[16:17] sc1\n"
".Lch_pnx_74:\n"
"s_waitcnt vmcnt(7)\n"
"s_bitcmp1_b32 s37, 5\n"
"s_cbranch_scc1 .Lch_pdm_76\n"
"v_bitop3_b32 v38, v64, v65, v66 bitop3:0x80\n"
"v_bitop3_b32 v38, v38, 1, v67 bitop3:0x80\n"
"v_cmp_ne_u32 vcc, 0, v38\n"
"s_nop 0\n"
"s_cmp_eq_u64 vcc, -1\n"
"s_cbranch_scc1 .Lch_pv_78\n"
"global_load_dwordx4 v[64:67], v37, s[16:17] offset:-2048 sc1\n"
"s_branch .Lch_pnx_77\n"
".Lch_pv_78:\n"
"s_bitset1_b32 s37, 5\n"
"s_cmp_eq_u32 s37, 255\n"
"s_cbranch_scc1 .Lch_pd_60\n"
".Lch_pdm_76:\n"
"global_load_lds_dword v39, s[16:17] sc1\n"
".Lch_pnx_77:\n"
"s_waitcnt vmcnt(7)\n"
"s_bitcmp1_b32 s37, 6\n"
"s_cbranch_scc1 .Lch_pdm_79\n"
"v_bitop3_b32 v38, v68, v69, v70 bitop3:0x80\n"
"v_bitop3_b32 v38, v38, 1, v71 bitop3:0x80\n"
"v_cmp_ne_u32 vcc, 0, v38\n"
"s_nop 0\n"
"s_cmp_eq_u64 vcc, -1\n"
"s_cbranch_scc1 .Lch_pv_81\n"
"global_load_dwordx4 v[68:71], v37, s[16:17] offset:0 sc1\n"
"s_branch .Lch_pnx_80\n"
".Lch_pv_81:\n"
"s_bitset1_b32 s37, 6\n"
"s_cmp_eq_u32 s37, 255\n"
"s_cbranch_scc1 .Lch_pd_60\n"
".Lch_pdm_79:\n"
"global_load_lds_dword v39, s[16:17] sc1\n"
".Lch_pnx_80:\n"
"s_waitcnt vmcnt(7)\n"
"s_bitcmp1_b32 s37, 7\n"
"s_cbranch_scc1 .Lch_pdm_82\n"
"v_bitop3_b32 v38, v72, v73, v74 bitop3:0x80\n"
"v_bitop3_b32 v38, v38, 1, v75 bitop3:0x80\n"
"v_cmp_ne_u32 vcc, 0, v38\n"
"s_nop 0\n"
"s_cmp_eq_u64 vcc, -1\n"
"s_cbranch_scc1 .Lch_pv_84\n"
"global_load_dwordx4 v[72:75], v37, s[16:17] offset:2048 sc1\n"
"s_branch .Lch_pnx_83\n"
".Lch_pv_84:\n"
"s_bitset1_b32 s37, 7\n"
"s_cmp_eq_u32 s37, 255\n"
"s_cbranch_scc1 .Lch_pd_60\n"
".Lch_pdm_82:\n"
"global_load_lds_dword v39, s[16:17] sc1\n"
".Lch_pnx_83:\n"
"s_add_u32 s36, s36, 1\n"
"s_cmp_lt_u32 s36, 2000000\n"
"s_cbranch_scc1 .Lch_pl_59\n"
"s_branch .Lch_abort\n"
".Lch_pd_60:\n"
"v_pk_add_f32 v[44:45], v[44:45], v[48:49]\n"
"v_pk_add_f32 v[52:53], v[52:53], v[56:57]\n"
"v_pk_add_f32 v[60:61], v[60:61], v[64:65]\n"
"v_pk_add_f32 v[68:69], v[68:69], v[72:73]\n"
"v_pk_add_f32 v[46:47], v[46:47], v[50:51]\n"
"v_pk_add_f32 v[54:55], v[54:55], v[58:59]\n"
"v_pk_add_f32 v[62:63], v[62:63], v[66:67]\n"
"v_pk_add_f32 v[70:71], v[70:71], v[74:75]\n"
"v_pk_add_f32 v[44:45], v[44:45], v[52:53]\n"
"v_pk_add_f32 v[60:61], v[60:61], v[68:69]\n"
"v_pk_add_f32 v[46:47], v[46:47], v[54:55]\n"
"v_pk_add_f32 v[62:63], v[62:63], v[70:71]\n"
"v_pk_add_f32 v[76:77], v[44:45], v[60:61]\n"
"v_pk_add_f32 v[78:79], v[46:47], v[62:63]\n"
"v_cvt_pk_bf16_f32 v38, v76, v77\n"
"v_cvt_pk_bf16_f32 v39, v78, v79\n"
"v_lshlrev_b32 v76, 16, v38\n"
"v_and_b32 v77, 0xffff0000, v38\n"
"v_lshlrev_b32 v78, 16, v39\n"
"v_and_b32 v79, 0xffff0000, v39\n"
"v_add_f32 v76, v20, v76\n"
"v_add_f32 v77, v21, v77\n"
"v_add_f32 v78, v22, v78\n"
"v_add_f32 v79, v23, v79\n"
"v_cvt_pk_bf16_f32 v38, v76, v77\n"
"v_cvt_pk_bf16_f32 v39, v78, v79\n"
"v_lshlrev_b32 v20, 16, v38\n"
"v_and_b32 v21, 0xffff0000, v38\n"
"v_lshlrev_b32 v22, 16, v39\n"
"v_and_b32 v23, 0xffff0000, v39\n"
"ds_write_b64 v15, v[38:39]\n"
"s_branch .Lch_x1skip_57\n"
".Lch_clr_58:\n"
"s_cmp_eq_u32 s32, 0\n"
"s_cbranch_scc0 .Lch_x1skip_57\n"
"v_mov_b32 v44, 0\n"
"v_mov_b32 v45, 0\n"
"v_mov_b32 v46, 0\n"
"v_mov_b32 v47, 0\n"
"v_lshlrev_b32 v36, 1, v0\n"
"s_mov_b32 s34, s69\n"
".Lch_z_85:\n"
"s_cmp_lt_u32 s34, 96\n"
"s_cselect_b32 s35, 0, 786688\n"
"s_lshl_b32 s36, s34, 10\n"
"s_add_u32 s35, s35, s36\n"
"s_add_u32 s70, s16, s35\n"
"s_addc_u32 s71, s17, 0\n"
"global_store_dwordx4 v36, v[44:47], s[70:71] sc1\n"
"s_add_u32 s34, s34, 64\n"
"s_cmp_lt_u32 s34, 2240\n"
"s_cbranch_scc1 .Lch_z_85\n"
"s_nop 3\n"
".Lch_x1skip_57:\n"
"s_waitcnt lgkmcnt(0)\n"
"s_barrier\n"
"ds_read_b128 v[108:111], v19 offset:0\n"
"ds_read_b128 v[112:115], v19 offset:1024\n"
"ds_read_b128 v[44:47], v3 offset:2048\n"
"ds_read_b128 v[48:51], v3 offset:2112\n"
"ds_read_b128 v[52:55], v3 offset:2176\n"
"ds_read_b128 v[56:59], v3 offset:2240\n"
"ds_read_b128 v[60:63], v3 offset:2304\n"
"ds_read_b128 v[64:67], v3 offset:2368\n"
"ds_read_b128 v[68:71], v3 offset:2432\n"
"ds_read_b128 v[72:75], v3 offset:2496\n"
"ds_read_b128 v[76:79], v3 offset:2560\n"
"ds_read_b128 v[80:83], v3 offset:2624\n"
"ds_read_b128 v[84:87], v3 offset:2688\n"
"ds_read_b128 v[88:91], v3 offset:2752\n"
"ds_read_b128 v[92:95], v3 offset:2816\n"
"ds_read_b128 v[96:99], v3 offset:2880\n"
"ds_read_b128 v[100:103], v3 offset:2944\n"
"ds_read_b128 v[104:107], v3 offset:3008\n"
"s_waitcnt lgkmcnt(15)\n"
"s_waitcnt lgkmcnt(15)\n"
"v_mfma_f32_16x16x32_bf16 v[24:27], v[44:47], a[128:131], 0\n"
"v_lshlrev_b32 v116, 16, v108\n"
"v_lshlrev_b32 v128, 16, v112\n"
"v_and_b32 v117, 0xffff0000, v108\n"
"v_mfma_f32_16x16x32_bf16 v[28:31], v[44:47], a[132:135], 0\n"
"v_and_b32 v129, 0xffff0000, v112\n"
"v_lshlrev_b32 v118, 16, v109\n"
"v_lshlrev_b32 v130, 16, v113\n"
"v_mfma_f32_16x16x32_bf16 v[32:35], v[44:47], a[136:139], 0\n"
"v_and_b32 v119, 0xffff0000, v109\n"
"v_and_b32 v131, 0xffff0000, v113\n"
"v_lshlrev_b32 v120, 16, v110\n"
"s_waitcnt lgkmcnt(14)\n"
"v_mfma_f32_16x16x32_bf16 v[24:27], v[48:51], a[140:143], v[24:27]\n"
"v_lshlrev_b32 v132, 16, v114\n"
"v_and_b32 v121, 0xffff0000, v110\n"
"v_and_b32 v133, 0xffff0000, v114\n"
"v_mfma_f32_16x16x32_bf16 v[28:31], v[48:51], a[144:147], v[28:31]\n"
"v_lshlrev_b32 v122, 16, v111\n"
"v_lshlrev_b32 v134, 16, v115\n"
"v_and_b32 v123, 0xffff0000, v111\n"
"v_mfma_f32_16x16x32_bf16 v[32:35], v[48:51], a[148:151], v[32:35]\n"
"v_and_b32 v135, 0xffff0000, v115\n"
"v_add_f32 v36, v116, v117\n"
"v_add_f32 v38, v128, v129\n"
"s_waitcnt lgkmcnt(13)\n"
"v_mfma_f32_16x16x32_bf16 v[24:27], v[52:55], a[152:155], v[24:27]\n"
"v_add_f32 v124, v118, v119\n"
"v_add_f32 v136, v130, v131\n"
"v_add_f32 v36, v36, v124\n"
"v_mfma_f32_16x16x32_bf16 v[28:31], v[52:55], a[156:159], v[28:31]\n"
"v_add_f32 v38, v38, v136\n"
"v_add_f32 v124, v120, v121\n"
"v_add_f32 v136, v132, v133\n"
"v_mfma_f32_16x16x32_bf16 v[32:35], v[52:55], a[160:163], v[32:35]\n"
"v_add_f32 v36, v36, v124\n"
"v_add_f32 v38, v38, v136\n"
"v_add_f32 v124, v122, v123\n"
"s_waitcnt lgkmcnt(12)\n"
"v_mfma_f32_16x16x32_bf16 v[24:27], v[56:59], a[164:167], v[24:27]\n"
"v_add_f32 v136, v134, v135\n"
"v_add_f32 v36, v36, v124\n"
"v_add_f32 v38, v38, v136\n"
"v_mfma_f32_16x16x32_bf16 v[28:31], v[56:59], a[168:171], v[28:31]\n"
"v_add_f32_dpp v36, v36, v36 quad_perm:[1,0,3,2] row_mask:0xf bank_mask:0xf\n"
"v_add_f32_dpp v38, v38, v38 quad_perm:[1,0,3,2] row_mask:0xf bank_mask:0xf\n"
"s_nop 0\n"
"v_add_f32_dpp v36, v36, v36 quad_perm:[2,3,0,1] row_mask:0xf bank_mask:0xf\n"
"v_mfma_f32_16x16x32_bf16 v[32:35], v[56:59], a[172:175], v[32:35]\n"
"v_add_f32_dpp v38, v38, v38 quad_perm:[2,3,0,1] row_mask:0xf bank_mask:0xf\n"
"v_add_f32_dpp v36, v36, v36 row_half_mirror row_mask:0xf bank_mask:0xf\n"
"s_nop 0\n"
"v_add_f32_dpp v38, v38, v38 row_half_mirror row_mask:0xf bank_mask:0xf\n"
"s_waitcnt lgkmcnt(11)\n"
"v_mfma_f32_16x16x32_bf16 v[24:27], v[60:63], a[176:179], v[24:27]\n"
"v_add_f32_dpp v36, v36, v36 row_mirror row_mask:0xf bank_mask:0xf\n"
"v_add_f32_dpp v38, v38, v38 row_mirror row_mask:0xf bank_mask:0xf\n"
"v_mov_b32 v124, v36\n"
"v_mfma_f32_16x16x32_bf16 v[28:31], v[60:63], a[180:183], v[28:31]\n"
"v_mov_b32 v136, v38\n"
"v_permlane16_swap_b32 v124, v36\n"
"s_nop 0\n"
"v_permlane16_swap_b32 v136, v38\n"
"v_mfma_f32_16x16x32_bf16 v[32:35], v[60:63], a[184:187], v[32:35]\n"
"v_add_f32 v36, v36, v124\n"
"v_add_f32 v38, v38, v136\n"
"v_mov_b32 v124, v36\n"
"s_waitcnt lgkmcnt(10)\n"
"v_mfma_f32_16x16x32_bf16 v[24:27], v[64:67], a[188:191], v[24:27]\n"
"v_mov_b32 v136, v38\n"
"v_permlane32_swap_b32 v124, v36\n"
"s_nop 0\n"
"v_permlane32_swap_b32 v136, v38\n"
"v_mfma_f32_16x16x32_bf16 v[28:31], v[64:67], a[192:195], v[28:31]\n"
"v_add_f32 v36, v36, v124\n"
"v_add_f32 v38, v38, v136\n"
"v_mul_f32 v36, 0x3b000000, v36\n"
"v_mfma_f32_16x16x32_bf16 v[32:35], v[64:67], a[196:199], v[32:35]\n"
"v_mul_f32 v38, 0x3b000000, v38\n"
"v_sub_f32 v116, v116, v36\n"
"v_sub_f32 v128, v128, v38\n"
"s_waitcnt lgkmcnt(9)\n"
"v_mfma_f32_16x16x32_bf16 v[24:27], v[68:71], a[200:203], v[24:27]\n"
"v_sub_f32 v117, v117, v36\n"
"v_sub_f32 v129, v129, v38\n"
"v_sub_f32 v118, v118, v36\n"
"v_mfma_f32_16x16x32_bf16 v[28:31], v[68:71], a[204:207], v[28:31]\n"
"v_sub_f32 v130, v130, v38\n"
"v_sub_f32 v119, v119, v36\n"
"v_sub_f32 v131, v131, v38\n"
"v_mfma_f32_16x16x32_bf16 v[32:35], v[68:71], a[208:211], v[32:35]\n"
"v_sub_f32 v120, v120, v36\n"
"v_sub_f32 v132, v132, v38\n"
"v_sub_f32 v121, v121, v36\n"
"s_waitcnt lgkmcnt(8)\n"
"v_mfma_f32_16x16x32_bf16 v[24:27], v[72:75], a[212:215], v[24:27]\n"
"v_sub_f32 v133, v133, v38\n"
"v_sub_f32 v122, v122, v36\n"
"v_sub_f32 v134, v134, v38\n"
"v_mfma_f32_16x16x32_bf16 v[28:31], v[72:75], a[216:219], v[28:31]\n"
"v_sub_f32 v123, v123, v36\n"
"v_sub_f32 v135, v135, v38\n"
"v_mul_f32 v37, v116, v116\n"
"v_mfma_f32_16x16x32_bf16 v[32:35], v[72:75], a[220:223], v[32:35]\n"
"v_mul_f32 v39, v128, v128\n"
"v_fmac_f32 v37, v117, v117\n"
"v_fmac_f32 v39, v129, v129\n"
"s_waitcnt lgkmcnt(7)\n"
"v_mfma_f32_16x16x32_bf16 v[24:27], v[76:79], a[224:227], v[24:27]\n"
"v_fmac_f32 v37, v118, v118\n"
"v_fmac_f32 v39, v130, v130\n"
"v_fmac_f32 v37, v119, v119\n"
"v_mfma_f32_16x16x32_bf16 v[28:31], v[76:79], a[228:231], v[28:31]\n"
"v_fmac_f32 v39, v131, v131\n"
"v_fmac_f32 v37, v120, v120\n"
"v_fmac_f32 v39, v132, v132\n"
"v_mfma_f32_16x16x32_bf16 v[32:35], v[76:79], a[232:235], v[32:35]\n"
"v_fmac_f32 v37, v121, v121\n"
"v_fmac_f32 v39, v133, v133\n"
"v_fmac_f32 v37, v122, v122\n"
"s_waitcnt lgkmcnt(6)\n"
"v_mfma_f32_16x16x32_bf16 v[24:27], v[80:83], a[236:239], v[24:27]\n"
"v_fmac_f32 v39, v134, v134\n"
"v_fmac_f32 v37, v123, v123\n"
"v_fmac_f32 v39, v135, v135\n"
"v_mfma_f32_16x16x32_bf16 v[28:31], v[80:83], a[240:243], v[28:31]\n"
"v_add_f32_dpp v37, v37, v37 quad_perm:[1,0,3,2] row_mask:0xf bank_mask:0xf\n"
"v_add_f32_dpp v39, v39, v39 quad_perm:[1,0,3,2] row_mask:0xf bank_mask:0xf\n"
"s_nop 0\n"
"v_add_f32_dpp v37, v37, v37 quad_perm:[2,3,0,1] row_mask:0xf bank_mask:0xf\n"
"v_mfma_f32_16x16x32_bf16 v[32:35], v[80:83], a[244:247], v[32:35]\n"
"v_add_f32_dpp v39, v39, v39 quad_perm:[2,3,0,1] row_mask:0xf bank_mask:0xf\n"
"v_add_f32_dpp v37, v37, v37 row_half_mirror row_mask:0xf bank_mask:0xf\n"
"s_nop 0\n"
"v_add_f32_dpp v39, v39, v39 row_half_mirror row_mask:0xf bank_mask:0xf\n"
"s_waitcnt lgkmcnt(5)\n"
"v_mfma_f32_16x16x32_bf16 v[24:27], v[84:87], a[248:251], v[24:27]\n"
"v_add_f32_dpp v37, v37, v37 row_mirror row_mask:0xf bank_mask:0xf\n"
"v_add_f32_dpp v39, v39, v39 row_mirror row_mask:0xf bank_mask:0xf\n"
"v_mov_b32 v124, v37\n"
"v_mfma_f32_16x16x32_bf16 v[28:31], v[84:87], a[252:255], v[28:31]\n"
"v_mov_b32 v136, v39\n"
"v_permlane16_swap_b32 v124, v37\n"
"s_nop 0\n"
"v_permlane16_swap_b32 v136, v39\n"
"v_mfma_f32_16x16x32_bf16 v[32:35], v[84:87], v[144:147], v[32:35]\n"
"v_add_f32 v37, v37, v124\n"
"v_add_f32 v39, v39, v136\n"
"v_mov_b32 v124, v37\n"
"s_waitcnt lgkmcnt(4)\n"
"v_mfma_f32_16x16x32_bf16 v[24:27], v[88:91], v[148:151], v[24:27]\n"
"v_mov_b32 v136, v39\n"
"v_permlane32_swap_b32 v124, v37\n"
"s_nop 0\n"
"v_permlane32_swap_b32 v136, v39\n"
"v_mfma_f32_16x16x32_bf16 v[28:31], v[88:91], v[152:155], v[28:31]\n"
"v_add_f32 v37, v37, v124\n"
"v_add_f32 v39, v39, v136\n"
"v_mov_b32 v124, 0x3727c5ac\n"
"v_mfma_f32_16x16x32_bf16 v[32:35], v[88:91], v[156:159], v[32:35]\n"
"v_mov_b32 v136, 0x3727c5ac\n"
"v_fmamk_f32 v37, v37, 0x3b000000, v124\n"
"v_fmamk_f32 v39, v39, 0x3b000000, v136\n"
"s_waitcnt lgkmcnt(3)\n"
"v_mfma_f32_16x16x32_bf16 v[24:27], v[92:95], v[160:163], v[24:27]\n"
"v_rsq_f32 v37, v37\n"
"v_rsq_f32 v39, v39\n"
"v_mfma_f32_16x16x32_bf16 v[28:31], v[92:95], v[164:167], v[28:31]\n"
"v_mfma_f32_16x16x32_bf16 v[32:35], v[92:95], v[168:171], v[32:35]\n"
"s_waitcnt lgkmcnt(2)\n"
"v_mfma_f32_16x16x32_bf16 v[24:27], v[96:99], v[172:175], v[24:27]\n"
"v_mfma_f32_16x16x32_bf16 v[28:31], v[96:99], v[176:179], v[28:31]\n"
"v_mfma_f32_16x16x32_bf16 v[32:35], v[96:99], v[180:183], v[32:35]\n"
"s_waitcnt lgkmcnt(1)\n"
"v_mfma_f32_16x16x32_bf16 v[24:27], v[100:103], v[184:187], v[24:27]\n"
"v_mfma_f32_16x16x32_bf16 v[28:31], v[100:103], v[188:191], v[28:31]\n"
"v_mfma_f32_16x16x32_bf16 v[32:35], v[100:103], v[192:195], v[32:35]\n"
"s_waitcnt lgkmcnt(0)\n"
"v_mfma_f32_16x16x32_bf16 v[24:27], v[104:107], v[196:199], v[24:27]\n"
"v_mfma_f32_16x16x32_bf16 v[28:31], v[104:107], v[200:203], v[28:31]\n"
"v_mfma_f32_16x16x32_bf16 v[32:35], v[104:107], v[204:207], v[32:35]\n"
"s_nop 6\n"
"v_cndmask_b32_e64 v40, v24, v28, s[44:45]\n"
"v_cndmask_b32_e64 v40, v40, v32, s[42:43]\n"
"v_cndmask_b32_e64 v41, v25, v29, s[44:45]\n"
"v_cndmask_b32_e64 v41, v41, v33, s[42:43]\n"
"v_fma_f32 v40, -v36, v140, v40\n"
"v_fma_f32 v41, -v38, v140, v41\n"
"v_mul_f32 v40, v40, v37\n"
"v_mul_f32 v41, v41, v39\n"
"v_cvt_pk_bf16_f32 v36, v40, v41\n"
"v_lshlrev_b32 v40, 16, v36\n"
"v_and_b32 v41, 0xffff0000, v36\n"
"v_mul_f32 v36, 0xbfb8aa3b, v40\n"
"v_mul_f32 v37, 0xbfb8aa3b, v41\n"
"v_exp_f32 v36, v36\n"
"v_exp_f32 v37, v37\n"
"v_add_f32 v36, 1.0, v36\n"
"v_add_f32 v37, 1.0, v37\n"
"v_rcp_f32 v36, v36\n"
"v_rcp_f32 v37, v37\n"
"v_mul_f32 v36, v36, v40\n"
"v_mul_f32 v37, v37, v41\n"
"v_cvt_pk_bf16_f32 v36, v36, v37\n"
"v_cndmask_b32_e64 v36, 0, v36, s[50:51]\n"
"s_mov_b64 exec, s[46:47]\n"
"ds_write_b16 v17, v36\n"
"ds_write_b16_d16_hi v17, v36 offset:1024\n"
"s_mov_b64 exec, -1\n"
"s_waitcnt lgkmcnt(0)\n"
"s_barrier\n"
"ds_read_b128 v[44:47], v3 offset:6144\n"
"ds_read_b128 v[48:51], v3 offset:6208\n"
"ds_read_b128 v[52:55], v3 offset:6272\n"
"ds_read_b128 v[56:59], v3 offset:6336\n"
"ds_read_b128 v[60:63], v3 offset:6400\n"
"ds_read_b128 v[64:67], v3 offset:6464\n"
"s_waitcnt lgkmcnt(5)\n"
"v_mfma_f32_16x16x32_bf16 v[24:27], v[44:47], v[208:211], 0\n"
"v_mfma_f32_16x16x32_bf16 v[28:31], v[44:47], v[212:215], 0\n"
"s_waitcnt lgkmcnt(4)\n"
"v_mfma_f32_16x16x32_bf16 v[24:27], v[48:51], v[216:219], v[24:27]\n"
"v_mfma_f32_16x16x32_bf16 v[28:31], v[48:51], v[220:223], v[28:31]\n"
"s_waitcnt lgkmcnt(3)\n"
"v_mfma_f32_16x16x32_bf16 v[24:27], v[52:55], v[224:227], v[24:27]\n"
"v_mfma_f32_16x16x32_bf16 v[28:31], v[52:55], v[228:231], v[28:31]\n"
"s_waitcnt lgkmcnt(2)\n"
"v_mfma_f32_16x16x32_bf16 v[24:27], v[56:59], v[232:235], v[24:27]\n"
"v_mfma_f32_16x16x32_bf16 v[28:31], v[56:59], v[236:239], v[28:31]\n"
"s_waitcnt lgkmcnt(1)\n"
"v_mfma_f32_16x16x32_bf16 v[24:27], v[60:63], v[240:243], v[24:27]\n"
"v_mfma_f32_16x16x32_bf16 v[28:31], v[60:63], v[244:247], v[28:31]\n"
"s_waitcnt lgkmcnt(0)\n"
"v_mfma_f32_16x16x32_bf16 v[24:27], v[64:67], v[248:251], v[24:27]\n"
"v_mfma_f32_16x16x32_bf16 v[28:31], v[64:67], v[252:255], v[28:31]\n"
"s_cmp_lt_u32 s32, 1\n"
"s_cselect_b32 s34, 0, 2228224\n"
"s_lshl_b32 s33, s32, 16\n"
"s_add_u32 s33, s33, 295168\n"
"s_add_u32 s33, s33, s34\n"
"v_add_u32 v38, s33, v12\n"
"s_nop 0\n"
"v_cndmask_b32_e64 v36, v24, v25, s[40:41]\n"
"v_cndmask_b32_e64 v37, v28, v29, s[40:41]\n"
"v_cndmask_b32_e64 v36, v36, v37, s[42:43]\n"
"v_or_b32 v36, 1, v36\n"
"global_store_dword v38, v36, s[16:17] sc1\n"
"s_cmp_eq_u32 s76, 0\n"
"s_cbranch_scc1 .Lch_noq_86\n"
"s_cmp_eq_u32 s62, 0\n"
"s_cbranch_scc0 .Lch_x2skip_87\n"
"s_sleep 10\n"
"v_add_u32 v36, s33, v13\n"
"v_add_u32 v37, s33, v14\n"
"v_mov_b32 v39, 262144\n"
"s_mov_b32 m0, 143360\n"
"s_mov_b32 s36, 0\n"
"s_mov_b32 s37, 0\n"
"global_load_dwordx4 v[44:47], v36, s[16:17] offset:-4096 sc1\n"
"global_load_dwordx4 v[48:51], v36, s[16:17] offset:-2048 sc1\n"
"global_load_dwordx4 v[52:55], v36, s[16:17] offset:0 sc1\n"
"global_load_dwordx4 v[56:59], v36, s[16:17] offset:2048 sc1\n"
"global_load_dwordx4 v[60:63], v37, s[16:17] offset:-4096 sc1\n"
"global_load_dwordx4 v[64:67], v37, s[16:17] offset:-2048 sc1\n"
"global_load_dwordx4 v[68:71], v37, s[16:17] offset:0 sc1\n"
"global_load_dwordx4 v[72:75], v37, s[16:17] offset:2048 sc1\n"
".Lch_pl_88:\n"
"s_waitcnt vmcnt(7)\n"
"s_bitcmp1_b32 s37, 0\n"
"s_cbranch_scc1 .Lch_pdm_90\n"
"v_bitop3_b32 v38, v44, v45, v46 bitop3:0x80\n"
"v_bitop3_b32 v38, v38, 1, v47 bitop3:0x80\n"
"v_cmp_ne_u32 vcc, 0, v38\n"
"s_nop 0\n"
"s_cmp_eq_u64 vcc, -1\n"
"s_cbranch_scc1 .Lch_pv_92\n"
"global_load_dwordx4 v[44:47], v36, s[16:17] offset:-4096 sc1\n"
"s_branch .Lch_pnx_91\n"
".Lch_pv_92:\n"
"s_bitset1_b32 s37, 0\n"
"s_cmp_eq_u32 s37, 255\n"
"s_cbranch_scc1 .Lch_pd_89\n"
".Lch_pdm_90:\n"
"global_load_lds_dword v39, s[16:17] sc1\n"
".Lch_pnx_91:\n"
"s_waitcnt vmcnt(7)\n"
"s_bitcmp1_b32 s37, 1\n"
"s_cbranch_scc1 .Lch_pdm_93\n"
"v_bitop3_b32 v38, v48, v49, v50 bitop3:0x80\n"
"v_bitop3_b32 v38, v38, 1, v51 bitop3:0x80\n"
"v_cmp_ne_u32 vcc, 0, v38\n"
"s_nop 0\n"
"s_cmp_eq_u64 vcc, -1\n"
"s_cbranch_scc1 .Lch_pv_95\n"
"global_load_dwordx4 v[48:51], v36, s[16:17] offset:-2048 sc1\n"
"s_branch .Lch_pnx_94\n"
".Lch_pv_95:\n"
"s_bitset1_b32 s37, 1\n"
"s_cmp_eq_u32 s37, 255\n"
"s_cbranch_scc1 .Lch_pd_89\n"
".Lch_pdm_93:\n"
"global_load_lds_dword v39, s[16:17] sc1\n"
".Lch_pnx_94:\n"
"s_waitcnt vmcnt(7)\n"
"s_bitcmp1_b32 s37, 2\n"
"s_cbranch_scc1 .Lch_pdm_96\n"
"v_bitop3_b32 v38, v52, v53, v54 bitop3:0x80\n"
"v_bitop3_b32 v38, v38, 1, v55 bitop3:0x80\n"
"v_cmp_ne_u32 vcc, 0, v38\n"
"s_nop 0\n"
"s_cmp_eq_u64 vcc, -1\n"
"s_cbranch_scc1 .Lch_pv_98\n"
"global_load_dwordx4 v[52:55], v36, s[16:17] offset:0 sc1\n"
"s_branch .Lch_pnx_97\n"
".Lch_pv_98:\n"
"s_bitset1_b32 s37, 2\n"
"s_cmp_eq_u32 s37, 255\n"
"s_cbranch_scc1 .Lch_pd_89\n"
".Lch_pdm_96:\n"
"global_load_lds_dword v39, s[16:17] sc1\n"
".Lch_pnx_97:\n"
"s_waitcnt vmcnt(7)\n"
"s_bitcmp1_b32 s37, 3\n"
"s_cbranch_scc1 .Lch_pdm_99\n"
"v_bitop3_b32 v38, v56, v57, v58 bitop3:0x80\n"
"v_bitop3_b32 v38, v38, 1, v59 bitop3:0x80\n"
"v_cmp_ne_u32 vcc, 0, v38\n"
"s_nop 0\n"
"s_cmp_eq_u64 vcc, -1\n"
"s_cbranch_scc1 .Lch_pv_101\n"
"global_load_dwordx4 v[56:59], v36, s[16:17] offset:2048 sc1\n"
"s_branch .Lch_pnx_100\n"
".Lch_pv_101:\n"
"s_bitset1_b32 s37, 3\n"
"s_cmp_eq_u32 s37, 255\n"
"s_cbranch_scc1 .Lch_pd_89\n"
".Lch_pdm_99:\n"
"global_load_lds_dword v39, s[16:17] sc1\n"
".Lch_pnx_100:\n"
"s_waitcnt vmcnt(7)\n"
"s_bitcmp1_b32 s37, 4\n"
"s_cbranch_scc1 .Lch_pdm_102\n"
"v_bitop3_b32 v38, v60, v61, v62 bitop3:0x80\n"
"v_bitop3_b32 v38, v38, 1, v63 bitop3:0x80\n"
"v_cmp_ne_u32 vcc, 0, v38\n"
"s_nop 0\n"
"s_cmp_eq_u64 vcc, -1\n"
"s_cbranch_scc1 .Lch_pv_104\n"
"global_load_dwordx4 v[60:63], v37, s[16:17] offset:-4096 sc1\n"
"s_branch .Lch_pnx_103\n"
".Lch_pv_104:\n"
"s_bitset1_b32 s37, 4\n"
"s_cmp_eq_u32 s37, 255\n"
"s_cbranch_scc1 .Lch_pd_89\n"
".Lch_pdm_102:\n"
"global_load_lds_dword v39, s[16:17] sc1\n"
".Lch_pnx_103:\n"
"s_waitcnt vmcnt(7)\n"
"s_bitcmp1_b32 s37, 5\n"
"s_cbranch_scc1 .Lch_pdm_105\n"
"v_bitop3_b32 v38, v64, v65, v66 bitop3:0x80\n"
"v_bitop3_b32 v38, v38, 1, v67 bitop3:0x80\n"
"v_cmp_ne_u32 vcc, 0, v38\n"
"s_nop 0\n"
"s_cmp_eq_u64 vcc, -1\n"
"s_cbranch_scc1 .Lch_pv_107\n"
"global_load_dwordx4 v[64:67], v37, s[16:17] offset:-2048 sc1\n"
"s_branch .Lch_pnx_106\n"
".Lch_pv_107:\n"
"s_bitset1_b32 s37, 5\n"
"s_cmp_eq_u32 s37, 255\n"
"s_cbranch_scc1 .Lch_pd_89\n"
".Lch_pdm_105:\n"
"global_load_lds_dword v39, s[16:17] sc1\n"
".Lch_pnx_106:\n"
"s_waitcnt vmcnt(7)\n"
"s_bitcmp1_b32 s37, 6\n"
"s_cbranch_scc1 .Lch_pdm_108\n"
"v_bitop3_b32 v38, v68, v69, v70 bitop3:0x80\n"
"v_bitop3_b32 v38, v38, 1, v71 bitop3:0x80\n"
"v_cmp_ne_u32 vcc, 0, v38\n"
"s_nop 0\n"
"s_cmp_eq_u64 vcc, -1\n"
"s_cbranch_scc1 .Lch_pv_110\n"
"global_load_dwordx4 v[68:71], v37, s[16:17] offset:0 sc1\n"
"s_branch .Lch_pnx_109\n"
".Lch_pv_110:\n"
"s_bitset1_b32 s37, 6\n"
"s_cmp_eq_u32 s37, 255\n"
"s_cbranch_scc1 .Lch_pd_89\n"
".Lch_pdm_108:\n"
"global_load_lds_dword v39, s[16:17] sc1\n"
".Lch_pnx_109:\n"
"s_waitcnt vmcnt(7)\n"
"s_bitcmp1_b32 s37, 7\n"
"s_cbranch_scc1 .Lch_pdm_111\n"
"v_bitop3_b32 v38, v72, v73, v74 bitop3:0x80\n"
"v_bitop3_b32 v38, v38, 1, v75 bitop3:0x80\n"
"v_cmp_ne_u32 vcc, 0, v38\n"
"s_nop 0\n"
"s_cmp_eq_u64 vcc, -1\n"
"s_cbranch_scc1 .Lch_pv_113\n"
"global_load_dwordx4 v[72:75], v37, s[16:17] offset:2048 sc1\n"
"s_branch .Lch_pnx_112\n"
".Lch_pv_113:\n"
"s_bitset1_b32 s37, 7\n"
"s_cmp_eq_u32 s37, 255\n"
"s_cbranch_scc1 .Lch_pd_89\n"
".Lch_pdm_111:\n"
"global_load_lds_dword v39, s[16:17] sc1\n"
".Lch_pnx_112:\n"
"s_add_u32 s36, s36, 1\n"
"s_cmp_lt_u32 s36, 2000000\n"
"s_cbranch_scc1 .Lch_pl_88\n"
"s_branch .Lch_abort\n"
".Lch_pd_89:\n"
"v_pk_add_f32 v[44:45], v[44:45], v[48:49]\n"
"v_pk_add_f32 v[52:53], v[52:53], v[56:57]\n"
"v_pk_add_f32 v[60:61], v[60:61], v[64:65]\n"
"v_pk_add_f32 v[68:69], v[68:69], v[72:73]\n"
"v_pk_add_f32 v[46:47], v[46:47], v[50:51]\n"
"v_pk_add_f32 v[54:55], v[54:55], v[58:59]\n"
"v_pk_add_f32 v[62:63], v[62:63], v[66:67]\n"
"v_pk_add_f32 v[70:71], v[70:71], v[74:75]\n"
"v_pk_add_f32 v[44:45], v[44:45], v[52:53]\n"
"v_pk_add_f32 v[60:61], v[60:61], v[68:69]\n"
"v_pk_add_f32 v[46:47], v[46:47], v[54:55]\n"
"v_pk_add_f32 v[62:63], v[62:63], v[70:71]\n"
"v_pk_add_f32 v[76:77], v[44:45], v[60:61]\n"
"v_pk_add_f32 v[78:79], v[46:47], v[62:63]\n"
"v_cvt_pk_bf16_f32 v38, v76, v77\n"
"v_cvt_pk_bf16_f32 v39, v78, v79\n"
"v_lshlrev_b32 v76, 16, v38\n"
"v_and_b32 v77, 0xffff0000, v38\n"
"v_lshlrev_b32 v78, 16, v39\n"
"v_and_b32 v79, 0xffff0000, v39\n"
"v_add_f32 v76, v20, v76\n"
"v_add_f32 v77, v21, v77\n"
"v_add_f32 v78, v22, v78\n"
"v_add_f32 v79, v23, v79\n"
"v_cvt_pk_bf16_f32 v38, v76, v77\n"
"v_cvt_pk_bf16_f32 v39, v78, v79\n"
"v_lshlrev_b32 v80, 16, v38\n"
"v_and_b32 v81, 0xffff0000, v38\n"
"v_lshlrev_b32 v82, 16, v39\n"
"v_and_b32 v83, 0xffff0000, v39\n"
"v_add_u32 v37, 135168, v142\n"
"ds_write_b64 v37, v[38:39]\n"
"s_and_b32 s34, s32, 1\n"
"s_lshl_b32 s34, s34, 11\n"
"v_add_u32 v36, s34, v16\n"
"ds_write_b128 v36, v[80:83]\n"
".Lch_x2skip_87:\n"
"s_waitcnt lgkmcnt(0)\n"
"s_barrier\n"
"ds_read_b128 v[108:111], v19 offset:2048\n"
"ds_read_b128 v[44:47], v3 offset:4096\n"
"ds_read_b128 v[48:51], v3 offset:4160\n"
"ds_read_b128 v[52:55], v3 offset:4224\n"
"ds_read_b128 v[56:59], v3 offset:4288\n"
"ds_read_b128 v[60:63], v3 offset:4352\n"
"ds_read_b128 v[64:67], v3 offset:4416\n"
"ds_read_b128 v[68:71], v3 offset:4480\n"
"ds_read_b128 v[72:75], v3 offset:4544\n"
"ds_read_b128 v[76:79], v3 offset:4608\n"
"ds_read_b128 v[80:83], v3 offset:4672\n"
"ds_read_b128 v[84:87], v3 offset:4736\n"
"ds_read_b128 v[88:91], v3 offset:4800\n"
"ds_read_b128 v[92:95], v3 offset:4864\n"
"ds_read_b128 v[96:99], v3 offset:4928\n"
"ds_read_b128 v[100:103], v3 offset:4992\n"
"ds_read_b128 v[104:107], v3 offset:5056\n"
"s_lshl_b32 s34, s32, 14\n"
"s_sub_u32 s34, 114688, s34\n"
"v_add_u32 v40, s34, v18\n"
"s_waitcnt lgkmcnt(15)\n"
"s_waitcnt lgkmcnt(15)\n"
"v_mfma_f32_16x16x32_bf16 v[24:27], v[44:47], a[0:3], 0\n"
"v_lshlrev_b32 v116, 16, v108\n"
"v_and_b32 v117, 0xffff0000, v108\n"
"v_mfma_f32_16x16x32_bf16 v[28:31], v[44:47], a[4:7], 0\n"
"v_lshlrev_b32 v118, 16, v109\n"
"v_and_b32 v119, 0xffff0000, v109\n"
"s_waitcnt lgkmcnt(14)\n"
"v_mfma_f32_16x16x32_bf16 v[24:27], v[48:51], a[8:11], v[24:27]\n"
"v_lshlrev_b32 v120, 16, v110\n"
"v_and_b32 v121, 0xffff0000, v110\n"
"v_mfma_f32_16x16x32_bf16 v[28:31], v[48:51], a[12:15], v[28:31]\n"
"v_lshlrev_b32 v122, 16, v111\n"
"v_and_b32 v123, 0xffff0000, v111\n"
"s_waitcnt lgkmcnt(13)\n"
"v_mfma_f32_16x16x32_bf16 v[24:27], v[52:55], a[16:19], v[24:27]\n"
"v_add_f32 v36, v116, v117\n"
"v_add_f32 v124, v118, v119\n"
"v_mfma_f32_16x16x32_bf16 v[28:31], v[52:55], a[20:23], v[28:31]\n"
"v_add_f32 v36, v36, v124\n"
"v_add_f32 v124, v120, v121\n"
"s_waitcnt lgkmcnt(12)\n"
"v_mfma_f32_16x16x32_bf16 v[24:27], v[56:59], a[24:27], v[24:27]\n"
"v_add_f32 v36, v36, v124\n"
"v_add_f32 v124, v122, v123\n"
"v_mfma_f32_16x16x32_bf16 v[28:31], v[56:59], a[28:31], v[28:31]\n"
"v_add_f32 v36, v36, v124\n"
"s_nop 1\n"
"v_add_f32_dpp v36, v36, v36 quad_perm:[1,0,3,2] row_mask:0xf bank_mask:0xf\n"
"s_waitcnt lgkmcnt(11)\n"
"v_mfma_f32_16x16x32_bf16 v[24:27], v[60:63], a[32:35], v[24:27]\n"
"v_add_f32_dpp v36, v36, v36 quad_perm:[2,3,0,1] row_mask:0xf bank_mask:0xf\n"
"s_nop 1\n"
"v_add_f32_dpp v36, v36, v36 row_half_mirror row_mask:0xf bank_mask:0xf\n"
"v_mfma_f32_16x16x32_bf16 v[28:31], v[60:63], a[36:39], v[28:31]\n"
"s_nop 0\n"
"v_add_f32_dpp v36, v36, v36 row_mirror row_mask:0xf bank_mask:0xf\n"
"v_mov_b32 v124, v36\n"
"s_waitcnt lgkmcnt(10)\n"
"v_mfma_f32_16x16x32_bf16 v[24:27], v[64:67], a[40:43], v[24:27]\n"
"v_permlane16_swap_b32 v124, v36\n"
"v_add_f32 v36, v36, v124\n"
"v_mfma_f32_16x16x32_bf16 v[28:31], v[64:67], a[44:47], v[28:31]\n"
"v_mov_b32 v124, v36\n"
"s_nop 1\n"
"v_permlane32_swap_b32 v124, v36\n"
"s_waitcnt lgkmcnt(9)\n"
"v_mfma_f32_16x16x32_bf16 v[24:27], v[68:71], a[48:51], v[24:27]\n"
"v_add_f32 v36, v36, v124\n"
"v_mul_f32 v36, 0x3b000000, v36\n"
"v_mfma_f32_16x16x32_bf16 v[28:31], v[68:71], a[52:55], v[28:31]\n"
"v_sub_f32 v116, v116, v36\n"
"v_sub_f32 v117, v117, v36\n"
"s_waitcnt lgkmcnt(8)\n"
"v_mfma_f32_16x16x32_bf16 v[24:27], v[72:75], a[56:59], v[24:27]\n"
"v_sub_f32 v118, v118, v36\n"
"v_sub_f32 v119, v119, v36\n"
"v_mfma_f32_16x16x32_bf16 v[28:31], v[72:75], a[60:63], v[28:31]\n"
"v_sub_f32 v120, v120, v36\n"
"v_sub_f32 v121, v121, v36\n"
"s_waitcnt lgkmcnt(7)\n"
"v_mfma_f32_16x16x32_bf16 v[24:27], v[76:79], a[64:67], v[24:27]\n"
"v_sub_f32 v122, v122, v36\n"
"v_sub_f32 v123, v123, v36\n"
"v_mfma_f32_16x16x32_bf16 v[28:31], v[76:79], a[68:71], v[28:31]\n"
"v_mul_f32 v37, v116, v116\n"
"v_fmac_f32 v37, v117, v117\n"
"s_waitcnt lgkmcnt(6)\n"
"v_mfma_f32_16x16x32_bf16 v[24:27], v[80:83], a[72:75], v[24:27]\n"
"v_fmac_f32 v37, v118, v118\n"
"v_fmac_f32 v37, v119, v119\n"
"v_mfma_f32_16x16x32_bf16 v[28:31], v[80:83], a[76:79], v[28:31]\n"
"v_fmac_f32 v37, v120, v120\n"
"v_fmac_f32 v37, v121, v121\n"
"s_waitcnt lgkmcnt(5)\n"
"v_mfma_f32_16x16x32_bf16 v[24:27], v[84:87], a[80:83], v[24:27]\n"
"v_fmac_f32 v37, v122, v122\n"
"v_fmac_f32 v37, v123, v123\n"
"v_mfma_f32_16x16x32_bf16 v[28:31], v[84:87], a[84:87], v[28:31]\n"
"s_nop 0\n"
"v_add_f32_dpp v37, v37, v37 quad_perm:[1,0,3,2] row_mask:0xf bank_mask:0xf\n"
"s_nop 1\n"
"v_add_f32_dpp v37, v37, v37 quad_perm:[2,3,0,1] row_mask:0xf bank_mask:0xf\n"
"s_waitcnt lgkmcnt(4)\n"
"v_mfma_f32_16x16x32_bf16 v[24:27], v[88:91], a[88:91], v[24:27]\n"
"v_add_f32_dpp v37, v37, v37 row_half_mirror row_mask:0xf bank_mask:0xf\n"
"s_nop 1\n"
"v_add_f32_dpp v37, v37, v37 row_mirror row_mask:0xf bank_mask:0xf\n"
"v_mfma_f32_16x16x32_bf16 v[28:31], v[88:91], a[92:95], v[28:31]\n"
"v_mov_b32 v124, v37\n"
"s_nop 1\n"
"v_permlane16_swap_b32 v124, v37\n"
"s_waitcnt lgkmcnt(3)\n"
"v_mfma_f32_16x16x32_bf16 v[24:27], v[92:95], a[96:99], v[24:27]\n"
"v_add_f32 v37, v37, v124\n"
"v_mov_b32 v124, v37\n"
"v_mfma_f32_16x16x32_bf16 v[28:31], v[92:95], a[100:103], v[28:31]\n"
"s_nop 0\n"
"v_permlane32_swap_b32 v124, v37\n"
"v_add_f32 v37, v37, v124\n"
"s_waitcnt lgkmcnt(2)\n"
"v_mfma_f32_16x16x32_bf16 v[24:27], v[96:99], a[104:107], v[24:27]\n"
"v_mov_b32 v124, 0x3727c5ac\n"
"v_fmamk_f32 v37, v37, 0x3b000000, v124\n"
"v_mfma_f32_16x16x32_bf16 v[28:31], v[96:99], a[108:111], v[28:31]\n"
"v_rsq_f32 v37, v37\n"
"s_waitcnt lgkmcnt(1)\n"
"v_mfma_f32_16x16x32_bf16 v[24:27], v[100:103], a[112:115], v[24:27]\n"
"v_mfma_f32_16x16x32_bf16 v[28:31], v[100:103], a[116:119], v[28:31]\n"
"s_waitcnt lgkmcnt(0)\n"
"v_mfma_f32_16x16x32_bf16 v[24:27], v[104:107], a[120:123], v[24:27]\n"
"v_mfma_f32_16x16x32_bf16 v[28:31], v[104:107], a[124:127], v[28:31]\n"
"s_nop 7\n"
"v_cndmask_b32_e64 v38, v24, v28, s[40:41]\n"
"v_fma_f32 v38, -v36, v141, v38\n"
"v_mul_f32 v38, v38, v37\n"
"v_cvt_pk_bf16_f32 v38, v38, v38\n"
"s_mov_b32 s78, 1\n"
"v_lshl_or_b32 v38, v38, 16, s78\n"
"s_mov_b64 exec, s[48:49]\n"
"global_store_dword v40, v38, s[16:17] sc1\n"
"s_mov_b64 exec, -1\n"
".Lch_noq_86:\n"
"s_xor_b32 s35, s76, 1\n"
"s_cmp_eq_u32 s62, 0\n"
"s_cselect_b32 s34, s35, s77\n"
"s_and_b32 s34, s34, s63\n"
"s_cmp_eq_u32 s34, 0\n"
"s_cbranch_scc1 .Lch_noout_114\n"
"s_nop 7\n"
"s_nop 3\n"
"v_add_u32 v40, 8192, v143\n"
"v_add_u32 v36, s33, v143\n"
"v_add_u32 v37, s33, v40\n"
"v_mov_b32 v39, 262144\n"
"s_mov_b32 m0, 143360\n"
"s_mov_b32 s36, 0\n"
"s_mov_b32 s37, 0\n"
"global_load_dwordx4 v[44:47], v36, s[16:17] offset:-4096 sc1\n"
"global_load_dwordx4 v[48:51], v36, s[16:17] offset:-2048 sc1\n"
"global_load_dwordx4 v[52:55], v36, s[16:17] offset:0 sc1\n"
"global_load_dwordx4 v[56:59], v36, s[16:17] offset:2048 sc1\n"
"global_load_dwordx4 v[60:63], v37, s[16:17] offset:-4096 sc1\n"
"global_load_dwordx4 v[64:67], v37, s[16:17] offset:-2048 sc1\n"
"global_load_dwordx4 v[68:71], v37, s[16:17] offset:0 sc1\n"
"global_load_dwordx4 v[72:75], v37, s[16:17] offset:2048 sc1\n"
".Lch_pl_115:\n"
"s_waitcnt vmcnt(7)\n"
"s_bitcmp1_b32 s37, 0\n"
"s_cbranch_scc1 .Lch_pdm_117\n"
"v_bitop3_b32 v38, v44, v45, v46 bitop3:0x80\n"
"v_bitop3_b32 v38, v38, 1, v47 bitop3:0x80\n"
"v_cmp_ne_u32 vcc, 0, v38\n"
"s_nop 0\n"
"s_cmp_eq_u64 vcc, -1\n"
"s_cbranch_scc1 .Lch_pv_119\n"
"global_load_dwordx4 v[44:47], v36, s[16:17] offset:-4096 sc1\n"
"s_branch .Lch_pnx_118\n"
".Lch_pv_119:\n"
"s_bitset1_b32 s37, 0\n"
"s_cmp_eq_u32 s37, 255\n"
"s_cbranch_scc1 .Lch_pd_116\n"
".Lch_pdm_117:\n"
"global_load_lds_dword v39, s[16:17] sc1\n"
".Lch_pnx_118:\n"
"s_waitcnt vmcnt(7)\n"
"s_bitcmp1_b32 s37, 1\n"
"s_cbranch_scc1 .Lch_pdm_120\n"
"v_bitop3_b32 v38, v48, v49, v50 bitop3:0x80\n"
"v_bitop3_b32 v38, v38, 1, v51 bitop3:0x80\n"
"v_cmp_ne_u32 vcc, 0, v38\n"
"s_nop 0\n"
"s_cmp_eq_u64 vcc, -1\n"
"s_cbranch_scc1 .Lch_pv_122\n"
"global_load_dwordx4 v[48:51], v36, s[16:17] offset:-2048 sc1\n"
"s_branch .Lch_pnx_121\n"
".Lch_pv_122:\n"
"s_bitset1_b32 s37, 1\n"
"s_cmp_eq_u32 s37, 255\n"
"s_cbranch_scc1 .Lch_pd_116\n"
".Lch_pdm_120:\n"
"global_load_lds_dword v39, s[16:17] sc1\n"
".Lch_pnx_121:\n"
"s_waitcnt vmcnt(7)\n"
"s_bitcmp1_b32 s37, 2\n"
"s_cbranch_scc1 .Lch_pdm_123\n"
"v_bitop3_b32 v38, v52, v53, v54 bitop3:0x80\n"
"v_bitop3_b32 v38, v38, 1, v55 bitop3:0x80\n"
"v_cmp_ne_u32 vcc, 0, v38\n"
"s_nop 0\n"
"s_cmp_eq_u64 vcc, -1\n"
"s_cbranch_scc1 .Lch_pv_125\n"
"global_load_dwordx4 v[52:55], v36, s[16:17] offset:0 sc1\n"
"s_branch .Lch_pnx_124\n"
".Lch_pv_125:\n"
"s_bitset1_b32 s37, 2\n"
"s_cmp_eq_u32 s37, 255\n"
"s_cbranch_scc1 .Lch_pd_116\n"
".Lch_pdm_123:\n"
"global_load_lds_dword v39, s[16:17] sc1\n"
".Lch_pnx_124:\n"
"s_waitcnt vmcnt(7)\n"
"s_bitcmp1_b32 s37, 3\n"
"s_cbranch_scc1 .Lch_pdm_126\n"
"v_bitop3_b32 v38, v56, v57, v58 bitop3:0x80\n"
"v_bitop3_b32 v38, v38, 1, v59 bitop3:0x80\n"
"v_cmp_ne_u32 vcc, 0, v38\n"
"s_nop 0\n"
"s_cmp_eq_u64 vcc, -1\n"
"s_cbranch_scc1 .Lch_pv_128\n"
"global_load_dwordx4 v[56:59], v36, s[16:17] offset:2048 sc1\n"
"s_branch .Lch_pnx_127\n"
".Lch_pv_128:\n"
"s_bitset1_b32 s37, 3\n"
"s_cmp_eq_u32 s37, 255\n"
"s_cbranch_scc1 .Lch_pd_116\n"
".Lch_pdm_126:\n"
"global_load_lds_dword v39, s[16:17] sc1\n"
".Lch_pnx_127:\n"
"s_waitcnt vmcnt(7)\n"
"s_bitcmp1_b32 s37, 4\n"
"s_cbranch_scc1 .Lch_pdm_129\n"
"v_bitop3_b32 v38, v60, v61, v62 bitop3:0x80\n"
"v_bitop3_b32 v38, v38, 1, v63 bitop3:0x80\n"
"v_cmp_ne_u32 vcc, 0, v38\n"
"s_nop 0\n"
"s_cmp_eq_u64 vcc, -1\n"
"s_cbranch_scc1 .Lch_pv_131\n"
"global_load_dwordx4 v[60:63], v37, s[16:17] offset:-4096 sc1\n"
"s_branch .Lch_pnx_130\n"
".Lch_pv_131:\n"
"s_bitset1_b32 s37, 4\n"
"s_cmp_eq_u32 s37, 255\n"
"s_cbranch_scc1 .Lch_pd_116\n"
".Lch_pdm_129:\n"
"global_load_lds_dword v39, s[16:17] sc1\n"
".Lch_pnx_130:\n"
"s_waitcnt vmcnt(7)\n"
"s_bitcmp1_b32 s37, 5\n"
"s_cbranch_scc1 .Lch_pdm_132\n"
"v_bitop3_b32 v38, v64, v65, v66 bitop3:0x80\n"
"v_bitop3_b32 v38, v38, 1, v67 bitop3:0x80\n"
"v_cmp_ne_u32 vcc, 0, v38\n"
"s_nop 0\n"
"s_cmp_eq_u64 vcc, -1\n"
"s_cbranch_scc1 .Lch_pv_134\n"
"global_load_dwordx4 v[64:67], v37, s[16:17] offset:-2048 sc1\n"
"s_branch .Lch_pnx_133\n"
".Lch_pv_134:\n"
"s_bitset1_b32 s37, 5\n"
"s_cmp_eq_u32 s37, 255\n"
"s_cbranch_scc1 .Lch_pd_116\n"
".Lch_pdm_132:\n"
"global_load_lds_dword v39, s[16:17] sc1\n"
".Lch_pnx_133:\n"
"s_waitcnt vmcnt(7)\n"
"s_bitcmp1_b32 s37, 6\n"
"s_cbranch_scc1 .Lch_pdm_135\n"
"v_bitop3_b32 v38, v68, v69, v70 bitop3:0x80\n"
"v_bitop3_b32 v38, v38, 1, v71 bitop3:0x80\n"
"v_cmp_ne_u32 vcc, 0, v38\n"
"s_nop 0\n"
"s_cmp_eq_u64 vcc, -1\n"
"s_cbranch_scc1 .Lch_pv_137\n"
"global_load_dwordx4 v[68:71], v37, s[16:17] offset:0 sc1\n"
"s_branch .Lch_pnx_136\n"
".Lch_pv_137:\n"
"s_bitset1_b32 s37, 6\n"
"s_cmp_eq_u32 s37, 255\n"
"s_cbranch_scc1 .Lch_pd_116\n"
".Lch_pdm_135:\n"
"global_load_lds_dword v39, s[16:17] sc1\n"
".Lch_pnx_136:\n"
"s_waitcnt vmcnt(7)\n"
"s_bitcmp1_b32 s37, 7\n"
"s_cbranch_scc1 .Lch_pdm_138\n"
"v_bitop3_b32 v38, v72, v73, v74 bitop3:0x80\n"
"v_bitop3_b32 v38, v38, 1, v75 bitop3:0x80\n"
"v_cmp_ne_u32 vcc, 0, v38\n"
"s_nop 0\n"
"s_cmp_eq_u64 vcc, -1\n"
"s_cbranch_scc1 .Lch_pv_140\n"
"global_load_dwordx4 v[72:75], v37, s[16:17] offset:2048 sc1\n"
"s_branch .Lch_pnx_139\n"
".Lch_pv_140:\n"
"s_bitset1_b32 s37, 7\n"
"s_cmp_eq_u32 s37, 255\n"
"s_cbranch_scc1 .Lch_pd_116\n"
".Lch_pdm_138:\n"
"global_load_lds_dword v39, s[16:17] sc1\n"
".Lch_pnx_139:\n"
"s_add_u32 s36, s36, 1\n"
"s_cmp_lt_u32 s36, 2000000\n"
"s_cbranch_scc1 .Lch_pl_115\n"
"s_branch .Lch_abort\n"
".Lch_pd_116:\n"
"v_pk_add_f32 v[44:45], v[44:45], v[48:49]\n"
"v_pk_add_f32 v[52:53], v[52:53], v[56:57]\n"
"v_pk_add_f32 v[60:61], v[60:61], v[64:65]\n"
"v_pk_add_f32 v[68:69], v[68:69], v[72:73]\n"
"v_pk_add_f32 v[46:47], v[46:47], v[50:51]\n"
"v_pk_add_f32 v[54:55], v[54:55], v[58:59]\n"
"v_pk_add_f32 v[62:63], v[62:63], v[66:67]\n"
"v_pk_add_f32 v[70:71], v[70:71], v[74:75]\n"
"v_pk_add_f32 v[44:45], v[44:45], v[52:53]\n"
"v_pk_add_f32 v[60:61], v[60:61], v[68:69]\n"
"v_pk_add_f32 v[46:47], v[46:47], v[54:55]\n"
"v_pk_add_f32 v[62:63], v[62:63], v[70:71]\n"
"v_pk_add_f32 v[76:77], v[44:45], v[60:61]\n"
"v_pk_add_f32 v[78:79], v[46:47], v[62:63]\n"
"v_cvt_pk_bf16_f32 v38, v76, v77\n"
"v_cvt_pk_bf16_f32 v39, v78, v79\n"
"v_lshlrev_b32 v76, 16, v38\n"
"v_and_b32 v77, 0xffff0000, v38\n"
"v_lshlrev_b32 v78, 16, v39\n"
"v_and_b32 v79, 0xffff0000, v39\n"
"v_add_f32 v76, v20, v76\n"
"v_add_f32 v77, v21, v77\n"
"v_add_f32 v78, v22, v78\n"
"v_add_f32 v79, v23, v79\n"
"v_cvt_pk_bf16_f32 v38, v76, v77\n"
"v_cvt_pk_bf16_f32 v39, v78, v79\n"
"s_sub_u32 s34, s32, 1\n"
"s_sub_u32 s34, s34, s62\n"
"s_lshl_b32 s34, s34, 10\n"
"v_add_u32 v36, s34, v142\n"
"s_mov_b64 exec, s[64:65]\n"
"global_store_dwordx2 v36, v[38:39], s[20:21]\n"
"s_mov_b64 exec, -1\n"
".Lch_noout_114:\n"
"s_add_u32 s32, s32, 1\n"
"s_cmp_eq_u32 s32, 8\n"
"s_cselect_b32 s34, 3, 2\n"
"s_cmp_eq_u32 s32, 1\n"
"s_cselect_b32 s34, 1, s34\n"
"s_lshl_b32 s34, s34, 17\n"
"s_add_u32 s66, s66, s34\n"
"s_add_u32 s67, s67, 262144\n"
"s_cmp_lt_u32 s32, 9\n"
"s_nop 7\n"
"s_nop 3\n"
"s_cbranch_scc1 .Lch_round\n"
"s_branch .Lch_end\n"
".Lch_abort:\n"
"s_mov_b64 exec, -1\n"
"v_mov_b32 v36, 262144\n"
"v_mov_b32 v37, 1\n"
"global_store_dword v36, v37, s[16:17] sc1\n"
".Lch_end:\n"
"s_waitcnt vmcnt(0)\n"
"s_endpgm\n"

    :: "s"(p.ws), "s"(p.x), "s"(p.out), "s"(wob), "s"(p.w1), "s"(p.w2), "s"(info), "s"(dbg),
       "v"(pk2(k0, k1)), "v"(pk2(k2, k3)), "v"(pk2(woa[0], woa[1])), "v"(pk2(woa[2], woa[3])), "v"(pk2(woa[0] + 65536, woa[1] + 65536)), "v"(pk2(woa[2] + 65536, woa[3] + 65536)),
       "v"(pk2(k12, k13)), "v"(pk2(k14, k15)), "v"(pk2(k16, k17)), "v"(pk2(k18, k19)), "v"(pk2(k20, k21)), "v"(pk2(k22, k23)), "v"(pk2(k24, k25)), "v"(pk2(k26, k27)),
       "v"(pk2(k28, k29[0])), "v"(pk2(k29[1], k29[2])), "s"(wqb)
    : "memory", "vcc", "scc", "v48", "v49", "v50", "v51", "v52", "v53", "v54", "v55", "v56", "v57", "v58", "v59", "v60", "v61", "v62", "v63", "v64", "v65", "v66", "v67", "v68", "v69", "v70", "v71", "v72", "v73", "v74", "v75", "v76", "v77", "v78", "v79", "v80", "v81", "v82", "v83", "v84", "v85", "v86", "v87", "v88", "v89", "v90", "v91", "v92", "v93", "v94", "v95", "v96", "v97", "v98", "v99", "v100", "v101", "v102", "v103", "v104", "v105", "v106", "v107", "v108", "v109", "v110", "v111", "v112", "v113", "v114", "v115", "v116", "v117", "v118", "v119", "v120", "v121", "v122", "v123", "v124", "v125", "v126", "v127", "v128", "v129", "v130", "v131", "v132", "v133", "v134", "v135", "v136", "v137", "v138", "v139", "v140", "v141", "v142", "v143", "v144", "v145", "v146", "v147", "v148", "v149", "v150", "v151", "v152", "v153", "v154", "v155", "v156", "v157", "v158", "v159", "v160", "v161", "v162", "v163", "v164", "v165", "v166", "v167", "v168", "v169", "v170", "v171", "v172", "v173", "v174", "v175", "v176", "v177", "v178", "v179", "v180", "v181", "v182", "v183", "v184", "v185", "v186", "v187", "v188", "v189", "v190", "v191", "v192", "v193", "v194", "v195", "v196", "v197", "v198", "v199", "v200", "v201", "v202", "v203", "v204", "v205", "v206", "v207", "v208", "v209", "v210", "v211", "v212", "v213", "v214", "v215", "v216", "v217", "v218", "v219", "v220", "v221", "v222", "v223", "v224", "v225", "v226", "v227", "v228", "v229", "v230", "v231", "v232", "v233", "v234", "v235", "v236", "v237", "v238", "v239", "v240", "v241", "v242", "v243", "v244", "v245", "v246", "v247", "v248", "v249", "v250", "v251", "v252", "v253", "v254", "v255", "a0", "a1", "a2", "a3", "a4", "a5", "a6", "a7", "a8", "a9", "a10", "a11", "a12", "a13", "a14", "a15", "a16", "a17", "a18", "a19", "a20", "a21", "a22", "a23", "a24", "a25", "a26", "a27", "a28", "a29", "a30", "a31", "a32", "a33", "a34", "a35", "a36", "a37", "a38", "a39", "a40", "a41", "a42", "a43", "a44", "a45", "a46", "a47", "a48", "a49", "a50", "a51", "a52", "a53", "a54", "a55", "a56", "a57", "a58", "a59", "a60", "a61", "a62", "a63", "a64", "a65", "a66", "a67", "a68", "a69", "a70", "a71", "a72", "a73", "a74", "a75", "a76", "a77", "a78", "a79", "a80", "a81", "a82", "a83", "a84", "a85", "a86", "a87", "a88", "a89", "a90", "a91", "a92", "a93", "a94", "a95", "a96", "a97", "a98", "a99", "a100", "a101", "a102", "a103", "a104", "a105", "a106", "a107", "a108", "a109", "a110", "a111", "a112", "a113", "a114", "a115", "a116", "a117", "a118", "a119", "a120", "a121", "a122", "a123", "a124", "a125", "a126", "a127", "a128", "a129", "a130", "a131", "a132", "a133", "a134", "a135", "a136", "a137", "a138", "a139", "a140", "a141", "a142", "a143", "a144", "a145", "a146", "a147", "a148", "a149", "a150", "a151", "a152", "a153", "a154", "a155", "a156", "a157", "a158", "a159", "a160", "a161", "a162", "a163", "a164", "a165", "a166", "a167", "a168", "a169", "a170", "a171", "a172", "a173", "a174", "a175", "a176", "a177", "a178", "a179", "a180", "a181", "a182", "a183", "a184", "a185", "a186", "a187", "a188", "a189", "a190", "a191", "a192", "a193", "a194", "a195", "a196", "a197", "a198", "a199", "a200", "a201", "a202", "a203", "a204", "a205", "a206", "a207", "a208", "a209", "a210", "a211", "a212", "a213", "a214", "a215", "a216", "a217", "a218", "a219", "a220", "a221", "a222", "a223", "a224", "a225", "a226", "a227", "a228", "a229", "a230", "a231", "a232", "a233", "a234", "a235", "a236", "a237", "a238", "a239", "a240", "a241", "a242", "a243", "a244", "a245", "a246", "a247", "a248", "a249", "a250", "a251", "a252", "a253", "a254", "a255", "s16", "s17", "s18", "s19", "s20", "s21", "s22", "s23", "s24", "s25", "s26", "s27", "s28", "s29", "s30", "s31", "s32", "s33", "s34", "s35", "s36", "s37", "s38", "s39", "s40", "s41", "s42", "s43", "s44", "s45", "s46", "s47", "s48", "s49", "s50", "s51", "s52", "s53", "s54", "s55", "s56", "s57", "s58", "s59", "s60", "s61", "s62", "s63", "s64", "s65", "s66", "s67", "s68", "s69", "s70", "s71", "s72", "s73", "s74", "s75", "s76", "s77", "s78", "s79");
}

__device__ __forceinline__ void stream_role(const Params& p) {
  const int tid = threadIdx.x, lane = tid & 63, w = __builtin_amdgcn_readfirstlane(tid >> 6);
  const int s = blockIdx.x - NCHAIN, h = s / NSUB, sub = s % NSUB;
  const int wv = sub * 4 + w;
  const int q0 = wv;
  const int g = lane >> 4, c = lane & 15;
  const size_t base = ((size_t)h * SK + q0 * 4) * 256;
  const char* kb = (const char*)p.K + base;
  const char* vb = (const char*)p.V + base;
  const unsigned q0off = WS_QTOP + h * 512, p0off = WS_PART + h * 4096;
  const unsigned koff = (c >> 2) * 256 + g * 64 + (c & 3) * 16, voff = lane * 16;
  unsigned char* dbg = p.ws + WS_DBG + (size_t)(blockIdx.x * 4 + w) * 1024;
  const int xg = blockIdx.x & 7, jw = ((blockIdx.x >> 3) - 4) * 4 + w;
  const char* pf0 = (const char*)p.wq + xg * 256;
  const char* pf1 = (const char*)p.w1 + ((344 * xg) & ~127);
  const char* pf2 = (const char*)p.w2 + (size_t)xg * 176128;
  const char* pf3 = (const char*)p.wo + (size_t)xg * 524288;
  const unsigned flags = __builtin_amdgcn_readfirstlane((wv < 16 ? 1u : 0u) | (w == 0 ? 2u : 0u) | (unsigned)(w << 2) | (unsigned)(sub << 4) | (unsigned)(jw << 8));
  asm volatile(
".Lst_begin:\n"
"v_mov_b32 v200, %11\n"
"v_mov_b32 v201, %12\n"
"v_mov_b32 v0, v200\n"
"v_mov_b32 v1, v201\n"
"s_mov_b64 s[16:17], %0\n"
"s_mov_b64 s[18:19], %1\n"
"s_mov_b64 s[22:23], %2\n"
"s_mov_b64 s[76:77], %3\n"
"s_bfe_u32 s63, %4, 0x20002\n"
"s_bfe_u32 s70, %4, 0x30004\n"
"s_mul_i32 s25, s63, 38912\n"
"s_add_u32 s79, s25, 38912\n"
"s_mul_i32 s61, s63, 528\n"
"s_lshl_b32 s78, s63, 7\n"
"s_lshl_b32 s72, s70, 9\n"
"s_add_u32 s72, s72, s78\n"
"s_add_u32 s78, s78, 16\n"
"s_lshl_b32 s73, s70, 3\n"
"s_add_u32 s73, s73, 3584\n"
"s_mov_b32 s56, %5\n"
"s_sub_u32 s57, %5, 131072\n"
"s_mov_b32 s59, %6\n"
"s_add_u32 s58, %6, 131072\n"
"s_bitcmp1_b32 %4, 0\n"
"s_cselect_b64 s[30:31], -1, 1\n"
"s_cselect_b32 s93, 28672, 0\n"
"s_add_u32 s93, s93, 1003520\n"
"s_bitcmp1_b32 %4, 0\n"
"s_cselect_b32 s46, 0x00020002, 0\n"
"s_or_b32 s46, s46, 0x11111111\n"
"s_mov_b32 s47, s46\n"
"s_bitcmp1_b32 %4, 1\n"
"s_mov_b32 s50, -1\n"
"s_cselect_b32 s51, 3, 0\n"
"s_mov_b32 s34, 0xaaaaaaaa\n"
"s_mov_b32 s35, 0xaaaaaaaa\n"
"s_mov_b32 s36, 0xcccccccc\n"
"s_mov_b32 s37, 0xcccccccc\n"
"s_mov_b32 s48, 0xff00ff\n"
"s_mov_b32 s49, 0xff00ff\n"
"s_mov_b32 s38, 0xf000f\n"
"s_mov_b32 s39, 0xf000f\n"
"s_mov_b32 s40, 0xf000f0\n"
"s_mov_b32 s41, 0xf000f0\n"
"s_mov_b32 s42, 0xf000f00\n"
"s_mov_b32 s43, 0xf000f00\n"
"s_mov_b32 s44, 0xf000f000\n"
"s_mov_b32 s45, 0xf000f000\n"
"s_mov_b32 s80, 0x30003\n"
"s_mov_b32 s81, 0x30003\n"
"s_mov_b32 s82, 0xc000c\n"
"s_mov_b32 s83, 0xc000c\n"
"s_mov_b32 s84, 0x300030\n"
"s_mov_b32 s85, 0x300030\n"
"s_mov_b32 s86, 0xc000c0\n"
"s_mov_b32 s87, 0xc000c0\n"
"s_mov_b32 s66, 0\n"
"s_mov_b32 s67, -1\n"
"s_mov_b32 s64, 0x3e0293ee\n"
"s_mov_b32 s65, 0xbe0293ee\n"
"s_mov_b32 s69, 0x07060302\n"
"s_mov_b32 s62, 155648\n"
"s_mov_b32 s26, s25\n"
"s_mov_b32 s28, s25\n"
"s_add_u32 s27, s25, 16384\n"
"s_mov_b32 s24, 0\n"
"s_mov_b32 s89, 1\n"
"s_mov_b64 s[32:33], -1\n"
"s_mov_b32 s68, 0x2000000\n"
"v_accvgpr_write_b32 a228, 0\n"
"v_accvgpr_write_b32 a229, 0\n"
"v_accvgpr_write_b32 a230, 0\n"
"v_accvgpr_write_b32 a231, 0\n"
"v_mov_b32 v252, 0\n"
"v_mov_b32 v253, 0\n"
"v_mov_b32 v254, 0\n"
"v_mov_b32 v255, 0\n"
"s_bfe_u32 s63, %4, 0x80008\n"
"v_lshrrev_b32 v20, 8, v1\n"
"v_and_b32 v21, 0xff, v1\n"
"v_lshl_or_b32 v20, v20, 13, v21\n"
"v_lshrrev_b32 v21, 9, v1\n"
"v_mul_u32_u24 v21, 2752, v21\n"
"v_and_b32 v47, 0x1ff, v1\n"
"v_add_u32 v21, v21, v47\n"
"s_getpc_b64 s[74:75]\n"
".Lst_pc:\n"
"s_add_u32 s20, s74, (.Lch_begin-.Lst_pc)&0xffffffff\n"
"s_addc_u32 s21, s75, ((.Lch_begin-.Lst_pc)>>32)&0xffffffff\n"
"s_sub_u32 s74, s74, .Lst_pc-.Lst_begin\n"
"s_subb_u32 s75, s75, 0\n"
"s_min_u32 s70, s63, ((.Lst_end-.Lst_begin)>>10)-1\n"
"s_sub_u32 s71, s63, 64\n"
"s_min_u32 s71, s71, ((.Lch_end-.Lch_begin)>>10)-1\n"
"s_cmp_lt_u32 s63, 64\n"
"s_cselect_b64 s[74:75], s[74:75], s[20:21]\n"
"s_cselect_b32 s70, s70, s71\n"
"s_lshl_b32 s70, s70, 10\n"
"s_add_u32 s20, s74, s70\n"
"s_addc_u32 s21, s75, 0\n"
"global_load_dwordx4 v[108:111], v1, s[20:21]\n"
"s_mov_b64 s[74:75], %7\n"
"s_add_u32 s70, s63, 0\n"
"s_min_u32 s70, s70, 511\n"
"s_lshr_b32 s71, s70, 7\n"
"s_lshl_b32 s71, s71, 11\n"
"s_and_b32 s70, s70, 127\n"
"s_lshl_b32 s70, s70, 15\n"
"s_add_u32 s70, s70, s71\n"
"s_add_u32 s20, s74, s70\n"
"s_addc_u32 s21, s75, 0\n"
"global_load_dwordx4 v[112:115], v20, s[20:21]\n"
"s_add_u32 s70, s63, 112\n"
"s_min_u32 s70, s70, 511\n"
"s_lshr_b32 s71, s70, 7\n"
"s_lshl_b32 s71, s71, 11\n"
"s_and_b32 s70, s70, 127\n"
"s_lshl_b32 s70, s70, 15\n"
"s_add_u32 s70, s70, s71\n"
"s_add_u32 s20, s74, s70\n"
"s_addc_u32 s21, s75, 0\n"
"global_load_dwordx4 v[116:119], v20, s[20:21]\n"
"s_add_u32 s70, s63, 224\n"
"s_min_u32 s70, s70, 511\n"
"s_lshr_b32 s71, s70, 7\n"
"s_lshl_b32 s71, s71, 11\n"
"s_and_b32 s70, s70, 127\n"
"s_lshl_b32 s70, s70, 15\n"
"s_add_u32 s70, s70, s71\n"
"s_add_u32 s20, s74, s70\n"
"s_addc_u32 s21, s75, 0\n"
"global_load_dwordx4 v[120:123], v20, s[20:21]\n"
"s_add_u32 s70, s63, 336\n"
"s_min_u32 s70, s70, 511\n"
"s_lshr_b32 s71, s70, 7\n"
"s_lshl_b32 s71, s71, 11\n"
"s_and_b32 s70, s70, 127\n"
"s_lshl_b32 s70, s70, 15\n"
"s_add_u32 s70, s70, s71\n"
"s_add_u32 s20, s74, s70\n"
"s_addc_u32 s21, s75, 0\n"
"global_load_dwordx4 v[124:127], v20, s[20:21]\n"
"s_add_u32 s70, s63, 448\n"
"s_min_u32 s70, s70, 511\n"
"s_lshr_b32 s71, s70, 7\n"
"s_lshl_b32 s71, s71, 11\n"
"s_and_b32 s70, s70, 127\n"
"s_lshl_b32 s70, s70, 15\n"
"s_add_u32 s70, s70, s71\n"
"s_add_u32 s20, s74, s70\n"
"s_addc_u32 s21, s75, 0\n"
"global_load_dwordx4 v[128:131], v20, s[20:21]\n"
"s_mov_b64 s[74:75], %9\n"
"s_add_u32 s70, s63, 0\n"
"s_min_u32 s70, s70, 171\n"
"s_lshl_b32 s70, s70, 10\n"
"s_add_u32 s20, s74, s70\n"
"s_addc_u32 s21, s75, 0\n"
"global_load_dwordx4 v[132:135], v1, s[20:21]\n"
"s_add_u32 s70, s63, 112\n"
"s_min_u32 s70, s70, 171\n"
"s_lshl_b32 s70, s70, 10\n"
"s_add_u32 s20, s74, s70\n"
"s_addc_u32 s21, s75, 0\n"
"global_load_dwordx4 v[136:139], v1, s[20:21]\n"
"s_add_u32 s20, s16, 0\n"
"s_addc_u32 s21, s17, 0\n"
"s_mov_b32 m0, s26\n"
"s_add_u32 s26, s26, 1024\n"
"global_load_lds_dwordx4 v0, s[20:21] nt\n"
"s_add_u32 s20, s16, 28672\n"
"s_addc_u32 s21, s17, 0\n"
"s_mov_b32 m0, s26\n"
"s_add_u32 s26, s26, 1024\n"
"s_cmp_eq_u32 s26, s79\n"
"s_cselect_b32 s26, s25, s26\n"
"global_load_lds_dwordx4 v0, s[20:21] nt\n"
"s_add_u32 s20, s16, 57344\n"
"s_addc_u32 s21, s17, 0\n"
"s_mov_b32 m0, s26\n"
"s_add_u32 s26, s26, 1024\n"
"global_load_lds_dwordx4 v0, s[20:21] nt\n"
"s_add_u32 s20, s16, 86016\n"
"s_addc_u32 s21, s17, 0\n"
"s_mov_b32 m0, s26\n"
"s_add_u32 s26, s26, 1024\n"
"s_cmp_eq_u32 s26, s79\n"
"s_cselect_b32 s26, s25, s26\n"
"global_load_lds_dwordx4 v0, s[20:21] nt\n"
"s_add_u32 s20, s16, 114688\n"
"s_addc_u32 s21, s17, 0\n"
"s_mov_b32 m0, s26\n"
"s_add_u32 s26, s26, 1024\n"
"global_load_lds_dwordx4 v0, s[20:21] nt\n"
"s_add_u32 s20, s16, 143360\n"
"s_addc_u32 s21, s17, 0\n"
"s_mov_b32 m0, s26\n"
"s_add_u32 s26, s26, 1024\n"
"s_cmp_eq_u32 s26, s79\n"
"s_cselect_b32 s26, s25, s26\n"
"global_load_lds_dwordx4 v0, s[20:21] nt\n"
"s_add_u32 s20, s16, 172032\n"
"s_addc_u32 s21, s17, 0\n"
"s_mov_b32 m0, s26\n"
"s_add_u32 s26, s26, 1024\n"
"global_load_lds_dwordx4 v0, s[20:21] nt\n"
"s_waitcnt vmcnt(6)\n"
"s_add_u32 s20, s16, 200704\n"
"s_addc_u32 s21, s17, 0\n"
"s_mov_b32 m0, s26\n"
"s_add_u32 s26, s26, 1024\n"
"s_cmp_eq_u32 s26, s79\n"
"s_cselect_b32 s26, s25, s26\n"
"global_load_lds_dwordx4 v0, s[20:21] nt\n"
"s_waitcnt vmcnt(6)\n"
"s_add_u32 s20, s16, 229376\n"
"s_addc_u32 s21, s17, 0\n"
"s_mov_b32 m0, s26\n"
"s_add_u32 s26, s26, 1024\n"
"global_load_lds_dwordx4 v0, s[20:21] nt\n"
"s_mov_b64 s[74:75], %10\n"
"s_add_u32 s70, s63, 0\n"
"s_min_u32 s70, s70, 511\n"
"s_lshl_b32 s70, s70, 10\n"
"s_add_u32 s20, s74, s70\n"
"s_addc_u32 s21, s75, 0\n"
"global_load_dwordx4 v[140:143], v1, s[20:21]\n"
"s_add_u32 s70, s63, 112\n"
"s_min_u32 s70, s70, 511\n"
"s_lshl_b32 s70, s70, 10\n"
"s_add_u32 s20, s74, s70\n"
"s_addc_u32 s21, s75, 0\n"
"global_load_dwordx4 v[144:147], v1, s[20:21]\n"
"s_add_u32 s70, s63, 224\n"
"s_min_u32 s70, s70, 511\n"
"s_lshl_b32 s70, s70, 10\n"
"s_add_u32 s20, s74, s70\n"
"s_addc_u32 s21, s75, 0\n"
"global_load_dwordx4 v[148:151], v1, s[20:21]\n"
"s_add_u32 s70, s63, 336\n"
"s_min_u32 s70, s70, 511\n"
"s_lshl_b32 s70, s70, 10\n"
"s_add_u32 s20, s74, s70\n"
"s_addc_u32 s21, s75, 0\n"
"global_load_dwordx4 v[152:155], v1, s[20:21]\n"
"s_add_u32 s70, s63, 448\n"
"s_min_u32 s70, s70, 511\n"
"s_lshl_b32 s70, s70, 10\n"
"s_add_u32 s20, s74, s70\n"
"s_addc_u32 s21, s75, 0\n"
"global_load_dwordx4 v[156:159], v1, s[20:21]\n"
"s_mov_b64 s[74:75], %8\n"
"s_add_u32 s70, s63, 0\n"
"s_min_u32 s70, s70, 254\n"
"s_mul_i32 s70, s70, 5504\n"
"s_add_u32 s20, s74, s70\n"
"s_addc_u32 s21, s75, 0\n"
"global_load_dwordx4 v[160:163], v21, s[20:21]\n"
"s_add_u32 s70, s63, 112\n"
"s_min_u32 s70, s70, 254\n"
"s_mul_i32 s70, s70, 5504\n"
"s_add_u32 s20, s74, s70\n"
"s_addc_u32 s21, s75, 0\n"
"global_load_dwordx4 v[164:167], v21, s[20:21]\n"
"s_add_u32 s70, s63, 224\n"
"s_min_u32 s70, s70, 254\n"
"s_mul_i32 s70, s70, 5504\n"
"s_add_u32 s20, s74, s70\n"
"s_addc_u32 s21, s75, 0\n"
"global_load_dwordx4 v[168:171], v21, s[20:21]\n"
"s_waitcnt vmcnt(14)\n"
"s_add_u32 s20, s16, 258048\n"
"s_addc_u32 s21, s17, 0\n"
"s_mov_b32 m0, s26\n"
"s_add_u32 s26, s26, 1024\n"
"s_cmp_eq_u32 s26, s79\n"
"s_cselect_b32 s26, s25, s26\n"
"global_load_lds_dwordx4 v0, s[20:21] nt\n"
"s_waitcnt vmcnt(14)\n"
"s_add_u32 s20, s16, 286720\n"
"s_addc_u32 s21, s17, 0\n"
"s_mov_b32 m0, s26\n"
"s_add_u32 s26, s26, 1024\n"
"global_load_lds_dwordx4 v0, s[20:21] nt\n"
"s_waitcnt vmcnt(14)\n"
"s_add_u32 s20, s16, 315392\n"
"s_addc_u32 s21, s17, 0\n"
"s_mov_b32 m0, s26\n"
"s_add_u32 s26, s26, 1024\n"
"s_cmp_eq_u32 s26, s79\n"
"s_cselect_b32 s26, s25, s26\n"
"global_load_lds_dwordx4 v0, s[20:21] nt\n"
"s_waitcnt vmcnt(14)\n"
"s_add_u32 s20, s16, 344064\n"
"s_addc_u32 s21, s17, 0\n"
"s_mov_b32 m0, s26\n"
"s_add_u32 s26, s26, 1024\n"
"global_load_lds_dwordx4 v0, s[20:21] nt\n"
"s_waitcnt vmcnt(14)\n"
"s_add_u32 s20, s16, 372736\n"
"s_addc_u32 s21, s17, 0\n"
"s_mov_b32 m0, s26\n"
"s_add_u32 s26, s26, 1024\n"
"s_cmp_eq_u32 s26, s79\n"
"s_cselect_b32 s26, s25, s26\n"
"global_load_lds_dwordx4 v0, s[20:21] nt\n"
"s_waitcnt vmcnt(14)\n"
"s_add_u32 s20, s16, 401408\n"
"s_addc_u32 s21, s17, 0\n"
"s_mov_b32 m0, s26\n"
"s_add_u32 s26, s26, 1024\n"
"global_load_lds_dwordx4 v0, s[20:21] nt\n"
"s_waitcnt vmcnt(14)\n"
"s_add_u32 s20, s16, 430080\n"
"s_addc_u32 s21, s17, 0\n"
"s_mov_b32 m0, s26\n"
"s_add_u32 s26, s26, 1024\n"
"s_cmp_eq_u32 s26, s79\n"
"s_cselect_b32 s26, s25, s26\n"
"global_load_lds_dwordx4 v0, s[20:21] nt\n"
"s_waitcnt vmcnt(6)\n"
"s_add_u32 s20, s16, 458752\n"
"s_addc_u32 s21, s17, 0\n"
"global_load_dwordx4 a[0:3], v0, s[20:21] offset:0 nt\n"
"s_waitcnt vmcnt(6)\n"
"s_add_u32 s20, s16, 487424\n"
"s_addc_u32 s21, s17, 0\n"
"global_load_dwordx4 a[4:7], v0, s[20:21] offset:0 nt\n"
"s_waitcnt vmcnt(6)\n"
"s_add_u32 s20, s16, 516096\n"
"s_addc_u32 s21, s17, 0\n"
"global_load_dwordx4 a[8:11], v0, s[20:21] offset:0 nt\n"
"s_waitcnt vmcnt(6)\n"
"s_add_u32 s20, s16, 544768\n"
"s_addc_u32 s21, s17, 0\n"
"global_load_dwordx4 a[12:15], v0, s[20:21] offset:0 nt\n"
"s_waitcnt vmcnt(6)\n"
"s_add_u32 s20, s16, 573440\n"
"s_addc_u32 s21, s17, 0\n"
"global_load_dwordx4 a[16:19], v0, s[20:21] offset:0 nt\n"
"s_waitcnt vmcnt(6)\n"
"s_add_u32 s20, s16, 602112\n"
"s_addc_u32 s21, s17, 0\n"
"global_load_dwordx4 a[20:23], v0, s[20:21] offset:0 nt\n"
"s_waitcnt vmcnt(6)\n"
"s_add_u32 s20, s16, 630784\n"
"s_addc_u32 s21, s17, 0\n"
"global_load_dwordx4 a[24:27], v0, s[20:21] offset:0 nt\n"
"s_waitcnt vmcnt(6)\n"
"s_add_u32 s20, s16, 659456\n"
"s_addc_u32 s21, s17, 0\n"
"global_load_dwordx4 a[28:31], v0, s[20:21] offset:0 nt\n"
"s_waitcnt vmcnt(6)\n"
"s_add_u32 s20, s16, 688128\n"
"s_addc_u32 s21, s17, 0\n"
"global_load_dwordx4 a[32:35], v0, s[20:21] offset:0 nt\n"
"s_waitcnt vmcnt(6)\n"
"s_add_u32 s20, s16, 716800\n"
"s_addc_u32 s21, s17, 0\n"
"global_load_dwordx4 a[36:39], v0, s[20:21] offset:0 nt\n"
"s_waitcnt vmcnt(6)\n"
"s_add_u32 s20, s16, 745472\n"
"s_addc_u32 s21, s17, 0\n"
"global_load_dwordx4 a[40:43], v0, s[20:21] offset:0 nt\n"
"s_waitcnt vmcnt(6)\n"
"s_add_u32 s20, s16, 774144\n"
"s_addc_u32 s21, s17, 0\n"
"global_load_dwordx4 a[44:47], v0, s[20:21] offset:0 nt\n"
"s_waitcnt vmcnt(6)\n"
"s_add_u32 s20, s16, 802816\n"
"s_addc_u32 s21, s17, 0\n"
"global_load_dwordx4 a[48:51], v0, s[20:21] offset:0 nt\n"
"s_waitcnt vmcnt(6)\n"
"s_add_u32 s20, s16, 831488\n"
"s_addc_u32 s21, s17, 0\n"
"global_load_dwordx4 a[52:55], v0, s[20:21] offset:0 nt\n"
"s_waitcnt vmcnt(6)\n"
"s_add_u32 s20, s16, 860160\n"
"s_addc_u32 s21, s17, 0\n"
"global_load_dwordx4 a[56:59], v0, s[20:21] offset:0 nt\n"
"s_waitcnt vmcnt(6)\n"
"s_add_u32 s20, s16, 888832\n"
"s_addc_u32 s21, s17, 0\n"
"global_load_dwordx4 a[60:63], v0, s[20:21] offset:0 nt\n"
"s_waitcnt vmcnt(6)\n"
"s_add_u32 s20, s16, 917504\n"
"s_addc_u32 s21, s17, 0\n"
"global_load_dwordx4 a[64:67], v0, s[20:21] offset:0 nt\n"
"s_waitcnt vmcnt(6)\n"
"s_add_u32 s20, s16, 946176\n"
"s_addc_u32 s21, s17, 0\n"
"global_load_dwordx4 a[68:71], v0, s[20:21] offset:0 nt\n"
"s_waitcnt vmcnt(6)\n"
"s_add_u32 s20, s16, 974848\n"
"s_addc_u32 s21, s17, 0\n"
"global_load_dwordx4 a[72:75], v0, s[20:21] offset:0 nt\n"
"v_and_b32 v47, 0xff, v0\n"
"v_lshl_add_u32 v47, v47, 1, s56\n"
"global_load_dwordx4 v[4:7], v47, s[22:23] sc1\n"
"global_load_dwordx4 v[8:11], v47, s[22:23] offset:16 sc1\n"
"s_waitcnt vmcnt(8)\n"
"s_add_u32 s20, s16, 1003520\n"
"s_addc_u32 s21, s17, 0\n"
"global_load_dwordx4 a[76:79], v0, s[20:21] offset:0 nt\n"
"s_waitcnt vmcnt(8)\n"
"s_add_u32 s20, s16, s93\n"
"s_addc_u32 s21, s17, 0\n"
"s_mov_b64 exec, s[30:31]\n"
"global_load_dwordx4 a[80:83], v0, s[20:21] offset:0 nt\n"
"s_mov_b64 exec, -1\n"
"s_waitcnt vmcnt(8)\n"
"s_add_u32 s20, s18, 0\n"
"s_addc_u32 s21, s19, 0\n"
"global_load_dwordx4 a[84:87], v1, s[20:21] offset:0 nt\n"
"s_waitcnt vmcnt(8)\n"
"s_add_u32 s20, s18, 28672\n"
"s_addc_u32 s21, s19, 0\n"
"global_load_dwordx4 a[88:91], v1, s[20:21] offset:0 nt\n"
"s_waitcnt vmcnt(8)\n"
"s_add_u32 s20, s18, 57344\n"
"s_addc_u32 s21, s19, 0\n"
"global_load_dwordx4 a[92:95], v1, s[20:21] offset:0 nt\n"
"s_waitcnt vmcnt(8)\n"
"s_add_u32 s20, s18, 86016\n"
"s_addc_u32 s21, s19, 0\n"
"global_load_dwordx4 a[96:99], v1, s[20:21] offset:0 nt\n"
"s_waitcnt vmcnt(8)\n"
"s_add_u32 s20, s18, 114688\n"
"s_addc_u32 s21, s19, 0\n"
"global_load_dwordx4 a[100:103], v1, s[20:21] offset:0 nt\n"
"s_waitcnt vmcnt(6)\n"
"s_add_u32 s20, s18, 143360\n"
"s_addc_u32 s21, s19, 0\n"
"global_load_dwordx4 a[104:107], v1, s[20:21] offset:0 nt\n"
"s_waitcnt vmcnt(6)\n"
"s_add_u32 s20, s18, 172032\n"
"s_addc_u32 s21, s19, 0\n"
"global_load_dwordx4 a[108:111], v1, s[20:21] offset:0 nt\n"
"s_waitcnt vmcnt(9)\n"
"v_bitop3_b32 v2, v4, v5, v6 bitop3:0x80\n"
"v_bitop3_b32 v3, v7, v8, v9 bitop3:0x80\n"
"v_bitop3_b32 v2, v2, v10, v11 bitop3:0x80\n"
"v_bitop3_b32 v2, v2, 1, v3 bitop3:0x80\n"
"v_cmp_ne_u32 vcc, 0, v2\n"
"s_nop 1\n"
"s_cmp_eq_u64 vcc, -1\n"
"s_cbranch_scc0 .Lst_qslow_1\n"
".Lst_qok_2:\n"
"v_perm_b32 v20, v5, v4, s69\n"
"v_perm_b32 v21, v7, v6, s69\n"
"v_perm_b32 v22, v9, v8, s69\n"
"v_perm_b32 v23, v11, v10, s69\n"
"v_cndmask_b32_e64 v4, 0, v20, s[38:39]\n"
"v_cndmask_b32_e64 v5, 0, v21, s[38:39]\n"
"v_cndmask_b32_e64 v6, 0, v22, s[38:39]\n"
"v_cndmask_b32_e64 v7, 0, v23, s[38:39]\n"
"v_cndmask_b32_e64 v8, 0, v20, s[40:41]\n"
"v_cndmask_b32_e64 v9, 0, v21, s[40:41]\n"
"v_cndmask_b32_e64 v10, 0, v22, s[40:41]\n"
"v_cndmask_b32_e64 v11, 0, v23, s[40:41]\n"
"v_cndmask_b32_e64 v12, 0, v20, s[42:43]\n"
"v_cndmask_b32_e64 v13, 0, v21, s[42:43]\n"
"v_cndmask_b32_e64 v14, 0, v22, s[42:43]\n"
"v_cndmask_b32_e64 v15, 0, v23, s[42:43]\n"
"v_cndmask_b32_e64 v16, 0, v20, s[44:45]\n"
"v_cndmask_b32_e64 v17, 0, v21, s[44:45]\n"
"v_cndmask_b32_e64 v18, 0, v22, s[44:45]\n"
"v_cndmask_b32_e64 v19, 0, v23, s[44:45]\n"
"s_sub_u32 s56, s56, 16384\n"
"s_mov_b32 s29, s28\n"
"s_waitcnt vmcnt(53)\n"
"v_add_u32 v47, s29, v1\n"
"ds_read_b128 v[28:31], v47\n"
"s_add_u32 s29, s29, 2048\n"
"s_cmp_eq_u32 s29, s79\n"
"s_cselect_b32 s29, s25, s29\n"
"s_waitcnt vmcnt(52)\n"
"ds_read_b128 v[36:39], v47 offset:1024\n"
"s_waitcnt vmcnt(29)\n"
"v_mfma_f32_16x16x32_bf16 v[20:23], a[0:3], v[4:7], 0\n"
"s_waitcnt vmcnt(28)\n"
"v_mfma_f32_16x16x32_bf16 v[20:23], a[4:7], v[8:11], v[20:23]\n"
"s_waitcnt lgkmcnt(1)\n"
"v_mfma_f32_16x16x32_bf16 v[20:23], v[28:31], v[12:15], v[20:23]\n"
"s_waitcnt vmcnt(51)\n"
"v_add_u32 v47, s29, v1\n"
"ds_read_b128 v[28:31], v47\n"
"s_add_u32 s29, s29, 2048\n"
"s_cmp_eq_u32 s29, s79\n"
"s_cselect_b32 s29, s25, s29\n"
"s_waitcnt lgkmcnt(1)\n"
"v_mfma_f32_16x16x32_bf16 v[20:23], v[36:39], v[16:19], v[20:23]\n"
"s_waitcnt vmcnt(50)\n"
"ds_read_b128 v[36:39], v47 offset:1024\n"
"s_add_u32 s20, s18, 200704\n"
"s_addc_u32 s21, s19, 0\n"
"global_load_dwordx4 a[112:115], v1, s[20:21] offset:0 nt\n"
"s_waitcnt vmcnt(28)\n"
"v_mfma_f32_16x16x32_bf16 v[24:27], a[8:11], v[4:7], 0\n"
"s_waitcnt vmcnt(27)\n"
"v_mfma_f32_16x16x32_bf16 v[24:27], a[12:15], v[8:11], v[24:27]\n"
"s_waitcnt lgkmcnt(1)\n"
"v_mfma_f32_16x16x32_bf16 v[24:27], v[28:31], v[12:15], v[24:27]\n"
"s_waitcnt vmcnt(50)\n"
"v_add_u32 v47, s29, v1\n"
"ds_read_b128 v[28:31], v47\n"
"s_add_u32 s29, s29, 2048\n"
"s_cmp_eq_u32 s29, s79\n"
"s_cselect_b32 s29, s25, s29\n"
"s_waitcnt lgkmcnt(1)\n"
"v_mfma_f32_16x16x32_bf16 v[24:27], v[36:39], v[16:19], v[24:27]\n"
"s_waitcnt vmcnt(49)\n"
"ds_read_b128 v[36:39], v47 offset:1024\n"
"v_cndmask_b32_e64 v2, v20, v21, s[34:35]\n"
"v_cndmask_b32_e64 v3, v22, v23, s[34:35]\n"
"v_cndmask_b32_e64 v46, v2, v3, s[36:37]\n"
"s_add_u32 s20, s18, 229376\n"
"s_addc_u32 s21, s19, 0\n"
"global_load_dwordx4 a[116:119], v1, s[20:21] offset:0 nt\n"
"s_waitcnt vmcnt(27)\n"
"v_mfma_f32_16x16x32_bf16 v[20:23], a[16:19], v[4:7], 0\n"
"s_waitcnt vmcnt(26)\n"
"v_mfma_f32_16x16x32_bf16 v[20:23], a[20:23], v[8:11], v[20:23]\n"
"s_waitcnt lgkmcnt(1)\n"
"v_mfma_f32_16x16x32_bf16 v[20:23], v[28:31], v[12:15], v[20:23]\n"
"s_waitcnt vmcnt(49)\n"
"v_add_u32 v47, s29, v1\n"
"ds_read_b128 v[28:31], v47\n"
"s_add_u32 s29, s29, 2048\n"
"s_cmp_eq_u32 s29, s79\n"
"s_cselect_b32 s29, s25, s29\n"
"s_waitcnt lgkmcnt(1)\n"
"v_mfma_f32_16x16x32_bf16 v[20:23], v[36:39], v[16:19], v[20:23]\n"
"s_waitcnt vmcnt(48)\n"
"ds_read_b128 v[36:39], v47 offset:1024\n"
"v_cndmask_b32_e64 v2, v24, v25, s[34:35]\n"
"v_cndmask_b32_e64 v3, v26, v27, s[34:35]\n"
"v_cndmask_b32_e64 v2, v2, v3, s[36:37]\n"
"v_cndmask_b32_e64 v3, v2, v46, s[34:35]\n"
"v_cndmask_b32_e64 v46, v46, v2, s[34:35]\n"
"s_nop 0\n"
"v_add_f32_dpp v43, v3, v46 quad_perm:[1,0,3,2] row_mask:0xf bank_mask:0xf\n"
"s_add_u32 s20, s18, 258048\n"
"s_addc_u32 s21, s19, 0\n"
"global_load_dwordx4 a[120:123], v1, s[20:21] offset:0 nt\n"
"s_waitcnt vmcnt(26)\n"
"v_mfma_f32_16x16x32_bf16 v[24:27], a[24:27], v[4:7], 0\n"
"s_waitcnt vmcnt(25)\n"
"v_mfma_f32_16x16x32_bf16 v[24:27], a[28:31], v[8:11], v[24:27]\n"
"s_waitcnt lgkmcnt(1)\n"
"v_mfma_f32_16x16x32_bf16 v[24:27], v[28:31], v[12:15], v[24:27]\n"
"s_waitcnt vmcnt(48)\n"
"v_add_u32 v47, s29, v1\n"
"ds_read_b128 v[28:31], v47\n"
"s_add_u32 s29, s29, 2048\n"
"s_cmp_eq_u32 s29, s79\n"
"s_cselect_b32 s29, s25, s29\n"
"s_waitcnt lgkmcnt(1)\n"
"v_mfma_f32_16x16x32_bf16 v[24:27], v[36:39], v[16:19], v[24:27]\n"
"s_waitcnt vmcnt(39)\n"
"ds_read_b128 v[36:39], v47 offset:1024\n"
"v_cndmask_b32_e64 v2, v20, v21, s[34:35]\n"
"v_cndmask_b32_e64 v3, v22, v23, s[34:35]\n"
"v_cndmask_b32_e64 v46, v2, v3, s[36:37]\n"
"s_add_u32 s20, s18, 286720\n"
"s_addc_u32 s21, s19, 0\n"
"global_load_dwordx4 a[124:127], v1, s[20:21] offset:0 nt\n"
"s_waitcnt vmcnt(25)\n"
"v_mfma_f32_16x16x32_bf16 v[20:23], a[32:35], v[4:7], 0\n"
"s_waitcnt vmcnt(24)\n"
"v_mfma_f32_16x16x32_bf16 v[20:23], a[36:39], v[8:11], v[20:23]\n"
"s_waitcnt lgkmcnt(1)\n"
"v_mfma_f32_16x16x32_bf16 v[20:23], v[28:31], v[12:15], v[20:23]\n"
"s_waitcnt vmcnt(39)\n"
"v_add_u32 v47, s29, v1\n"
"ds_read_b128 v[28:31], v47\n"
"s_add_u32 s29, s29, 2048\n"
"s_cmp_eq_u32 s29, s79\n"
"s_cselect_b32 s29, s25, s29\n"
"s_waitcnt lgkmcnt(1)\n"
"v_mfma_f32_16x16x32_bf16 v[20:23], v[36:39], v[16:19], v[20:23]\n"
"s_waitcnt vmcnt(38)\n"
"ds_read_b128 v[36:39], v47 offset:1024\n"
"v_cndmask_b32_e64 v2, v24, v25, s[34:35]\n"
"v_cndmask_b32_e64 v3, v26, v27, s[34:35]\n"
"v_cndmask_b32_e64 v2, v2, v3, s[36:37]\n"
"v_cndmask_b32_e64 v3, v2, v46, s[34:35]\n"
"v_cndmask_b32_e64 v46, v46, v2, s[34:35]\n"
"s_nop 0\n"
"v_add_f32_dpp v46, v3, v46 quad_perm:[1,0,3,2] row_mask:0xf bank_mask:0xf\n"
"v_cndmask_b32_e64 v2, v46, v43, s[36:37]\n"
"v_cndmask_b32_e64 v3, v43, v46, s[36:37]\n"
"s_nop 0\n"
"v_add_f32_dpp v43, v2, v3 quad_perm:[2,3,0,1] row_mask:0xf bank_mask:0xf\n"
"s_add_u32 s20, s18, 315392\n"
"s_addc_u32 s21, s19, 0\n"
"global_load_dwordx4 a[128:131], v1, s[20:21] offset:0 nt\n"
"s_waitcnt vmcnt(24)\n"
"v_mfma_f32_16x16x32_bf16 v[24:27], a[40:43], v[4:7], 0\n"
"s_waitcnt vmcnt(23)\n"
"v_mfma_f32_16x16x32_bf16 v[24:27], a[44:47], v[8:11], v[24:27]\n"
"s_waitcnt lgkmcnt(1)\n"
"v_mfma_f32_16x16x32_bf16 v[24:27], v[28:31], v[12:15], v[24:27]\n"
"s_waitcnt vmcnt(38)\n"
"v_add_u32 v47, s29, v1\n"
"ds_read_b128 v[28:31], v47\n"
"s_add_u32 s29, s29, 2048\n"
"s_cmp_eq_u32 s29, s79\n"
"s_cselect_b32 s29, s25, s29\n"
"s_waitcnt lgkmcnt(1)\n"
"v_mfma_f32_16x16x32_bf16 v[24:27], v[36:39], v[16:19], v[24:27]\n"
"s_waitcnt vmcnt(37)\n"
"ds_read_b128 v[36:39], v47 offset:1024\n"
"v_cndmask_b32_e64 v2, v20, v21, s[34:35]\n"
"v_cndmask_b32_e64 v3, v22, v23, s[34:35]\n"
"v_cndmask_b32_e64 v46, v2, v3, s[36:37]\n"
"s_add_u32 s20, s18, 344064\n"
"s_addc_u32 s21, s19, 0\n"
"global_load_dwordx4 a[132:135], v1, s[20:21] offset:0 nt\n"
"s_waitcnt vmcnt(23)\n"
"v_mfma_f32_16x16x32_bf16 v[20:23], a[48:51], v[4:7], 0\n"
"s_waitcnt vmcnt(22)\n"
"v_mfma_f32_16x16x32_bf16 v[20:23], a[52:55], v[8:11], v[20:23]\n"
"s_waitcnt lgkmcnt(1)\n"
"v_mfma_f32_16x16x32_bf16 v[20:23], v[28:31], v[12:15], v[20:23]\n"
"s_waitcnt vmcnt(37)\n"
"v_add_u32 v47, s29, v1\n"
"ds_read_b128 v[28:31], v47\n"
"s_add_u32 s29, s29, 2048\n"
"s_cmp_eq_u32 s29, s79\n"
"s_cselect_b32 s29, s25, s29\n"
"s_waitcnt lgkmcnt(1)\n"
"v_mfma_f32_16x16x32_bf16 v[20:23], v[36:39], v[16:19], v[20:23]\n"
"s_waitcnt vmcnt(36)\n"
"ds_read_b128 v[36:39], v47 offset:1024\n"
"v_cndmask_b32_e64 v2, v24, v25, s[34:35]\n"
"v_cndmask_b32_e64 v3, v26, v27, s[34:35]\n"
"v_cndmask_b32_e64 v2, v2, v3, s[36:37]\n"
"v_cndmask_b32_e64 v3, v2, v46, s[34:35]\n"
"v_cndmask_b32_e64 v46, v46, v2, s[34:35]\n"
"s_nop 0\n"
"v_add_f32_dpp v44, v3, v46 quad_perm:[1,0,3,2] row_mask:0xf bank_mask:0xf\n"
"s_add_u32 s20, s18, 372736\n"
"s_addc_u32 s21, s19, 0\n"
"global_load_dwordx4 a[136:139], v1, s[20:21] offset:0 nt\n"
"s_waitcnt vmcnt(22)\n"
"v_mfma_f32_16x16x32_bf16 v[24:27], a[56:59], v[4:7], 0\n"
"s_waitcnt vmcnt(21)\n"
"v_mfma_f32_16x16x32_bf16 v[24:27], a[60:63], v[8:11], v[24:27]\n"
"s_waitcnt lgkmcnt(1)\n"
"v_mfma_f32_16x16x32_bf16 v[24:27], v[28:31], v[12:15], v[24:27]\n"
"s_waitcnt lgkmcnt(0)\n"
"v_mfma_f32_16x16x32_bf16 v[24:27], v[36:39], v[16:19], v[24:27]\n"
"v_cndmask_b32_e64 v2, v20, v21, s[34:35]\n"
"v_cndmask_b32_e64 v3, v22, v23, s[34:35]\n"
"v_cndmask_b32_e64 v46, v2, v3, s[36:37]\n"
"s_add_u32 s20, s18, 401408\n"
"s_addc_u32 s21, s19, 0\n"
"global_load_dwordx4 a[140:143], v1, s[20:21] offset:0 nt\n"
"s_waitcnt vmcnt(21)\n"
"v_mfma_f32_16x16x32_bf16 v[20:23], a[64:67], v[4:7], 0\n"
"s_waitcnt vmcnt(20)\n"
"v_mfma_f32_16x16x32_bf16 v[20:23], a[68:71], v[8:11], v[20:23]\n"
"s_waitcnt vmcnt(19)\n"
"v_mfma_f32_16x16x32_bf16 v[20:23], a[72:75], v[12:15], v[20:23]\n"
"s_waitcnt vmcnt(16)\n"
"v_mfma_f32_16x16x32_bf16 v[20:23], a[76:79], v[16:19], v[20:23]\n"
"v_cndmask_b32_e64 v2, v24, v25, s[34:35]\n"
"v_cndmask_b32_e64 v3, v26, v27, s[34:35]\n"
"v_cndmask_b32_e64 v2, v2, v3, s[36:37]\n"
"v_cndmask_b32_e64 v3, v2, v46, s[34:35]\n"
"v_cndmask_b32_e64 v46, v46, v2, s[34:35]\n"
"s_nop 0\n"
"v_add_f32_dpp v46, v3, v46 quad_perm:[1,0,3,2] row_mask:0xf bank_mask:0xf\n"
"v_cndmask_b32_e64 v2, v46, v44, s[36:37]\n"
"v_cndmask_b32_e64 v3, v44, v46, s[36:37]\n"
"s_nop 0\n"
"v_add_f32_dpp v44, v2, v3 quad_perm:[2,3,0,1] row_mask:0xf bank_mask:0xf\n"
"s_waitcnt vmcnt(15)\n"
"v_mfma_f32_16x16x32_bf16 v[24:27], a[80:83], v[4:7], 0\n"
"v_cndmask_b32_e64 v2, v20, v21, s[34:35]\n"
"v_cndmask_b32_e64 v3, v22, v23, s[34:35]\n"
"v_cndmask_b32_e64 v46, v2, v3, s[36:37]\n"
"s_nop 4\n"
"v_cndmask_b32_e64 v2, v24, v25, s[34:35]\n"
"v_cndmask_b32_e64 v3, v26, v27, s[34:35]\n"
"v_cndmask_b32_e64 v2, v2, v3, s[36:37]\n"
"v_cndmask_b32_e64 v3, v2, v46, s[34:35]\n"
"v_cndmask_b32_e64 v46, v46, v2, s[34:35]\n"
"s_nop 0\n"
"v_add_f32_dpp v45, v3, v46 quad_perm:[1,0,3,2] row_mask:0xf bank_mask:0xf\n"
"v_cndmask_b32_e64 v2, v46, v45, s[36:37]\n"
"v_cndmask_b32_e64 v3, v45, v46, s[36:37]\n"
"s_nop 0\n"
"v_add_f32_dpp v45, v2, v3 quad_perm:[2,3,0,1] row_mask:0xf bank_mask:0xf\n"
"v_and_b32 v47, 0xff, v0\n"
"v_lshl_add_u32 v47, v47, 1, s56\n"
"global_load_dwordx4 v[4:7], v47, s[22:23] sc1\n"
"global_load_dwordx4 v[8:11], v47, s[22:23] offset:16 sc1\n"
"v_mov_b32 v2, 0xff800000\n"
"v_cndmask_b32_e64 v45, v2, v45, s[46:47]\n"
"v_max3_f32 v2, v43, v44, v45\n"
"s_nop 1\n"
"v_max_f32_dpp v2, v2, v2 quad_perm:[1,0,3,2] row_mask:0xf bank_mask:0xf\n"
"s_nop 1\n"
"v_max_f32_dpp v2, v2, v2 quad_perm:[2,3,0,1] row_mask:0xf bank_mask:0xf\n"
"s_nop 1\n"
"v_max_f32_dpp v2, v2, v2 row_half_mirror row_mask:0xf bank_mask:0xf\n"
"s_nop 1\n"
"v_max_f32_dpp v2, v2, v2 row_mirror row_mask:0xf bank_mask:0xf\n"
"v_mov_b32 v3, v2\n"
"s_nop 1\n"
"v_permlane16_swap_b32 v3, v2\n"
"v_max_f32 v2, v2, v3\n"
"v_mov_b32 v3, v2\n"
"s_nop 1\n"
"v_permlane32_swap_b32 v3, v2\n"
"v_max_f32 v2, v2, v3\n"
"v_mul_f32 v3, s64, v2\n"
"v_mul_f32 v2, s65, v2\n"
"v_readfirstlane_b32 s54, v3\n"
"v_fma_f32 v43, v43, s64, v2\n"
"v_fma_f32 v44, v44, s64, v2\n"
"v_fma_f32 v45, v45, s64, v2\n"
"v_exp_f32 v43, v43\n"
"v_exp_f32 v44, v44\n"
"v_exp_f32 v45, v45\n"
"v_mov_b32 v3, 0xffff\n"
"v_not_b32 v47, v3\n"
"v_cndmask_b32_e64 v3, v3, v47, s[34:35]\n"
"v_cndmask_b32_e64 v28, 0, v3, s[80:81]\n"
"v_cndmask_b32_e64 v29, 0, v3, s[82:83]\n"
"v_cndmask_b32_e64 v30, 0, v3, s[84:85]\n"
"v_cndmask_b32_e64 v31, 0, v3, s[86:87]\n"
"v_cvt_pk_bf16_f32 v43, v43, v43\n"
"v_cvt_pk_bf16_f32 v44, v44, v44\n"
"v_cvt_pk_bf16_f32 v45, v45, v45\n"
"v_and_b32 v2, 0xffff0000, v43\n"
"v_and_b32 v3, 0xffff0000, v44\n"
"v_add_f32 v2, v2, v3\n"
"v_and_b32 v3, 0xffff0000, v45\n"
"v_add_f32 v2, v2, v3\n"
"s_nop 1\n"
"v_add_f32_dpp v2, v2, v2 quad_perm:[1,0,3,2] row_mask:0xf bank_mask:0xf\n"
"s_nop 1\n"
"v_add_f32_dpp v2, v2, v2 quad_perm:[2,3,0,1] row_mask:0xf bank_mask:0xf\n"
"s_nop 1\n"
"v_add_f32_dpp v2, v2, v2 row_half_mirror row_mask:0xf bank_mask:0xf\n"
"s_nop 1\n"
"v_add_f32_dpp v2, v2, v2 row_mirror row_mask:0xf bank_mask:0xf\n"
"v_mov_b32 v3, v2\n"
"s_nop 1\n"
"v_permlane16_swap_b32 v3, v2\n"
"v_add_f32 v2, v2, v3\n"
"v_mov_b32 v3, v2\n"
"s_nop 1\n"
"v_permlane32_swap_b32 v3, v2\n"
"v_add_f32 v2, v2, v3\n"
"s_nop 0\n"
"v_readfirstlane_b32 s55, v2\n"
"v_and_b32_dpp v20, v43, v28 row_newbcast:8 row_mask:0xf bank_mask:0xf\n"
"v_and_b32_dpp v21, v43, v29 row_newbcast:8 row_mask:0xf bank_mask:0xf\n"
"v_and_b32_dpp v22, v43, v30 row_newbcast:8 row_mask:0xf bank_mask:0xf\n"
"v_and_b32_dpp v23, v43, v31 row_newbcast:8 row_mask:0xf bank_mask:0xf\n"
"v_and_b32_dpp v24, v43, v28 row_newbcast:12 row_mask:0xf bank_mask:0xf\n"
"v_and_b32_dpp v25, v43, v29 row_newbcast:12 row_mask:0xf bank_mask:0xf\n"
"v_and_b32_dpp v26, v43, v30 row_newbcast:12 row_mask:0xf bank_mask:0xf\n"
"v_and_b32_dpp v27, v43, v31 row_newbcast:12 row_mask:0xf bank_mask:0xf\n"
"s_waitcnt vmcnt(16)\n"
"v_mfma_f32_16x16x32_bf16 v[36:39], a[84:87], v[20:23], 0\n"
"v_and_b32_dpp v20, v43, v28 row_newbcast:9 row_mask:0xf bank_mask:0xf\n"
"v_and_b32_dpp v21, v43, v29 row_newbcast:9 row_mask:0xf bank_mask:0xf\n"
"v_and_b32_dpp v22, v43, v30 row_newbcast:9 row_mask:0xf bank_mask:0xf\n"
"v_and_b32_dpp v23, v43, v31 row_newbcast:9 row_mask:0xf bank_mask:0xf\n"
"s_waitcnt vmcnt(15)\n"
"v_mfma_f32_16x16x32_bf16 v[36:39], a[88:91], v[24:27], v[36:39]\n"
"v_and_b32_dpp v24, v43, v28 row_newbcast:13 row_mask:0xf bank_mask:0xf\n"
"v_and_b32_dpp v25, v43, v29 row_newbcast:13 row_mask:0xf bank_mask:0xf\n"
"v_and_b32_dpp v26, v43, v30 row_newbcast:13 row_mask:0xf bank_mask:0xf\n"
"v_and_b32_dpp v27, v43, v31 row_newbcast:13 row_mask:0xf bank_mask:0xf\n"
"s_waitcnt vmcnt(14)\n"
"v_mfma_f32_16x16x32_bf16 v[36:39], a[92:95], v[20:23], v[36:39]\n"
"v_and_b32_dpp v20, v43, v28 row_newbcast:10 row_mask:0xf bank_mask:0xf\n"
"v_and_b32_dpp v21, v43, v29 row_newbcast:10 row_mask:0xf bank_mask:0xf\n"
"v_and_b32_dpp v22, v43, v30 row_newbcast:10 row_mask:0xf bank_mask:0xf\n"
"v_and_b32_dpp v23, v43, v31 row_newbcast:10 row_mask:0xf bank_mask:0xf\n"
"s_waitcnt vmcnt(13)\n"
"v_mfma_f32_16x16x32_bf16 v[36:39], a[96:99], v[24:27], v[36:39]\n"
"v_and_b32_dpp v24, v43, v28 row_newbcast:14 row_mask:0xf bank_mask:0xf\n"
"v_and_b32_dpp v25, v43, v29 row_newbcast:14 row_mask:0xf bank_mask:0xf\n"
"v_and_b32_dpp v26, v43, v30 row_newbcast:14 row_mask:0xf bank_mask:0xf\n"
"v_and_b32_dpp v27, v43, v31 row_newbcast:14 row_mask:0xf bank_mask:0xf\n"
"s_waitcnt vmcnt(12)\n"
"v_mfma_f32_16x16x32_bf16 v[36:39], a[100:103], v[20:23], v[36:39]\n"
"v_and_b32_dpp v20, v43, v28 row_newbcast:11 row_mask:0xf bank_mask:0xf\n"
"v_and_b32_dpp v21, v43, v29 row_newbcast:11 row_mask:0xf bank_mask:0xf\n"
"v_and_b32_dpp v22, v43, v30 row_newbcast:11 row_mask:0xf bank_mask:0xf\n"
"v_and_b32_dpp v23, v43, v31 row_newbcast:11 row_mask:0xf bank_mask:0xf\n"
"s_waitcnt vmcnt(11)\n"
"v_mfma_f32_16x16x32_bf16 v[36:39], a[104:107], v[24:27], v[36:39]\n"
"v_and_b32_dpp v24, v43, v28 row_newbcast:15 row_mask:0xf bank_mask:0xf\n"
"v_and_b32_dpp v25, v43, v29 row_newbcast:15 row_mask:0xf bank_mask:0xf\n"
"v_and_b32_dpp v26, v43, v30 row_newbcast:15 row_mask:0xf bank_mask:0xf\n"
"v_and_b32_dpp v27, v43, v31 row_newbcast:15 row_mask:0xf bank_mask:0xf\n"
"s_waitcnt vmcnt(10)\n"
"v_mfma_f32_16x16x32_bf16 v[36:39], a[108:111], v[20:23], v[36:39]\n"
"v_and_b32_dpp v20, v44, v28 row_newbcast:8 row_mask:0xf bank_mask:0xf\n"
"v_and_b32_dpp v21, v44, v29 row_newbcast:8 row_mask:0xf bank_mask:0xf\n"
"v_and_b32_dpp v22, v44, v30 row_newbcast:8 row_mask:0xf bank_mask:0xf\n"
"v_and_b32_dpp v23, v44, v31 row_newbcast:8 row_mask:0xf bank_mask:0xf\n"
"s_waitcnt vmcnt(9)\n"
"v_mfma_f32_16x16x32_bf16 v[36:39], a[112:115], v[24:27], v[36:39]\n"
"v_and_b32_dpp v24, v44, v28 row_newbcast:12 row_mask:0xf bank_mask:0xf\n"
"v_and_b32_dpp v25, v44, v29 row_newbcast:12 row_mask:0xf bank_mask:0xf\n"
"v_and_b32_dpp v26, v44, v30 row_newbcast:12 row_mask:0xf bank_mask:0xf\n"
"v_and_b32_dpp v27, v44, v31 row_newbcast:12 row_mask:0xf bank_mask:0xf\n"
"s_waitcnt vmcnt(8)\n"
"v_mfma_f32_16x16x32_bf16 v[36:39], a[116:119], v[20:23], v[36:39]\n"
"s_add_u32 s20, s18, 430080\n"
"s_addc_u32 s21, s19, 0\n"
"global_load_dwordx4 a[144:147], v1, s[20:21] offset:0 nt\n"
"s_add_u32 s20, s18, 458752\n"
"s_addc_u32 s21, s19, 0\n"
"global_load_dwordx4 a[148:151], v1, s[20:21] offset:0 nt\n"
"v_and_b32_dpp v20, v44, v28 row_newbcast:9 row_mask:0xf bank_mask:0xf\n"
"v_and_b32_dpp v21, v44, v29 row_newbcast:9 row_mask:0xf bank_mask:0xf\n"
"v_and_b32_dpp v22, v44, v30 row_newbcast:9 row_mask:0xf bank_mask:0xf\n"
"v_and_b32_dpp v23, v44, v31 row_newbcast:9 row_mask:0xf bank_mask:0xf\n"
"s_waitcnt vmcnt(9)\n"
"v_mfma_f32_16x16x32_bf16 v[36:39], a[120:123], v[24:27], v[36:39]\n"
"s_add_u32 s20, s18, 487424\n"
"s_addc_u32 s21, s19, 0\n"
"global_load_dwordx4 a[152:155], v1, s[20:21] offset:0 nt\n"
"v_and_b32_dpp v24, v44, v28 row_newbcast:13 row_mask:0xf bank_mask:0xf\n"
"v_and_b32_dpp v25, v44, v29 row_newbcast:13 row_mask:0xf bank_mask:0xf\n"
"v_and_b32_dpp v26, v44, v30 row_newbcast:13 row_mask:0xf bank_mask:0xf\n"
"v_and_b32_dpp v27, v44, v31 row_newbcast:13 row_mask:0xf bank_mask:0xf\n"
"s_waitcnt vmcnt(9)\n"
"v_mfma_f32_16x16x32_bf16 v[36:39], a[124:127], v[20:23], v[36:39]\n"
"s_add_u32 s20, s18, 516096\n"
"s_addc_u32 s21, s19, 0\n"
"global_load_dwordx4 a[156:159], v1, s[20:21] offset:0 nt\n"
"v_and_b32_dpp v20, v44, v28 row_newbcast:10 row_mask:0xf bank_mask:0xf\n"
"v_and_b32_dpp v21, v44, v29 row_newbcast:10 row_mask:0xf bank_mask:0xf\n"
"v_and_b32_dpp v22, v44, v30 row_newbcast:10 row_mask:0xf bank_mask:0xf\n"
"v_and_b32_dpp v23, v44, v31 row_newbcast:10 row_mask:0xf bank_mask:0xf\n"
"s_waitcnt vmcnt(9)\n"
"v_mfma_f32_16x16x32_bf16 v[36:39], a[128:131], v[24:27], v[36:39]\n"
"s_add_u32 s20, s18, 544768\n"
"s_addc_u32 s21, s19, 0\n"
"global_load_dwordx4 a[160:163], v1, s[20:21] offset:0 nt\n"
"v_and_b32_dpp v24, v44, v28 row_newbcast:14 row_mask:0xf bank_mask:0xf\n"
"v_and_b32_dpp v25, v44, v29 row_newbcast:14 row_mask:0xf bank_mask:0xf\n"
"v_and_b32_dpp v26, v44, v30 row_newbcast:14 row_mask:0xf bank_mask:0xf\n"
"v_and_b32_dpp v27, v44, v31 row_newbcast:14 row_mask:0xf bank_mask:0xf\n"
"s_waitcnt vmcnt(9)\n"
"v_mfma_f32_16x16x32_bf16 v[36:39], a[132:135], v[20:23], v[36:39]\n"
"s_add_u32 s20, s18, 573440\n"
"s_addc_u32 s21, s19, 0\n"
"global_load_dwordx4 a[164:167], v1, s[20:21] offset:0 nt\n"
"v_and_b32_dpp v20, v44, v28 row_newbcast:11 row_mask:0xf bank_mask:0xf\n"
"v_and_b32_dpp v21, v44, v29 row_newbcast:11 row_mask:0xf bank_mask:0xf\n"
"v_and_b32_dpp v22, v44, v30 row_newbcast:11 row_mask:0xf bank_mask:0xf\n"
"v_and_b32_dpp v23, v44, v31 row_newbcast:11 row_mask:0xf bank_mask:0xf\n"
"s_waitcnt vmcnt(9)\n"
"v_mfma_f32_16x16x32_bf16 v[36:39], a[136:139], v[24:27], v[36:39]\n"
"s_add_u32 s20, s18, 602112\n"
"s_addc_u32 s21, s19, 0\n"
"global_load_dwordx4 a[168:171], v1, s[20:21] offset:0 nt\n"
"v_and_b32_dpp v24, v44, v28 row_newbcast:15 row_mask:0xf bank_mask:0xf\n"
"v_and_b32_dpp v25, v44, v29 row_newbcast:15 row_mask:0xf bank_mask:0xf\n"
"v_and_b32_dpp v26, v44, v30 row_newbcast:15 row_mask:0xf bank_mask:0xf\n"
"v_and_b32_dpp v27, v44, v31 row_newbcast:15 row_mask:0xf bank_mask:0xf\n"
"s_waitcnt vmcnt(9)\n"
"v_mfma_f32_16x16x32_bf16 v[36:39], a[140:143], v[20:23], v[36:39]\n"
"s_add_u32 s20, s18, 630784\n"
"s_addc_u32 s21, s19, 0\n"
"global_load_dwordx4 a[172:175], v1, s[20:21] offset:0 nt\n"
"v_and_b32_dpp v20, v43, v28 row_newbcast:0 row_mask:0xf bank_mask:0xf\n"
"v_and_b32_dpp v21, v43, v29 row_newbcast:0 row_mask:0xf bank_mask:0xf\n"
"v_and_b32_dpp v22, v43, v30 row_newbcast:0 row_mask:0xf bank_mask:0xf\n"
"v_and_b32_dpp v23, v43, v31 row_newbcast:0 row_mask:0xf bank_mask:0xf\n"
"s_waitcnt vmcnt(7)\n"
"v_mfma_f32_16x16x32_bf16 v[36:39], a[144:147], v[24:27], v[36:39]\n"
"s_add_u32 s20, s18, 659456\n"
"s_addc_u32 s21, s19, 0\n"
"global_load_dwordx4 a[176:179], v1, s[20:21] offset:0 nt\n"
"v_and_b32_dpp v24, v43, v28 row_newbcast:4 row_mask:0xf bank_mask:0xf\n"
"v_and_b32_dpp v25, v43, v29 row_newbcast:4 row_mask:0xf bank_mask:0xf\n"
"v_and_b32_dpp v26, v43, v30 row_newbcast:4 row_mask:0xf bank_mask:0xf\n"
"v_and_b32_dpp v27, v43, v31 row_newbcast:4 row_mask:0xf bank_mask:0xf\n"
"s_waitcnt vmcnt(7)\n"
"v_mfma_f32_16x16x32_bf16 v[36:39], a[148:151], v[20:23], v[36:39]\n"
"s_add_u32 s20, s18, 688128\n"
"s_addc_u32 s21, s19, 0\n"
"global_load_dwordx4 a[180:183], v1, s[20:21] offset:0 nt\n"
"v_and_b32_dpp v20, v43, v28 row_newbcast:1 row_mask:0xf bank_mask:0xf\n"
"v_and_b32_dpp v21, v43, v29 row_newbcast:1 row_mask:0xf bank_mask:0xf\n"
"v_and_b32_dpp v22, v43, v30 row_newbcast:1 row_mask:0xf bank_mask:0xf\n"
"v_and_b32_dpp v23, v43, v31 row_newbcast:1 row_mask:0xf bank_mask:0xf\n"
"s_waitcnt vmcnt(7)\n"
"v_mfma_f32_16x16x32_bf16 v[36:39], a[152:155], v[24:27], v[36:39]\n"
"s_add_u32 s20, s18, 716800\n"
"s_addc_u32 s21, s19, 0\n"
"global_load_dwordx4 a[184:187], v1, s[20:21] offset:0 nt\n"
"v_and_b32_dpp v24, v43, v28 row_newbcast:5 row_mask:0xf bank_mask:0xf\n"
"v_and_b32_dpp v25, v43, v29 row_newbcast:5 row_mask:0xf bank_mask:0xf\n"
"v_and_b32_dpp v26, v43, v30 row_newbcast:5 row_mask:0xf bank_mask:0xf\n"
"v_and_b32_dpp v27, v43, v31 row_newbcast:5 row_mask:0xf bank_mask:0xf\n"
"s_waitcnt vmcnt(7)\n"
"v_mfma_f32_16x16x32_bf16 v[36:39], a[156:159], v[20:23], v[36:39]\n"
"s_add_u32 s20, s18, 745472\n"
"s_addc_u32 s21, s19, 0\n"
"global_load_dwordx4 a[188:191], v1, s[20:21] offset:0 nt\n"
"v_and_b32_dpp v20, v43, v28 row_newbcast:2 row_mask:0xf bank_mask:0xf\n"
"v_and_b32_dpp v21, v43, v29 row_newbcast:2 row_mask:0xf bank_mask:0xf\n"
"v_and_b32_dpp v22, v43, v30 row_newbcast:2 row_mask:0xf bank_mask:0xf\n"
"v_and_b32_dpp v23, v43, v31 row_newbcast:2 row_mask:0xf bank_mask:0xf\n"
"s_waitcnt vmcnt(7)\n"
"v_mfma_f32_16x16x32_bf16 v[36:39], a[160:163], v[24:27], v[36:39]\n"
"s_add_u32 s20, s18, 774144\n"
"s_addc_u32 s21, s19, 0\n"
"global_load_dwordx4 a[192:195], v1, s[20:21] offset:0 nt\n"
"v_and_b32_dpp v24, v43, v28 row_newbcast:6 row_mask:0xf bank_mask:0xf\n"
"v_and_b32_dpp v25, v43, v29 row_newbcast:6 row_mask:0xf bank_mask:0xf\n"
"v_and_b32_dpp v26, v43, v30 row_newbcast:6 row_mask:0xf bank_mask:0xf\n"
"v_and_b32_dpp v27, v43, v31 row_newbcast:6 row_mask:0xf bank_mask:0xf\n"
"s_waitcnt vmcnt(7)\n"
"v_mfma_f32_16x16x32_bf16 v[36:39], a[164:167], v[20:23], v[36:39]\n"
"s_add_u32 s20, s18, 802816\n"
"s_addc_u32 s21, s19, 0\n"
"global_load_dwordx4 a[196:199], v1, s[20:21] offset:0 nt\n"
"v_and_b32_dpp v20, v43, v28 row_newbcast:3 row_mask:0xf bank_mask:0xf\n"
"v_and_b32_dpp v21, v43, v29 row_newbcast:3 row_mask:0xf bank_mask:0xf\n"
"v_and_b32_dpp v22, v43, v30 row_newbcast:3 row_mask:0xf bank_mask:0xf\n"
"v_and_b32_dpp v23, v43, v31 row_newbcast:3 row_mask:0xf bank_mask:0xf\n"
"s_waitcnt vmcnt(7)\n"
"v_mfma_f32_16x16x32_bf16 v[36:39], a[168:171], v[24:27], v[36:39]\n"
"s_add_u32 s20, s18, 831488\n"
"s_addc_u32 s21, s19, 0\n"
"global_load_dwordx4 a[200:203], v1, s[20:21] offset:0 nt\n"
"v_and_b32_dpp v24, v43, v28 row_newbcast:7 row_mask:0xf bank_mask:0xf\n"
"v_and_b32_dpp v25, v43, v29 row_newbcast:7 row_mask:0xf bank_mask:0xf\n"
"v_and_b32_dpp v26, v43, v30 row_newbcast:7 row_mask:0xf bank_mask:0xf\n"
"v_and_b32_dpp v27, v43, v31 row_newbcast:7 row_mask:0xf bank_mask:0xf\n"
"s_waitcnt vmcnt(7)\n"
"v_mfma_f32_16x16x32_bf16 v[36:39], a[172:175], v[20:23], v[36:39]\n"
"s_add_u32 s20, s18, 860160\n"
"s_addc_u32 s21, s19, 0\n"
"global_load_dwordx4 a[204:207], v1, s[20:21] offset:0 nt\n"
"v_and_b32_dpp v20, v44, v28 row_newbcast:0 row_mask:0xf bank_mask:0xf\n"
"v_and_b32_dpp v21, v44, v29 row_newbcast:0 row_mask:0xf bank_mask:0xf\n"
"v_and_b32_dpp v22, v44, v30 row_newbcast:0 row_mask:0xf bank_mask:0xf\n"
"v_and_b32_dpp v23, v44, v31 row_newbcast:0 row_mask:0xf bank_mask:0xf\n"
"s_waitcnt vmcnt(7)\n"
"v_mfma_f32_16x16x32_bf16 v[36:39], a[176:179], v[24:27], v[36:39]\n"
"s_add_u32 s20, s18, 888832\n"
"s_addc_u32 s21, s19, 0\n"
"global_load_dwordx4 a[208:211], v1, s[20:21] offset:0 nt\n"
"v_and_b32_dpp v24, v44, v28 row_newbcast:4 row_mask:0xf bank_mask:0xf\n"
"v_and_b32_dpp v25, v44, v29 row_newbcast:4 row_mask:0xf bank_mask:0xf\n"
"v_and_b32_dpp v26, v44, v30 row_newbcast:4 row_mask:0xf bank_mask:0xf\n"
"v_and_b32_dpp v27, v44, v31 row_newbcast:4 row_mask:0xf bank_mask:0xf\n"
"s_waitcnt vmcnt(7)\n"
"v_mfma_f32_16x16x32_bf16 v[36:39], a[180:183], v[20:23], v[36:39]\n"
"s_add_u32 s20, s18, 917504\n"
"s_addc_u32 s21, s19, 0\n"
"global_load_dwordx4 a[212:215], v1, s[20:21] offset:0 nt\n"
"v_and_b32_dpp v20, v44, v28 row_newbcast:1 row_mask:0xf bank_mask:0xf\n"
"v_and_b32_dpp v21, v44, v29 row_newbcast:1 row_mask:0xf bank_mask:0xf\n"
"v_and_b32_dpp v22, v44, v30 row_newbcast:1 row_mask:0xf bank_mask:0xf\n"
"v_and_b32_dpp v23, v44, v31 row_newbcast:1 row_mask:0xf bank_mask:0xf\n"
"s_waitcnt vmcnt(7)\n"
"v_mfma_f32_16x16x32_bf16 v[36:39], a[184:187], v[24:27], v[36:39]\n"
"s_add_u32 s20, s18, 946176\n"
"s_addc_u32 s21, s19, 0\n"
"global_load_dwordx4 a[216:219], v1, s[20:21] offset:0 nt\n"
"v_and_b32_dpp v24, v44, v28 row_newbcast:5 row_mask:0xf bank_mask:0xf\n"
"v_and_b32_dpp v25, v44, v29 row_newbcast:5 row_mask:0xf bank_mask:0xf\n"
"v_and_b32_dpp v26, v44, v30 row_newbcast:5 row_mask:0xf bank_mask:0xf\n"
"v_and_b32_dpp v27, v44, v31 row_newbcast:5 row_mask:0xf bank_mask:0xf\n"
"s_waitcnt vmcnt(7)\n"
"v_mfma_f32_16x16x32_bf16 v[36:39], a[188:191], v[20:23], v[36:39]\n"
"s_add_u32 s20, s18, 974848\n"
"s_addc_u32 s21, s19, 0\n"
"global_load_dwordx4 a[220:223], v1, s[20:21] offset:0 nt\n"
"v_and_b32_dpp v20, v44, v28 row_newbcast:2 row_mask:0xf bank_mask:0xf\n"
"v_and_b32_dpp v21, v44, v29 row_newbcast:2 row_mask:0xf bank_mask:0xf\n"
"v_and_b32_dpp v22, v44, v30 row_newbcast:2 row_mask:0xf bank_mask:0xf\n"
"v_and_b32_dpp v23, v44, v31 row_newbcast:2 row_mask:0xf bank_mask:0xf\n"
"s_waitcnt vmcnt(7)\n"
"v_mfma_f32_16x16x32_bf16 v[36:39], a[192:195], v[24:27], v[36:39]\n"
"s_add_u32 s20, s18, 1003520\n"
"s_addc_u32 s21, s19, 0\n"
"global_load_dwordx4 a[224:227], v1, s[20:21] offset:0 nt\n"
"v_and_b32_dpp v24, v44, v28 row_newbcast:6 row_mask:0xf bank_mask:0xf\n"
"v_and_b32_dpp v25, v44, v29 row_newbcast:6 row_mask:0xf bank_mask:0xf\n"
"v_and_b32_dpp v26, v44, v30 row_newbcast:6 row_mask:0xf bank_mask:0xf\n"
"v_and_b32_dpp v27, v44, v31 row_newbcast:6 row_mask:0xf bank_mask:0xf\n"
"s_waitcnt vmcnt(7)\n"
"v_mfma_f32_16x16x32_bf16 v[36:39], a[196:199], v[20:23], v[36:39]\n"
"s_add_u32 s20, s18, s93\n"
"s_addc_u32 s21, s19, 0\n"
"s_mov_b64 exec, s[30:31]\n"
"global_load_dwordx4 a[228:231], v1, s[20:21] offset:0 nt\n"
"s_mov_b64 exec, -1\n"
"v_and_b32_dpp v20, v44, v28 row_newbcast:3 row_mask:0xf bank_mask:0xf\n"
"v_and_b32_dpp v21, v44, v29 row_newbcast:3 row_mask:0xf bank_mask:0xf\n"
"v_and_b32_dpp v22, v44, v30 row_newbcast:3 row_mask:0xf bank_mask:0xf\n"
"v_and_b32_dpp v23, v44, v31 row_newbcast:3 row_mask:0xf bank_mask:0xf\n"
"s_waitcnt vmcnt(7)\n"
"v_mfma_f32_16x16x32_bf16 v[36:39], a[200:203], v[24:27], v[36:39]\n"
"s_add_u32 s16, s16, s68\n"
"s_addc_u32 s17, s17, 0\n"
"s_add_u32 s18, s18, s68\n"
"s_addc_u32 s19, s19, 0\n"
"s_add_u32 s20, s16, 0\n"
"s_addc_u32 s21, s17, 0\n"
"s_mov_b32 m0, s26\n"
"s_add_u32 s26, s26, 1024\n"
"s_mov_b64 exec, s[32:33]\n"
"global_load_lds_dwordx4 v0, s[20:21] nt\n"
"s_mov_b64 exec, -1\n"
"v_and_b32_dpp v24, v44, v28 row_newbcast:7 row_mask:0xf bank_mask:0xf\n"
"v_and_b32_dpp v25, v44, v29 row_newbcast:7 row_mask:0xf bank_mask:0xf\n"
"v_and_b32_dpp v26, v44, v30 row_newbcast:7 row_mask:0xf bank_mask:0xf\n"
"v_and_b32_dpp v27, v44, v31 row_newbcast:7 row_mask:0xf bank_mask:0xf\n"
"s_waitcnt vmcnt(7)\n"
"v_mfma_f32_16x16x32_bf16 v[36:39], a[204:207], v[20:23], v[36:39]\n"
"s_add_u32 s20, s16, 28672\n"
"s_addc_u32 s21, s17, 0\n"
"s_mov_b32 m0, s26\n"
"s_add_u32 s26, s26, 1024\n"
"s_cmp_eq_u32 s26, s79\n"
"s_cselect_b32 s26, s25, s26\n"
"s_mov_b64 exec, s[32:33]\n"
"global_load_lds_dwordx4 v0, s[20:21] nt\n"
"s_mov_b64 exec, -1\n"
"v_and_b32_dpp v20, v45, v28 row_newbcast:0 row_mask:0xf bank_mask:0xf\n"
"v_and_b32_dpp v21, v45, v29 row_newbcast:0 row_mask:0xf bank_mask:0xf\n"
"v_and_b32_dpp v22, v45, v30 row_newbcast:0 row_mask:0xf bank_mask:0xf\n"
"v_and_b32_dpp v23, v45, v31 row_newbcast:0 row_mask:0xf bank_mask:0xf\n"
"s_waitcnt vmcnt(7)\n"
"v_mfma_f32_16x16x32_bf16 v[36:39], a[208:211], v[24:27], v[36:39]\n"
"s_add_u32 s20, s16, 57344\n"
"s_addc_u32 s21, s17, 0\n"
"s_mov_b32 m0, s26\n"
"s_add_u32 s26, s26, 1024\n"
"s_mov_b64 exec, s[32:33]\n"
"global_load_lds_dwordx4 v0, s[20:21] nt\n"
"s_mov_b64 exec, -1\n"
"v_and_b32_dpp v24, v45, v28 row_newbcast:4 row_mask:0xf bank_mask:0xf\n"
"v_and_b32_dpp v25, v45, v29 row_newbcast:4 row_mask:0xf bank_mask:0xf\n"
"v_and_b32_dpp v26, v45, v30 row_newbcast:4 row_mask:0xf bank_mask:0xf\n"
"v_and_b32_dpp v27, v45, v31 row_newbcast:4 row_mask:0xf bank_mask:0xf\n"
"s_waitcnt vmcnt(7)\n"
"v_mfma_f32_16x16x32_bf16 v[36:39], a[212:215], v[20:23], v[36:39]\n"
"s_add_u32 s20, s16, 86016\n"
"s_addc_u32 s21, s17, 0\n"
"s_mov_b32 m0, s26\n"
"s_add_u32 s26, s26, 1024\n"
"s_cmp_eq_u32 s26, s79\n"
"s_cselect_b32 s26, s25, s26\n"
"s_mov_b64 exec, s[32:33]\n"
"global_load_lds_dwordx4 v0, s[20:21] nt\n"
"s_mov_b64 exec, -1\n"
"v_and_b32_dpp v20, v45, v28 row_newbcast:8 row_mask:0xf bank_mask:0xf\n"
"v_and_b32_dpp v21, v45, v29 row_newbcast:8 row_mask:0xf bank_mask:0xf\n"
"v_and_b32_dpp v22, v45, v30 row_newbcast:8 row_mask:0xf bank_mask:0xf\n"
"v_and_b32_dpp v23, v45, v31 row_newbcast:8 row_mask:0xf bank_mask:0xf\n"
"s_waitcnt vmcnt(7)\n"
"v_mfma_f32_16x16x32_bf16 v[36:39], a[216:219], v[24:27], v[36:39]\n"
"s_add_u32 s20, s16, 114688\n"
"s_addc_u32 s21, s17, 0\n"
"s_mov_b32 m0, s26\n"
"s_add_u32 s26, s26, 1024\n"
"s_mov_b64 exec, s[32:33]\n"
"global_load_lds_dwordx4 v0, s[20:21] nt\n"
"s_mov_b64 exec, -1\n"
"v_and_b32_dpp v24, v45, v28 row_newbcast:12 row_mask:0xf bank_mask:0xf\n"
"v_and_b32_dpp v25, v45, v29 row_newbcast:12 row_mask:0xf bank_mask:0xf\n"
"v_and_b32_dpp v26, v45, v30 row_newbcast:12 row_mask:0xf bank_mask:0xf\n"
"v_and_b32_dpp v27, v45, v31 row_newbcast:12 row_mask:0xf bank_mask:0xf\n"
"s_waitcnt vmcnt(7)\n"
"v_mfma_f32_16x16x32_bf16 v[36:39], a[220:223], v[20:23], v[36:39]\n"
"s_add_u32 s20, s16, 143360\n"
"s_addc_u32 s21, s17, 0\n"
"s_mov_b32 m0, s26\n"
"s_add_u32 s26, s26, 1024\n"
"s_cmp_eq_u32 s26, s79\n"
"s_cselect_b32 s26, s25, s26\n"
"s_mov_b64 exec, s[32:33]\n"
"global_load_lds_dwordx4 v0, s[20:21] nt\n"
"s_mov_b64 exec, -1\n"
"v_and_b32_dpp v20, v45, v28 row_newbcast:1 row_mask:0xf bank_mask:0xf\n"
"v_and_b32_dpp v21, v45, v29 row_newbcast:1 row_mask:0xf bank_mask:0xf\n"
"v_and_b32_dpp v22, v45, v30 row_newbcast:1 row_mask:0xf bank_mask:0xf\n"
"v_and_b32_dpp v23, v45, v31 row_newbcast:1 row_mask:0xf bank_mask:0xf\n"
"s_waitcnt vmcnt(7)\n"
"v_mfma_f32_16x16x32_bf16 v[36:39], a[224:227], v[24:27], v[36:39]\n"
"s_add_u32 s20, s16, 172032\n"
"s_addc_u32 s21, s17, 0\n"
"s_mov_b32 m0, s26\n"
"s_add_u32 s26, s26, 1024\n"
"s_mov_b64 exec, s[32:33]\n"
"global_load_lds_dwordx4 v0, s[20:21] nt\n"
"s_mov_b64 exec, -1\n"
"s_add_u32 s63, s62, s61\n"
"v_lshrrev_b32 v2, 8, v1\n"
"v_bfe_u32 v3, v1, 4, 4\n"
"v_lshlrev_b32 v2, 5, v2\n"
"v_add_u32 v2, v2, v3\n"
"v_lshl_add_u32 v2, v2, 2, s63\n"
"v_mov_b32 v3, s63\n"
"s_waitcnt vmcnt(7)\n"
"v_mfma_f32_16x16x32_bf16 v[36:39], a[228:231], v[20:23], v[36:39]\n"
"v_mov_b32 v46, s54\n"
"v_mov_b32 v47, s55\n"
"s_mov_b64 exec, s[48:49]\n"
"ds_write_b64 v3, v[46:47]\n"
"s_nop 3\n"
"ds_write_b32 v2, v36 offset:16\n"
"ds_write_b32 v2, v37 offset:48\n"
"ds_write_b32 v2, v38 offset:80\n"
"ds_write_b32 v2, v39 offset:112\n"
"s_mov_b64 exec, -1\n"
"s_add_u32 s63, s62, s78\n"
"v_mov_b32 v3, s62\n"
"v_bfe_u32 v47, v1, 4, 5\n"
"v_lshl_add_u32 v2, v47, 2, s63\n"
"s_waitcnt lgkmcnt(0)\n"
"s_barrier\n"
"ds_read_b64 v[20:21], v3 offset:0\n"
"ds_read_b64 v[22:23], v3 offset:528\n"
"ds_read_b64 v[24:25], v3 offset:1056\n"
"ds_read_b64 v[26:27], v3 offset:1584\n"
"ds_read_b32 v28, v2 offset:0\n"
"ds_read_b32 v29, v2 offset:528\n"
"ds_read_b32 v30, v2 offset:1056\n"
"ds_read_b32 v31, v2 offset:1584\n"
"s_xor_b32 s62, s62, 2112\n"
"s_add_u32 s63, s59, s72\n"
"s_sub_u32 s70, s73, s72\n"
"v_lshl_add_u32 v47, v47, 2, s63\n"
"v_mov_b32 v46, s70\n"
"v_cndmask_b32_e64 v46, 0, v46, s[66:67]\n"
"v_add_u32 v47, v47, v46\n"
"s_waitcnt lgkmcnt(4)\n"
"v_max3_f32 v2, v20, v22, v24\n"
"v_max_f32 v2, v2, v26\n"
"v_sub_f32 v20, v20, v2\n"
"v_sub_f32 v22, v22, v2\n"
"v_sub_f32 v24, v24, v2\n"
"v_sub_f32 v26, v26, v2\n"
"v_exp_f32 v20, v20\n"
"v_exp_f32 v22, v22\n"
"v_exp_f32 v24, v24\n"
"v_exp_f32 v26, v26\n"
"v_mul_f32 v3, v20, v21\n"
"v_fmac_f32 v3, v22, v23\n"
"v_fmac_f32 v3, v24, v25\n"
"v_fmac_f32 v3, v26, v27\n"
"s_waitcnt lgkmcnt(0)\n"
"v_mul_f32 v21, v20, v28\n"
"v_fmac_f32 v21, v22, v29\n"
"v_fmac_f32 v21, v24, v30\n"
"v_fmac_f32 v21, v26, v31\n"
"v_cndmask_b32_e64 v2, v2, v3, s[34:35]\n"
"v_cndmask_b32_e64 v21, v21, v2, s[66:67]\n"
"v_or_b32 v21, 1, v21\n"
"s_mov_b64 exec, s[50:51]\n"
"global_store_dword v47, v21, s[22:23] sc1\n"
"s_mov_b64 exec, -1\n"
"s_waitcnt vmcnt(7)\n"
"s_add_u32 s20, s16, 200704\n"
"s_addc_u32 s21, s17, 0\n"
"s_mov_b32 m0, s26\n"
"s_add_u32 s26, s26, 1024\n"
"s_cmp_eq_u32 s26, s79\n"
"s_cselect_b32 s26, s25, s26\n"
"s_mov_b64 exec, s[32:33]\n"
"global_load_lds_dwordx4 v0, s[20:21] nt\n"
"s_mov_b64 exec, -1\n"
"s_waitcnt vmcnt(7)\n"
"s_add_u32 s20, s16, 229376\n"
"s_addc_u32 s21, s17, 0\n"
"s_mov_b32 m0, s26\n"
"s_add_u32 s26, s26, 1024\n"
"s_mov_b64 exec, s[32:33]\n"
"global_load_lds_dwordx4 v0, s[20:21] nt\n"
"s_mov_b64 exec, -1\n"
"s_waitcnt vmcnt(7)\n"
"s_add_u32 s20, s16, 258048\n"
"s_addc_u32 s21, s17, 0\n"
"s_mov_b32 m0, s26\n"
"s_add_u32 s26, s26, 1024\n"
"s_cmp_eq_u32 s26, s79\n"
"s_cselect_b32 s26, s25, s26\n"
"s_mov_b64 exec, s[32:33]\n"
"global_load_lds_dwordx4 v0, s[20:21] nt\n"
"s_mov_b64 exec, -1\n"
"s_waitcnt vmcnt(7)\n"
"s_add_u32 s20, s16, 286720\n"
"s_addc_u32 s21, s17, 0\n"
"s_mov_b32 m0, s26\n"
"s_add_u32 s26, s26, 1024\n"
"s_mov_b64 exec, s[32:33]\n"
"global_load_lds_dwordx4 v0, s[20:21] nt\n"
"s_mov_b64 exec, -1\n"
"s_waitcnt vmcnt(7)\n"
"s_add_u32 s20, s16, 315392\n"
"s_addc_u32 s21, s17, 0\n"
"s_mov_b32 m0, s26\n"
"s_add_u32 s26, s26, 1024\n"
"s_cmp_eq_u32 s26, s79\n"
"s_cselect_b32 s26, s25, s26\n"
"s_mov_b64 exec, s[32:33]\n"
"global_load_lds_dwordx4 v0, s[20:21] nt\n"
"s_mov_b64 exec, -1\n"
"s_waitcnt vmcnt(7)\n"
"s_add_u32 s20, s16, 344064\n"
"s_addc_u32 s21, s17, 0\n"
"s_mov_b32 m0, s26\n"
"s_add_u32 s26, s26, 1024\n"
"s_mov_b64 exec, s[32:33]\n"
"global_load_lds_dwordx4 v0, s[20:21] nt\n"
"s_mov_b64 exec, -1\n"
"s_waitcnt vmcnt(7)\n"
"s_add_u32 s20, s16, 372736\n"
"s_addc_u32 s21, s17, 0\n"
"s_mov_b32 m0, s26\n"
"s_add_u32 s26, s26, 1024\n"
"s_cmp_eq_u32 s26, s79\n"
"s_cselect_b32 s26, s25, s26\n"
"s_mov_b64 exec, s[32:33]\n"
"global_load_lds_dwordx4 v0, s[20:21] nt\n"
"s_mov_b64 exec, -1\n"
"s_waitcnt vmcnt(6)\n"
"s_add_u32 s20, s16, 401408\n"
"s_addc_u32 s21, s17, 0\n"
"s_mov_b32 m0, s26\n"
"s_add_u32 s26, s26, 1024\n"
"s_mov_b64 exec, s[32:33]\n"
"global_load_lds_dwordx4 v0, s[20:21] nt\n"
"s_mov_b64 exec, -1\n"
"s_waitcnt vmcnt(6)\n"
"s_add_u32 s20, s16, 430080\n"
"s_addc_u32 s21, s17, 0\n"
"s_mov_b32 m0, s26\n"
"s_add_u32 s26, s26, 1024\n"
"s_cmp_eq_u32 s26, s79\n"
"s_cselect_b32 s26, s25, s26\n"
"s_mov_b64 exec, s[32:33]\n"
"global_load_lds_dwordx4 v0, s[20:21] nt\n"
"s_mov_b64 exec, -1\n"
"s_waitcnt vmcnt(6)\n"
"s_add_u32 s20, s16, 458752\n"
"s_addc_u32 s21, s17, 0\n"
"s_mov_b64 exec, s[32:33]\n"
"global_load_dwordx4 a[232:235], v0, s[20:21] offset:0 nt\n"
"s_mov_b64 exec, -1\n"
"s_waitcnt vmcnt(6)\n"
"s_add_u32 s20, s16, 487424\n"
"s_addc_u32 s21, s17, 0\n"
"s_mov_b64 exec, s[32:33]\n"
"global_load_dwordx4 a[236:239], v0, s[20:21] offset:0 nt\n"
"s_mov_b64 exec, -1\n"
"s_waitcnt vmcnt(6)\n"
"s_add_u32 s20, s16, 516096\n"
"s_addc_u32 s21, s17, 0\n"
"s_mov_b64 exec, s[32:33]\n"
"global_load_dwordx4 a[240:243], v0, s[20:21] offset:0 nt\n"
"s_mov_b64 exec, -1\n"
"s_waitcnt vmcnt(6)\n"
"s_add_u32 s20, s16, 544768\n"
"s_addc_u32 s21, s17, 0\n"
"s_mov_b64 exec, s[32:33]\n"
"global_load_dwordx4 a[244:247], v0, s[20:21] offset:0 nt\n"
"s_mov_b64 exec, -1\n"
"s_waitcnt vmcnt(6)\n"
"s_add_u32 s20, s16, 573440\n"
"s_addc_u32 s21, s17, 0\n"
"s_mov_b64 exec, s[32:33]\n"
"global_load_dwordx4 a[248:251], v0, s[20:21] offset:0 nt\n"
"s_mov_b64 exec, -1\n"
"s_waitcnt vmcnt(6)\n"
"s_add_u32 s20, s16, 602112\n"
"s_addc_u32 s21, s17, 0\n"
"s_mov_b64 exec, s[32:33]\n"
"global_load_dwordx4 a[252:255], v0, s[20:21] offset:0 nt\n"
"s_mov_b64 exec, -1\n"
"s_waitcnt vmcnt(6)\n"
"s_add_u32 s20, s16, 630784\n"
"s_addc_u32 s21, s17, 0\n"
"s_mov_b64 exec, s[32:33]\n"
"global_load_dwordx4 v[48:51], v0, s[20:21] offset:0 nt\n"
"s_mov_b64 exec, -1\n"
"s_mov_b32 s24, 1\n"
"s_add_u32 s59, s59, 262144\n"
".Lst_loop:\n"
"s_cmp_lt_u32 s24, 7\n"
"s_cselect_b64 s[32:33], -1, 1\n"
"s_cselect_b32 s68, 0x2000000, 0\n"
"s_cselect_b32 s89, 1, 0\n"
"s_waitcnt vmcnt(10)\n"
"v_bitop3_b32 v2, v4, v5, v6 bitop3:0x80\n"
"v_bitop3_b32 v3, v7, v8, v9 bitop3:0x80\n"
"v_bitop3_b32 v2, v2, v10, v11 bitop3:0x80\n"
"v_bitop3_b32 v2, v2, 1, v3 bitop3:0x80\n"
"v_cmp_ne_u32 vcc, 0, v2\n"
"s_nop 1\n"
"s_cmp_eq_u64 vcc, -1\n"
"s_cbranch_scc0 .Lst_qslow_5\n"
".Lst_qok_6:\n"
"v_perm_b32 v20, v5, v4, s69\n"
"v_perm_b32 v21, v7, v6, s69\n"
"v_perm_b32 v22, v9, v8, s69\n"
"v_perm_b32 v23, v11, v10, s69\n"
"v_cndmask_b32_e64 v4, 0, v20, s[38:39]\n"
"v_cndmask_b32_e64 v5, 0, v21, s[38:39]\n"
"v_cndmask_b32_e64 v6, 0, v22, s[38:39]\n"
"v_cndmask_b32_e64 v7, 0, v23, s[38:39]\n"
"v_cndmask_b32_e64 v8, 0, v20, s[40:41]\n"
"v_cndmask_b32_e64 v9, 0, v21, s[40:41]\n"
"v_cndmask_b32_e64 v10, 0, v22, s[40:41]\n"
"v_cndmask_b32_e64 v11, 0, v23, s[40:41]\n"
"v_cndmask_b32_e64 v12, 0, v20, s[42:43]\n"
"v_cndmask_b32_e64 v13, 0, v21, s[42:43]\n"
"v_cndmask_b32_e64 v14, 0, v22, s[42:43]\n"
"v_cndmask_b32_e64 v15, 0, v23, s[42:43]\n"
"v_cndmask_b32_e64 v16, 0, v20, s[44:45]\n"
"v_cndmask_b32_e64 v17, 0, v21, s[44:45]\n"
"v_cndmask_b32_e64 v18, 0, v22, s[44:45]\n"
"v_cndmask_b32_e64 v19, 0, v23, s[44:45]\n"
"s_cmp_lt_u32 s24, 7\n"
"s_cselect_b32 s63, 16384, 0\n"
"s_sub_u32 s56, s56, s63\n"
"s_mov_b32 s29, s27\n"
"s_waitcnt vmcnt(23)\n"
"v_add_u32 v47, s29, v1\n"
"ds_read_b128 v[28:31], v47\n"
"s_add_u32 s29, s29, 2048\n"
"s_cmp_eq_u32 s29, s79\n"
"s_cselect_b32 s29, s25, s29\n"
"s_cmp_eq_u32 s89, 0\n"
"s_cbranch_scc1 .Lst_th_9\n"
"s_waitcnt vmcnt(6)\n"
".Lst_th_9:\n"
"s_add_u32 s20, s16, 659456\n"
"s_addc_u32 s21, s17, 0\n"
"global_load_dwordx4 v[52:55], v0, s[20:21] offset:0 nt\n"
"s_waitcnt lgkmcnt(0)\n"
"v_mfma_f32_16x16x32_bf16 v[20:23], v[28:31], v[4:7], 0\n"
"s_waitcnt vmcnt(23)\n"
"ds_read_b128 v[36:39], v47 offset:1024\n"
"s_waitcnt lgkmcnt(0)\n"
"v_mfma_f32_16x16x32_bf16 v[20:23], v[36:39], v[8:11], v[20:23]\n"
"s_waitcnt vmcnt(22)\n"
"v_add_u32 v47, s29, v1\n"
"ds_read_b128 v[28:31], v47\n"
"s_add_u32 s29, s29, 2048\n"
"s_cmp_eq_u32 s29, s79\n"
"s_cselect_b32 s29, s25, s29\n"
"s_cmp_eq_u32 s89, 0\n"
"s_cbranch_scc1 .Lst_th_10\n"
"s_waitcnt vmcnt(6)\n"
".Lst_th_10:\n"
"s_add_u32 s20, s16, 688128\n"
"s_addc_u32 s21, s17, 0\n"
"global_load_dwordx4 v[56:59], v0, s[20:21] offset:0 nt\n"
"s_waitcnt lgkmcnt(0)\n"
"v_mfma_f32_16x16x32_bf16 v[20:23], v[28:31], v[12:15], v[20:23]\n"
"s_waitcnt vmcnt(22)\n"
"ds_read_b128 v[36:39], v47 offset:1024\n"
"s_waitcnt lgkmcnt(0)\n"
"v_mfma_f32_16x16x32_bf16 v[20:23], v[36:39], v[16:19], v[20:23]\n"
"s_nop 7\n"
"v_cndmask_b32_e64 v2, v20, v21, s[34:35]\n"
"v_cndmask_b32_e64 v3, v22, v23, s[34:35]\n"
"v_cndmask_b32_e64 v46, v2, v3, s[36:37]\n"
"s_waitcnt vmcnt(21)\n"
"v_add_u32 v47, s29, v1\n"
"ds_read_b128 v[28:31], v47\n"
"s_add_u32 s29, s29, 2048\n"
"s_cmp_eq_u32 s29, s79\n"
"s_cselect_b32 s29, s25, s29\n"
"s_cmp_eq_u32 s89, 0\n"
"s_cbranch_scc1 .Lst_th_11\n"
"s_waitcnt vmcnt(6)\n"
".Lst_th_11:\n"
"s_add_u32 s20, s16, 716800\n"
"s_addc_u32 s21, s17, 0\n"
"global_load_dwordx4 v[60:63], v0, s[20:21] offset:0 nt\n"
"s_waitcnt lgkmcnt(0)\n"
"v_mfma_f32_16x16x32_bf16 v[24:27], v[28:31], v[4:7], 0\n"
"s_waitcnt vmcnt(21)\n"
"ds_read_b128 v[36:39], v47 offset:1024\n"
"s_waitcnt lgkmcnt(0)\n"
"v_mfma_f32_16x16x32_bf16 v[24:27], v[36:39], v[8:11], v[24:27]\n"
"s_waitcnt vmcnt(20)\n"
"v_add_u32 v47, s29, v1\n"
"ds_read_b128 v[28:31], v47\n"
"s_add_u32 s29, s29, 2048\n"
"s_cmp_eq_u32 s29, s79\n"
"s_cselect_b32 s29, s25, s29\n"
"s_cmp_eq_u32 s89, 0\n"
"s_cbranch_scc1 .Lst_th_12\n"
"s_waitcnt vmcnt(6)\n"
".Lst_th_12:\n"
"s_add_u32 s20, s16, 745472\n"
"s_addc_u32 s21, s17, 0\n"
"global_load_dwordx4 v[64:67], v0, s[20:21] offset:0 nt\n"
"s_waitcnt lgkmcnt(0)\n"
"v_mfma_f32_16x16x32_bf16 v[24:27], v[28:31], v[12:15], v[24:27]\n"
"s_waitcnt vmcnt(19)\n"
"ds_read_b128 v[36:39], v47 offset:1024\n"
"s_waitcnt lgkmcnt(0)\n"
"v_mfma_f32_16x16x32_bf16 v[24:27], v[36:39], v[16:19], v[24:27]\n"
"s_nop 7\n"
"v_cndmask_b32_e64 v2, v24, v25, s[34:35]\n"
"v_cndmask_b32_e64 v3, v26, v27, s[34:35]\n"
"v_cndmask_b32_e64 v2, v2, v3, s[36:37]\n"
"v_cndmask_b32_e64 v3, v2, v46, s[34:35]\n"
"v_cndmask_b32_e64 v46, v46, v2, s[34:35]\n"
"s_nop 0\n"
"v_add_f32_dpp v40, v3, v46 quad_perm:[1,0,3,2] row_mask:0xf bank_mask:0xf\n"
"s_waitcnt vmcnt(18)\n"
"v_add_u32 v47, s29, v1\n"
"ds_read_b128 v[28:31], v47\n"
"s_add_u32 s29, s29, 2048\n"
"s_cmp_eq_u32 s29, s79\n"
"s_cselect_b32 s29, s25, s29\n"
"s_cmp_eq_u32 s89, 0\n"
"s_cbranch_scc1 .Lst_th_13\n"
"s_waitcnt vmcnt(6)\n"
".Lst_th_13:\n"
"s_add_u32 s20, s16, 774144\n"
"s_addc_u32 s21, s17, 0\n"
"global_load_dwordx4 v[68:71], v0, s[20:21] offset:0 nt\n"
"s_waitcnt lgkmcnt(0)\n"
"v_mfma_f32_16x16x32_bf16 v[20:23], v[28:31], v[4:7], 0\n"
"s_waitcnt vmcnt(18)\n"
"ds_read_b128 v[36:39], v47 offset:1024\n"
"s_waitcnt lgkmcnt(0)\n"
"v_mfma_f32_16x16x32_bf16 v[20:23], v[36:39], v[8:11], v[20:23]\n"
"s_waitcnt vmcnt(17)\n"
"v_add_u32 v47, s29, v1\n"
"ds_read_b128 v[28:31], v47\n"
"s_add_u32 s29, s29, 2048\n"
"s_cmp_eq_u32 s29, s79\n"
"s_cselect_b32 s29, s25, s29\n"
"s_cmp_eq_u32 s89, 0\n"
"s_cbranch_scc1 .Lst_th_14\n"
"s_waitcnt vmcnt(6)\n"
".Lst_th_14:\n"
"s_add_u32 s20, s16, 802816\n"
"s_addc_u32 s21, s17, 0\n"
"global_load_dwordx4 v[72:75], v0, s[20:21] offset:0 nt\n"
"s_waitcnt lgkmcnt(0)\n"
"v_mfma_f32_16x16x32_bf16 v[20:23], v[28:31], v[12:15], v[20:23]\n"
"s_waitcnt vmcnt(17)\n"
"ds_read_b128 v[36:39], v47 offset:1024\n"
"s_waitcnt lgkmcnt(0)\n"
"v_mfma_f32_16x16x32_bf16 v[20:23], v[36:39], v[16:19], v[20:23]\n"
"s_nop 7\n"
"v_cndmask_b32_e64 v2, v20, v21, s[34:35]\n"
"v_cndmask_b32_e64 v3, v22, v23, s[34:35]\n"
"v_cndmask_b32_e64 v46, v2, v3, s[36:37]\n"
"s_waitcnt vmcnt(16)\n"
"v_add_u32 v47, s29, v1\n"
"ds_read_b128 v[28:31], v47\n"
"s_add_u32 s29, s29, 2048\n"
"s_cmp_eq_u32 s29, s79\n"
"s_cselect_b32 s29, s25, s29\n"
"s_cmp_eq_u32 s89, 0\n"
"s_cbranch_scc1 .Lst_th_15\n"
"s_waitcnt vmcnt(6)\n"
".Lst_th_15:\n"
"s_add_u32 s20, s16, 831488\n"
"s_addc_u32 s21, s17, 0\n"
"global_load_dwordx4 v[76:79], v0, s[20:21] offset:0 nt\n"
"s_waitcnt lgkmcnt(0)\n"
"v_mfma_f32_16x16x32_bf16 v[24:27], v[28:31], v[4:7], 0\n"
"s_waitcnt vmcnt(16)\n"
"ds_read_b128 v[36:39], v47 offset:1024\n"
"s_waitcnt lgkmcnt(0)\n"
"v_mfma_f32_16x16x32_bf16 v[24:27], v[36:39], v[8:11], v[24:27]\n"
"s_waitcnt vmcnt(15)\n"
"v_add_u32 v47, s29, v1\n"
"ds_read_b128 v[28:31], v47\n"
"s_add_u32 s29, s29, 2048\n"
"s_cmp_eq_u32 s29, s79\n"
"s_cselect_b32 s29, s25, s29\n"
"s_cmp_eq_u32 s89, 0\n"
"s_cbranch_scc1 .Lst_th_16\n"
"s_waitcnt vmcnt(6)\n"
".Lst_th_16:\n"
"s_add_u32 s20, s16, 860160\n"
"s_addc_u32 s21, s17, 0\n"
"global_load_dwordx4 v[80:83], v0, s[20:21] offset:0 nt\n"
"s_waitcnt lgkmcnt(0)\n"
"v_mfma_f32_16x16x32_bf16 v[24:27], v[28:31], v[12:15], v[24:27]\n"
"s_waitcnt vmcnt(15)\n"
"ds_read_b128 v[36:39], v47 offset:1024\n"
"s_cmp_eq_u32 s89, 0\n"
"s_cbranch_scc1 .Lst_th_17\n"
"s_waitcnt vmcnt(6)\n"
".Lst_th_17:\n"
"s_add_u32 s20, s16, 888832\n"
"s_addc_u32 s21, s17, 0\n"
"global_load_dwordx4 v[84:87], v0, s[20:21] offset:0 nt\n"
"s_waitcnt lgkmcnt(0)\n"
"v_mfma_f32_16x16x32_bf16 v[24:27], v[36:39], v[16:19], v[24:27]\n"
"s_nop 7\n"
"v_cndmask_b32_e64 v2, v24, v25, s[34:35]\n"
"v_cndmask_b32_e64 v3, v26, v27, s[34:35]\n"
"v_cndmask_b32_e64 v2, v2, v3, s[36:37]\n"
"v_cndmask_b32_e64 v3, v2, v46, s[34:35]\n"
"v_cndmask_b32_e64 v46, v46, v2, s[34:35]\n"
"s_nop 0\n"
"v_add_f32_dpp v46, v3, v46 quad_perm:[1,0,3,2] row_mask:0xf bank_mask:0xf\n"
"v_cndmask_b32_e64 v2, v46, v40, s[36:37]\n"
"v_cndmask_b32_e64 v3, v40, v46, s[36:37]\n"
"s_nop 0\n"
"v_add_f32_dpp v40, v2, v3 quad_perm:[2,3,0,1] row_mask:0xf bank_mask:0xf\n"
"s_waitcnt vmcnt(15)\n"
"s_cmp_eq_u32 s89, 0\n"
"s_cbranch_scc1 .Lst_th_18\n"
"s_waitcnt vmcnt(6)\n"
".Lst_th_18:\n"
"s_add_u32 s20, s16, 917504\n"
"s_addc_u32 s21, s17, 0\n"
"global_load_dwordx4 v[88:91], v0, s[20:21] offset:0 nt\n"
"v_mfma_f32_16x16x32_bf16 v[20:23], a[232:235], v[4:7], 0\n"
"s_waitcnt vmcnt(15)\n"
"s_cmp_eq_u32 s89, 0\n"
"s_cbranch_scc1 .Lst_th_19\n"
"s_waitcnt vmcnt(6)\n"
".Lst_th_19:\n"
"s_add_u32 s20, s16, 946176\n"
"s_addc_u32 s21, s17, 0\n"
"global_load_dwordx4 v[92:95], v0, s[20:21] offset:0 nt\n"
"v_mfma_f32_16x16x32_bf16 v[20:23], a[236:239], v[8:11], v[20:23]\n"
"s_waitcnt vmcnt(15)\n"
"s_cmp_eq_u32 s89, 0\n"
"s_cbranch_scc1 .Lst_th_20\n"
"s_waitcnt vmcnt(6)\n"
".Lst_th_20:\n"
"s_add_u32 s20, s16, 974848\n"
"s_addc_u32 s21, s17, 0\n"
"global_load_dwordx4 v[96:99], v0, s[20:21] offset:0 nt\n"
"v_mfma_f32_16x16x32_bf16 v[20:23], a[240:243], v[12:15], v[20:23]\n"
"s_waitcnt vmcnt(15)\n"
"s_cmp_eq_u32 s89, 0\n"
"s_cbranch_scc1 .Lst_th_21\n"
"s_waitcnt vmcnt(6)\n"
".Lst_th_21:\n"
"s_add_u32 s20, s16, 1003520\n"
"s_addc_u32 s21, s17, 0\n"
"global_load_dwordx4 v[100:103], v0, s[20:21] offset:0 nt\n"
"v_mfma_f32_16x16x32_bf16 v[20:23], a[244:247], v[16:19], v[20:23]\n"
"s_nop 7\n"
"v_cndmask_b32_e64 v2, v20, v21, s[34:35]\n"
"v_cndmask_b32_e64 v3, v22, v23, s[34:35]\n"
"v_cndmask_b32_e64 v46, v2, v3, s[36:37]\n"
"s_waitcnt vmcnt(15)\n"
"s_cmp_eq_u32 s89, 0\n"
"s_cbranch_scc1 .Lst_th_22\n"
"s_waitcnt vmcnt(6)\n"
".Lst_th_22:\n"
"s_add_u32 s20, s16, s93\n"
"s_addc_u32 s21, s17, 0\n"
"s_mov_b64 exec, s[30:31]\n"
"global_load_dwordx4 v[104:107], v0, s[20:21] offset:0 nt\n"
"s_mov_b64 exec, -1\n"
"v_mfma_f32_16x16x32_bf16 v[24:27], a[248:251], v[4:7], 0\n"
"s_waitcnt vmcnt(15)\n"
"s_cmp_eq_u32 s89, 0\n"
"s_cbranch_scc1 .Lst_th_23\n"
"s_waitcnt vmcnt(6)\n"
".Lst_th_23:\n"
"s_add_u32 s20, s18, 0\n"
"s_addc_u32 s21, s19, 0\n"
"global_load_dwordx4 v[108:111], v1, s[20:21] offset:0 nt\n"
"v_mfma_f32_16x16x32_bf16 v[24:27], a[252:255], v[8:11], v[24:27]\n"
"s_waitcnt vmcnt(15)\n"
"s_cmp_eq_u32 s89, 0\n"
"s_cbranch_scc1 .Lst_th_24\n"
"s_waitcnt vmcnt(6)\n"
".Lst_th_24:\n"
"s_add_u32 s20, s18, 28672\n"
"s_addc_u32 s21, s19, 0\n"
"global_load_dwordx4 v[112:115], v1, s[20:21] offset:0 nt\n"
"v_mfma_f32_16x16x32_bf16 v[24:27], v[48:51], v[12:15], v[24:27]\n"
"s_waitcnt vmcnt(15)\n"
"s_cmp_eq_u32 s89, 0\n"
"s_cbranch_scc1 .Lst_th_25\n"
"s_waitcnt vmcnt(6)\n"
".Lst_th_25:\n"
"s_add_u32 s20, s18, 57344\n"
"s_addc_u32 s21, s19, 0\n"
"global_load_dwordx4 v[116:119], v1, s[20:21] offset:0 nt\n"
"v_mfma_f32_16x16x32_bf16 v[24:27], v[52:55], v[16:19], v[24:27]\n"
"s_nop 7\n"
"v_cndmask_b32_e64 v2, v24, v25, s[34:35]\n"
"v_cndmask_b32_e64 v3, v26, v27, s[34:35]\n"
"v_cndmask_b32_e64 v2, v2, v3, s[36:37]\n"
"v_cndmask_b32_e64 v3, v2, v46, s[34:35]\n"
"v_cndmask_b32_e64 v46, v46, v2, s[34:35]\n"
"s_nop 0\n"
"v_add_f32_dpp v41, v3, v46 quad_perm:[1,0,3,2] row_mask:0xf bank_mask:0xf\n"
"s_waitcnt vmcnt(15)\n"
"s_cmp_eq_u32 s89, 0\n"
"s_cbranch_scc1 .Lst_th_26\n"
"s_waitcnt vmcnt(6)\n"
".Lst_th_26:\n"
"s_add_u32 s20, s18, 86016\n"
"s_addc_u32 s21, s19, 0\n"
"global_load_dwordx4 v[120:123], v1, s[20:21] offset:0 nt\n"
"v_mfma_f32_16x16x32_bf16 v[20:23], v[56:59], v[4:7], 0\n"
"s_waitcnt vmcnt(15)\n"
"s_cmp_eq_u32 s89, 0\n"
"s_cbranch_scc1 .Lst_th_27\n"
"s_waitcnt vmcnt(6)\n"
".Lst_th_27:\n"
"s_add_u32 s20, s18, 114688\n"
"s_addc_u32 s21, s19, 0\n"
"global_load_dwordx4 v[124:127], v1, s[20:21] offset:0 nt\n"
"v_mfma_f32_16x16x32_bf16 v[20:23], v[60:63], v[8:11], v[20:23]\n"
"s_waitcnt vmcnt(15)\n"
"s_cmp_eq_u32 s89, 0\n"
"s_cbranch_scc1 .Lst_th_28\n"
"s_waitcnt vmcnt(6)\n"
".Lst_th_28:\n"
"s_add_u32 s20, s18, 143360\n"
"s_addc_u32 s21, s19, 0\n"
"global_load_dwordx4 v[128:131], v1, s[20:21] offset:0 nt\n"
"v_mfma_f32_16x16x32_bf16 v[20:23], v[64:67], v[12:15], v[20:23]\n"
"s_waitcnt vmcnt(15)\n"
"s_cmp_eq_u32 s89, 0\n"
"s_cbranch_scc1 .Lst_th_29\n"
"s_waitcnt vmcnt(6)\n"
".Lst_th_29:\n"
"s_add_u32 s20, s18, 172032\n"
"s_addc_u32 s21, s19, 0\n"
"global_load_dwordx4 v[132:135], v1, s[20:21] offset:0 nt\n"
"v_mfma_f32_16x16x32_bf16 v[20:23], v[68:71], v[16:19], v[20:23]\n"
"s_nop 7\n"
"v_cndmask_b32_e64 v2, v20, v21, s[34:35]\n"
"v_cndmask_b32_e64 v3, v22, v23, s[34:35]\n"
"v_cndmask_b32_e64 v46, v2, v3, s[36:37]\n"
"s_waitcnt vmcnt(15)\n"
"s_cmp_eq_u32 s89, 0\n"
"s_cbranch_scc1 .Lst_th_30\n"
"s_waitcnt vmcnt(6)\n"
".Lst_th_30:\n"
"s_add_u32 s20, s18, 200704\n"
"s_addc_u32 s21, s19, 0\n"
"global_load_dwordx4 v[136:139], v1, s[20:21] offset:0 nt\n"
"v_mfma_f32_16x16x32_bf16 v[24:27], v[72:75], v[4:7], 0\n"
"s_waitcnt vmcnt(15)\n"
"s_cmp_eq_u32 s89, 0\n"
"s_cbranch_scc1 .Lst_th_31\n"
"s_waitcnt vmcnt(6)\n"
".Lst_th_31:\n"
"s_add_u32 s20, s18, 229376\n"
"s_addc_u32 s21, s19, 0\n"
"global_load_dwordx4 v[140:143], v1, s[20:21] offset:0 nt\n"
"v_mfma_f32_16x16x32_bf16 v[24:27], v[76:79], v[8:11], v[24:27]\n"
"s_waitcnt vmcnt(15)\n"
"s_cmp_eq_u32 s89, 0\n"
"s_cbranch_scc1 .Lst_th_32\n"
"s_waitcnt vmcnt(6)\n"
".Lst_th_32:\n"
"s_add_u32 s20, s18, 258048\n"
"s_addc_u32 s21, s19, 0\n"
"global_load_dwordx4 v[144:147], v1, s[20:21] offset:0 nt\n"
"v_mfma_f32_16x16x32_bf16 v[24:27], v[80:83], v[12:15], v[24:27]\n"
"s_waitcnt vmcnt(15)\n"
"s_cmp_eq_u32 s89, 0\n"
"s_cbranch_scc1 .Lst_th_33\n"
"s_waitcnt vmcnt(6)\n"
".Lst_th_33:\n"
"s_add_u32 s20, s18, 286720\n"
"s_addc_u32 s21, s19, 0\n"
"global_load_dwordx4 v[148:151], v1, s[20:21] offset:0 nt\n"
"v_mfma_f32_16x16x32_bf16 v[24:27], v[84:87], v[16:19], v[24:27]\n"
"s_nop 7\n"
"v_cndmask_b32_e64 v2, v24, v25, s[34:35]\n"
"v_cndmask_b32_e64 v3, v26, v27, s[34:35]\n"
"v_cndmask_b32_e64 v2, v2, v3, s[36:37]\n"
"v_cndmask_b32_e64 v3, v2, v46, s[34:35]\n"
"v_cndmask_b32_e64 v46, v46, v2, s[34:35]\n"
"s_nop 0\n"
"v_add_f32_dpp v46, v3, v46 quad_perm:[1,0,3,2] row_mask:0xf bank_mask:0xf\n"
"v_cndmask_b32_e64 v2, v46, v41, s[36:37]\n"
"v_cndmask_b32_e64 v3, v41, v46, s[36:37]\n"
"s_nop 0\n"
"v_add_f32_dpp v41, v2, v3 quad_perm:[2,3,0,1] row_mask:0xf bank_mask:0xf\n"
"s_waitcnt vmcnt(15)\n"
"s_cmp_eq_u32 s89, 0\n"
"s_cbranch_scc1 .Lst_th_34\n"
"s_waitcnt vmcnt(6)\n"
".Lst_th_34:\n"
"s_add_u32 s20, s18, 315392\n"
"s_addc_u32 s21, s19, 0\n"
"global_load_dwordx4 v[152:155], v1, s[20:21] offset:0 nt\n"
"v_mfma_f32_16x16x32_bf16 v[20:23], v[88:91], v[4:7], 0\n"
"s_waitcnt vmcnt(15)\n"
"s_cmp_eq_u32 s89, 0\n"
"s_cbranch_scc1 .Lst_th_35\n"
"s_waitcnt vmcnt(6)\n"
".Lst_th_35:\n"
"s_add_u32 s20, s18, 344064\n"
"s_addc_u32 s21, s19, 0\n"
"global_load_dwordx4 v[156:159], v1, s[20:21] offset:0 nt\n"
"v_mfma_f32_16x16x32_bf16 v[20:23], v[92:95], v[8:11], v[20:23]\n"
"s_waitcnt vmcnt(15)\n"
"s_cmp_eq_u32 s89, 0\n"
"s_cbranch_scc1 .Lst_th_36\n"
"s_waitcnt vmcnt(6)\n"
".Lst_th_36:\n"
"s_add_u32 s20, s18, 372736\n"
"s_addc_u32 s21, s19, 0\n"
"global_load_dwordx4 v[160:163], v1, s[20:21] offset:0 nt\n"
"v_mfma_f32_16x16x32_bf16 v[20:23], v[96:99], v[12:15], v[20:23]\n"
"s_waitcnt vmcnt(15)\n"
"s_cmp_eq_u32 s89, 0\n"
"s_cbranch_scc1 .Lst_th_37\n"
"s_waitcnt vmcnt(6)\n"
".Lst_th_37:\n"
"s_add_u32 s20, s18, 401408\n"
"s_addc_u32 s21, s19, 0\n"
"global_load_dwordx4 v[164:167], v1, s[20:21] offset:0 nt\n"
"v_mfma_f32_16x16x32_bf16 v[20:23], v[100:103], v[16:19], v[20:23]\n"
"s_nop 7\n"
"v_cndmask_b32_e64 v2, v20, v21, s[34:35]\n"
"v_cndmask_b32_e64 v3, v22, v23, s[34:35]\n"
"v_cndmask_b32_e64 v46, v2, v3, s[36:37]\n"
"s_waitcnt vmcnt(15)\n"
"s_cmp_eq_u32 s89, 0\n"
"s_cbranch_scc1 .Lst_th_38\n"
"s_waitcnt vmcnt(6)\n"
".Lst_th_38:\n"
"s_add_u32 s20, s18, 430080\n"
"s_addc_u32 s21, s19, 0\n"
"global_load_dwordx4 v[168:171], v1, s[20:21] offset:0 nt\n"
"v_mfma_f32_16x16x32_bf16 v[24:27], v[104:107], v[4:7], 0\n"
"s_nop 7\n"
"v_cndmask_b32_e64 v2, v24, v25, s[34:35]\n"
"v_cndmask_b32_e64 v3, v26, v27, s[34:35]\n"
"v_cndmask_b32_e64 v2, v2, v3, s[36:37]\n"
"v_cndmask_b32_e64 v3, v2, v46, s[34:35]\n"
"v_cndmask_b32_e64 v46, v46, v2, s[34:35]\n"
"s_nop 0\n"
"v_add_f32_dpp v42, v3, v46 quad_perm:[1,0,3,2] row_mask:0xf bank_mask:0xf\n"
"v_cndmask_b32_e64 v2, v46, v42, s[36:37]\n"
"v_cndmask_b32_e64 v3, v42, v46, s[36:37]\n"
"s_nop 0\n"
"v_add_f32_dpp v42, v2, v3 quad_perm:[2,3,0,1] row_mask:0xf bank_mask:0xf\n"
"v_mov_b32 v2, 0xff800000\n"
"v_cndmask_b32_e64 v42, v2, v42, s[46:47]\n"
"v_max3_f32 v2, v40, v41, v42\n"
"s_nop 1\n"
"v_max_f32_dpp v2, v2, v2 quad_perm:[1,0,3,2] row_mask:0xf bank_mask:0xf\n"
"s_nop 1\n"
"v_max_f32_dpp v2, v2, v2 quad_perm:[2,3,0,1] row_mask:0xf bank_mask:0xf\n"
"s_nop 1\n"
"v_max_f32_dpp v2, v2, v2 row_half_mirror row_mask:0xf bank_mask:0xf\n"
"s_nop 1\n"
"v_max_f32_dpp v2, v2, v2 row_mirror row_mask:0xf bank_mask:0xf\n"
"v_mov_b32 v3, v2\n"
"s_nop 1\n"
"v_permlane16_swap_b32 v3, v2\n"
"v_max_f32 v2, v2, v3\n"
"v_mov_b32 v3, v2\n"
"s_nop 1\n"
"v_permlane32_swap_b32 v3, v2\n"
"v_max_f32 v2, v2, v3\n"
"v_mul_f32 v3, s64, v2\n"
"v_mul_f32 v2, s65, v2\n"
"v_readfirstlane_b32 s52, v3\n"
"v_fma_f32 v40, v40, s64, v2\n"
"v_fma_f32 v41, v41, s64, v2\n"
"v_fma_f32 v42, v42, s64, v2\n"
"v_exp_f32 v40, v40\n"
"v_exp_f32 v41, v41\n"
"v_exp_f32 v42, v42\n"
"v_mov_b32 v3, 0xffff\n"
"v_not_b32 v47, v3\n"
"v_cndmask_b32_e64 v3, v3, v47, s[34:35]\n"
"v_cndmask_b32_e64 v28, 0, v3, s[80:81]\n"
"v_cndmask_b32_e64 v29, 0, v3, s[82:83]\n"
"v_cndmask_b32_e64 v30, 0, v3, s[84:85]\n"
"v_cndmask_b32_e64 v31, 0, v3, s[86:87]\n"
"v_cvt_pk_bf16_f32 v40, v40, v40\n"
"v_cvt_pk_bf16_f32 v41, v41, v41\n"
"v_cvt_pk_bf16_f32 v42, v42, v42\n"
"v_and_b32 v2, 0xffff0000, v40\n"
"v_and_b32 v3, 0xffff0000, v41\n"
"v_add_f32 v2, v2, v3\n"
"v_and_b32 v3, 0xffff0000, v42\n"
"v_add_f32 v2, v2, v3\n"
"s_nop 1\n"
"v_add_f32_dpp v2, v2, v2 quad_perm:[1,0,3,2] row_mask:0xf bank_mask:0xf\n"
"s_nop 1\n"
"v_add_f32_dpp v2, v2, v2 quad_perm:[2,3,0,1] row_mask:0xf bank_mask:0xf\n"
"s_nop 1\n"
"v_add_f32_dpp v2, v2, v2 row_half_mirror row_mask:0xf bank_mask:0xf\n"
"s_nop 1\n"
"v_add_f32_dpp v2, v2, v2 row_mirror row_mask:0xf bank_mask:0xf\n"
"v_mov_b32 v3, v2\n"
"s_nop 1\n"
"v_permlane16_swap_b32 v3, v2\n"
"v_add_f32 v2, v2, v3\n"
"v_mov_b32 v3, v2\n"
"s_nop 1\n"
"v_permlane32_swap_b32 v3, v2\n"
"v_add_f32 v2, v2, v3\n"
"s_nop 0\n"
"v_readfirstlane_b32 s53, v2\n"
"v_and_b32_dpp v20, v40, v28 row_newbcast:0 row_mask:0xf bank_mask:0xf\n"
"v_and_b32_dpp v21, v40, v29 row_newbcast:0 row_mask:0xf bank_mask:0xf\n"
"v_and_b32_dpp v22, v40, v30 row_newbcast:0 row_mask:0xf bank_mask:0xf\n"
"v_and_b32_dpp v23, v40, v31 row_newbcast:0 row_mask:0xf bank_mask:0xf\n"
"s_waitcnt vmcnt(15)\n"
"s_cmp_eq_u32 s89, 0\n"
"s_cbranch_scc1 .Lst_th_39\n"
"s_waitcnt vmcnt(6)\n"
".Lst_th_39:\n"
"s_add_u32 s20, s18, 458752\n"
"s_addc_u32 s21, s19, 0\n"
"global_load_dwordx4 v[172:175], v1, s[20:21] offset:0 nt\n"
"v_mfma_f32_16x16x32_bf16 v[32:35], v[108:111], v[20:23], 0\n"
"v_and_b32_dpp v24, v40, v28 row_newbcast:4 row_mask:0xf bank_mask:0xf\n"
"v_and_b32_dpp v25, v40, v29 row_newbcast:4 row_mask:0xf bank_mask:0xf\n"
"v_and_b32_dpp v26, v40, v30 row_newbcast:4 row_mask:0xf bank_mask:0xf\n"
"v_and_b32_dpp v27, v40, v31 row_newbcast:4 row_mask:0xf bank_mask:0xf\n"
"s_waitcnt vmcnt(15)\n"
"s_cmp_eq_u32 s89, 0\n"
"s_cbranch_scc1 .Lst_th_40\n"
"s_waitcnt vmcnt(6)\n"
".Lst_th_40:\n"
"s_add_u32 s20, s18, 487424\n"
"s_addc_u32 s21, s19, 0\n"
"global_load_dwordx4 v[176:179], v1, s[20:21] offset:0 nt\n"
"v_mfma_f32_16x16x32_bf16 v[32:35], v[112:115], v[24:27], v[32:35]\n"
"v_and_b32_dpp v20, v40, v28 row_newbcast:8 row_mask:0xf bank_mask:0xf\n"
"v_and_b32_dpp v21, v40, v29 row_newbcast:8 row_mask:0xf bank_mask:0xf\n"
"v_and_b32_dpp v22, v40, v30 row_newbcast:8 row_mask:0xf bank_mask:0xf\n"
"v_and_b32_dpp v23, v40, v31 row_newbcast:8 row_mask:0xf bank_mask:0xf\n"
"s_waitcnt vmcnt(15)\n"
"s_cmp_eq_u32 s89, 0\n"
"s_cbranch_scc1 .Lst_th_41\n"
"s_waitcnt vmcnt(6)\n"
".Lst_th_41:\n"
"s_add_u32 s20, s18, 516096\n"
"s_addc_u32 s21, s19, 0\n"
"global_load_dwordx4 v[180:183], v1, s[20:21] offset:0 nt\n"
"v_mfma_f32_16x16x32_bf16 v[32:35], v[116:119], v[20:23], v[32:35]\n"
"v_and_b32_dpp v24, v40, v28 row_newbcast:12 row_mask:0xf bank_mask:0xf\n"
"v_and_b32_dpp v25, v40, v29 row_newbcast:12 row_mask:0xf bank_mask:0xf\n"
"v_and_b32_dpp v26, v40, v30 row_newbcast:12 row_mask:0xf bank_mask:0xf\n"
"v_and_b32_dpp v27, v40, v31 row_newbcast:12 row_mask:0xf bank_mask:0xf\n"
"s_waitcnt vmcnt(15)\n"
"s_cmp_eq_u32 s89, 0\n"
"s_cbranch_scc1 .Lst_th_42\n"
"s_waitcnt vmcnt(6)\n"
".Lst_th_42:\n"
"s_add_u32 s20, s18, 544768\n"
"s_addc_u32 s21, s19, 0\n"
"global_load_dwordx4 v[184:187], v1, s[20:21] offset:0 nt\n"
"v_mfma_f32_16x16x32_bf16 v[32:35], v[120:123], v[24:27], v[32:35]\n"
"v_and_b32_dpp v20, v40, v28 row_newbcast:1 row_mask:0xf bank_mask:0xf\n"
"v_and_b32_dpp v21, v40, v29 row_newbcast:1 row_mask:0xf bank_mask:0xf\n"
"v_and_b32_dpp v22, v40, v30 row_newbcast:1 row_mask:0xf bank_mask:0xf\n"
"v_and_b32_dpp v23, v40, v31 row_newbcast:1 row_mask:0xf bank_mask:0xf\n"
"s_waitcnt vmcnt(15)\n"
"s_cmp_eq_u32 s89, 0\n"
"s_cbranch_scc1 .Lst_th_43\n"
"s_waitcnt vmcnt(6)\n"
".Lst_th_43:\n"
"s_add_u32 s20, s18, 573440\n"
"s_addc_u32 s21, s19, 0\n"
"global_load_dwordx4 v[188:191], v1, s[20:21] offset:0 nt\n"
"v_mfma_f32_16x16x32_bf16 v[32:35], v[124:127], v[20:23], v[32:35]\n"
"v_and_b32_dpp v24, v40, v28 row_newbcast:5 row_mask:0xf bank_mask:0xf\n"
"v_and_b32_dpp v25, v40, v29 row_newbcast:5 row_mask:0xf bank_mask:0xf\n"
"v_and_b32_dpp v26, v40, v30 row_newbcast:5 row_mask:0xf bank_mask:0xf\n"
"v_and_b32_dpp v27, v40, v31 row_newbcast:5 row_mask:0xf bank_mask:0xf\n"
"s_waitcnt vmcnt(15)\n"
"s_cmp_eq_u32 s89, 0\n"
"s_cbranch_scc1 .Lst_th_44\n"
"s_waitcnt vmcnt(6)\n"
".Lst_th_44:\n"
"s_add_u32 s20, s18, 602112\n"
"s_addc_u32 s21, s19, 0\n"
"global_load_dwordx4 v[192:195], v1, s[20:21] offset:0 nt\n"
"v_mfma_f32_16x16x32_bf16 v[32:35], v[128:131], v[24:27], v[32:35]\n"
"v_and_b32_dpp v20, v40, v28 row_newbcast:9 row_mask:0xf bank_mask:0xf\n"
"v_and_b32_dpp v21, v40, v29 row_newbcast:9 row_mask:0xf bank_mask:0xf\n"
"v_and_b32_dpp v22, v40, v30 row_newbcast:9 row_mask:0xf bank_mask:0xf\n"
"v_and_b32_dpp v23, v40, v31 row_newbcast:9 row_mask:0xf bank_mask:0xf\n"
"s_waitcnt vmcnt(15)\n"
"s_cmp_eq_u32 s89, 0\n"
"s_cbranch_scc1 .Lst_th_45\n"
"s_waitcnt vmcnt(6)\n"
".Lst_th_45:\n"
"s_add_u32 s20, s18, 630784\n"
"s_addc_u32 s21, s19, 0\n"
"global_load_dwordx4 v[196:199], v1, s[20:21] offset:0 nt\n"
"v_mfma_f32_16x16x32_bf16 v[32:35], v[132:135], v[20:23], v[32:35]\n"
"v_and_b32_dpp v24, v40, v28 row_newbcast:13 row_mask:0xf bank_mask:0xf\n"
"v_and_b32_dpp v25, v40, v29 row_newbcast:13 row_mask:0xf bank_mask:0xf\n"
"v_and_b32_dpp v26, v40, v30 row_newbcast:13 row_mask:0xf bank_mask:0xf\n"
"v_and_b32_dpp v27, v40, v31 row_newbcast:13 row_mask:0xf bank_mask:0xf\n"
"s_waitcnt vmcnt(15)\n"
"s_cmp_eq_u32 s89, 0\n"
"s_cbranch_scc1 .Lst_th_46\n"
"s_waitcnt vmcnt(6)\n"
".Lst_th_46:\n"
"s_add_u32 s20, s18, 659456\n"
"s_addc_u32 s21, s19, 0\n"
"global_load_dwordx4 v[200:203], v1, s[20:21] offset:0 nt\n"
"v_mfma_f32_16x16x32_bf16 v[32:35], v[136:139], v[24:27], v[32:35]\n"
"v_and_b32_dpp v20, v40, v28 row_newbcast:2 row_mask:0xf bank_mask:0xf\n"
"v_and_b32_dpp v21, v40, v29 row_newbcast:2 row_mask:0xf bank_mask:0xf\n"
"v_and_b32_dpp v22, v40, v30 row_newbcast:2 row_mask:0xf bank_mask:0xf\n"
"v_and_b32_dpp v23, v40, v31 row_newbcast:2 row_mask:0xf bank_mask:0xf\n"
"s_waitcnt vmcnt(15)\n"
"s_cmp_eq_u32 s89, 0\n"
"s_cbranch_scc1 .Lst_th_47\n"
"s_waitcnt vmcnt(6)\n"
".Lst_th_47:\n"
"s_add_u32 s20, s18, 688128\n"
"s_addc_u32 s21, s19, 0\n"
"global_load_dwordx4 v[204:207], v1, s[20:21] offset:0 nt\n"
"v_mfma_f32_16x16x32_bf16 v[32:35], v[140:143], v[20:23], v[32:35]\n"
"v_and_b32_dpp v24, v40, v28 row_newbcast:6 row_mask:0xf bank_mask:0xf\n"
"v_and_b32_dpp v25, v40, v29 row_newbcast:6 row_mask:0xf bank_mask:0xf\n"
"v_and_b32_dpp v26, v40, v30 row_newbcast:6 row_mask:0xf bank_mask:0xf\n"
"v_and_b32_dpp v27, v40, v31 row_newbcast:6 row_mask:0xf bank_mask:0xf\n"
"s_waitcnt vmcnt(15)\n"
"s_cmp_eq_u32 s89, 0\n"
"s_cbranch_scc1 .Lst_th_48\n"
"s_waitcnt vmcnt(6)\n"
".Lst_th_48:\n"
"s_add_u32 s20, s18, 716800\n"
"s_addc_u32 s21, s19, 0\n"
"global_load_dwordx4 v[208:211], v1, s[20:21] offset:0 nt\n"
"v_mfma_f32_16x16x32_bf16 v[32:35], v[144:147], v[24:27], v[32:35]\n"
"v_and_b32_dpp v20, v40, v28 row_newbcast:10 row_mask:0xf bank_mask:0xf\n"
"v_and_b32_dpp v21, v40, v29 row_newbcast:10 row_mask:0xf bank_mask:0xf\n"
"v_and_b32_dpp v22, v40, v30 row_newbcast:10 row_mask:0xf bank_mask:0xf\n"
"v_and_b32_dpp v23, v40, v31 row_newbcast:10 row_mask:0xf bank_mask:0xf\n"
"s_waitcnt vmcnt(15)\n"
"s_cmp_eq_u32 s89, 0\n"
"s_cbranch_scc1 .Lst_th_49\n"
"s_waitcnt vmcnt(6)\n"
".Lst_th_49:\n"
"s_add_u32 s20, s18, 745472\n"
"s_addc_u32 s21, s19, 0\n"
"global_load_dwordx4 v[212:215], v1, s[20:21] offset:0 nt\n"
"v_mfma_f32_16x16x32_bf16 v[32:35], v[148:151], v[20:23], v[32:35]\n"
"v_and_b32_dpp v24, v40, v28 row_newbcast:14 row_mask:0xf bank_mask:0xf\n"
"v_and_b32_dpp v25, v40, v29 row_newbcast:14 row_mask:0xf bank_mask:0xf\n"
"v_and_b32_dpp v26, v40, v30 row_newbcast:14 row_mask:0xf bank_mask:0xf\n"
"v_and_b32_dpp v27, v40, v31 row_newbcast:14 row_mask:0xf bank_mask:0xf\n"
"s_waitcnt vmcnt(15)\n"
"s_cmp_eq_u32 s89, 0\n"
"s_cbranch_scc1 .Lst_th_50\n"
"s_waitcnt vmcnt(6)\n"
".Lst_th_50:\n"
"s_add_u32 s20, s18, 774144\n"
"s_addc_u32 s21, s19, 0\n"
"global_load_dwordx4 v[216:219], v1, s[20:21] offset:0 nt\n"
"v_mfma_f32_16x16x32_bf16 v[32:35], v[152:155], v[24:27], v[32:35]\n"
"v_and_b32_dpp v20, v40, v28 row_newbcast:3 row_mask:0xf bank_mask:0xf\n"
"v_and_b32_dpp v21, v40, v29 row_newbcast:3 row_mask:0xf bank_mask:0xf\n"
"v_and_b32_dpp v22, v40, v30 row_newbcast:3 row_mask:0xf bank_mask:0xf\n"
"v_and_b32_dpp v23, v40, v31 row_newbcast:3 row_mask:0xf bank_mask:0xf\n"
"s_waitcnt vmcnt(15)\n"
"s_cmp_eq_u32 s89, 0\n"
"s_cbranch_scc1 .Lst_th_51\n"
"s_waitcnt vmcnt(6)\n"
".Lst_th_51:\n"
"s_add_u32 s20, s18, 802816\n"
"s_addc_u32 s21, s19, 0\n"
"global_load_dwordx4 v[220:223], v1, s[20:21] offset:0 nt\n"
"v_mfma_f32_16x16x32_bf16 v[32:35], v[156:159], v[20:23], v[32:35]\n"
"v_and_b32_dpp v24, v40, v28 row_newbcast:7 row_mask:0xf bank_mask:0xf\n"
"v_and_b32_dpp v25, v40, v29 row_newbcast:7 row_mask:0xf bank_mask:0xf\n"
"v_and_b32_dpp v26, v40, v30 row_newbcast:7 row_mask:0xf bank_mask:0xf\n"
"v_and_b32_dpp v27, v40, v31 row_newbcast:7 row_mask:0xf bank_mask:0xf\n"
"s_waitcnt vmcnt(15)\n"
"s_cmp_eq_u32 s89, 0\n"
"s_cbranch_scc1 .Lst_th_52\n"
"s_waitcnt vmcnt(6)\n"
".Lst_th_52:\n"
"s_add_u32 s20, s18, 831488\n"
"s_addc_u32 s21, s19, 0\n"
"global_load_dwordx4 v[224:227], v1, s[20:21] offset:0 nt\n"
"v_mfma_f32_16x16x32_bf16 v[32:35], v[160:163], v[24:27], v[32:35]\n"
"v_and_b32_dpp v20, v40, v28 row_newbcast:11 row_mask:0xf bank_mask:0xf\n"
"v_and_b32_dpp v21, v40, v29 row_newbcast:11 row_mask:0xf bank_mask:0xf\n"
"v_and_b32_dpp v22, v40, v30 row_newbcast:11 row_mask:0xf bank_mask:0xf\n"
"v_and_b32_dpp v23, v40, v31 row_newbcast:11 row_mask:0xf bank_mask:0xf\n"
"s_waitcnt vmcnt(15)\n"
"s_cmp_eq_u32 s89, 0\n"
"s_cbranch_scc1 .Lst_th_53\n"
"s_waitcnt vmcnt(6)\n"
".Lst_th_53:\n"
"s_add_u32 s20, s18, 860160\n"
"s_addc_u32 s21, s19, 0\n"
"global_load_dwordx4 v[228:231], v1, s[20:21] offset:0 nt\n"
"v_mfma_f32_16x16x32_bf16 v[32:35], v[164:167], v[20:23], v[32:35]\n"
"v_and_b32_dpp v24, v40, v28 row_newbcast:15 row_mask:0xf bank_mask:0xf\n"
"v_and_b32_dpp v25, v40, v29 row_newbcast:15 row_mask:0xf bank_mask:0xf\n"
"v_and_b32_dpp v26, v40, v30 row_newbcast:15 row_mask:0xf bank_mask:0xf\n"
"v_and_b32_dpp v27, v40, v31 row_newbcast:15 row_mask:0xf bank_mask:0xf\n"
"s_waitcnt vmcnt(15)\n"
"s_cmp_eq_u32 s89, 0\n"
"s_cbranch_scc1 .Lst_th_54\n"
"s_waitcnt vmcnt(6)\n"
".Lst_th_54:\n"
"s_add_u32 s20, s18, 888832\n"
"s_addc_u32 s21, s19, 0\n"
"global_load_dwordx4 v[232:235], v1, s[20:21] offset:0 nt\n"
"v_mfma_f32_16x16x32_bf16 v[32:35], v[168:171], v[24:27], v[32:35]\n"
"v_and_b32_dpp v20, v41, v28 row_newbcast:0 row_mask:0xf bank_mask:0xf\n"
"v_and_b32_dpp v21, v41, v29 row_newbcast:0 row_mask:0xf bank_mask:0xf\n"
"v_and_b32_dpp v22, v41, v30 row_newbcast:0 row_mask:0xf bank_mask:0xf\n"
"v_and_b32_dpp v23, v41, v31 row_newbcast:0 row_mask:0xf bank_mask:0xf\n"
"s_waitcnt vmcnt(15)\n"
"s_cmp_eq_u32 s89, 0\n"
"s_cbranch_scc1 .Lst_th_55\n"
"s_waitcnt vmcnt(6)\n"
".Lst_th_55:\n"
"s_add_u32 s20, s18, 917504\n"
"s_addc_u32 s21, s19, 0\n"
"global_load_dwordx4 v[236:239], v1, s[20:21] offset:0 nt\n"
"v_mfma_f32_16x16x32_bf16 v[32:35], v[172:175], v[20:23], v[32:35]\n"
"v_and_b32_dpp v24, v41, v28 row_newbcast:4 row_mask:0xf bank_mask:0xf\n"
"v_and_b32_dpp v25, v41, v29 row_newbcast:4 row_mask:0xf bank_mask:0xf\n"
"v_and_b32_dpp v26, v41, v30 row_newbcast:4 row_mask:0xf bank_mask:0xf\n"
"v_and_b32_dpp v27, v41, v31 row_newbcast:4 row_mask:0xf bank_mask:0xf\n"
"s_waitcnt vmcnt(15)\n"
"s_cmp_eq_u32 s89, 0\n"
"s_cbranch_scc1 .Lst_th_56\n"
"s_waitcnt vmcnt(6)\n"
".Lst_th_56:\n"
"s_add_u32 s20, s18, 946176\n"
"s_addc_u32 s21, s19, 0\n"
"global_load_dwordx4 v[240:243], v1, s[20:21] offset:0 nt\n"
"v_mfma_f32_16x16x32_bf16 v[32:35], v[176:179], v[24:27], v[32:35]\n"
"v_and_b32_dpp v20, v41, v28 row_newbcast:8 row_mask:0xf bank_mask:0xf\n"
"v_and_b32_dpp v21, v41, v29 row_newbcast:8 row_mask:0xf bank_mask:0xf\n"
"v_and_b32_dpp v22, v41, v30 row_newbcast:8 row_mask:0xf bank_mask:0xf\n"
"v_and_b32_dpp v23, v41, v31 row_newbcast:8 row_mask:0xf bank_mask:0xf\n"
"s_waitcnt vmcnt(15)\n"
"s_cmp_eq_u32 s89, 0\n"
"s_cbranch_scc1 .Lst_th_57\n"
"s_waitcnt vmcnt(6)\n"
".Lst_th_57:\n"
"s_add_u32 s20, s18, 974848\n"
"s_addc_u32 s21, s19, 0\n"
"global_load_dwordx4 v[244:247], v1, s[20:21] offset:0 nt\n"
"v_mfma_f32_16x16x32_bf16 v[32:35], v[180:183], v[20:23], v[32:35]\n"
"v_and_b32_dpp v24, v41, v28 row_newbcast:12 row_mask:0xf bank_mask:0xf\n"
"v_and_b32_dpp v25, v41, v29 row_newbcast:12 row_mask:0xf bank_mask:0xf\n"
"v_and_b32_dpp v26, v41, v30 row_newbcast:12 row_mask:0xf bank_mask:0xf\n"
"v_and_b32_dpp v27, v41, v31 row_newbcast:12 row_mask:0xf bank_mask:0xf\n"
"s_waitcnt vmcnt(15)\n"
"s_cmp_eq_u32 s89, 0\n"
"s_cbranch_scc1 .Lst_th_58\n"
"s_waitcnt vmcnt(6)\n"
".Lst_th_58:\n"
"s_add_u32 s20, s18, 1003520\n"
"s_addc_u32 s21, s19, 0\n"
"global_load_dwordx4 v[248:251], v1, s[20:21] offset:0 nt\n"
"v_mfma_f32_16x16x32_bf16 v[32:35], v[184:187], v[24:27], v[32:35]\n"
"v_and_b32_dpp v20, v41, v28 row_newbcast:1 row_mask:0xf bank_mask:0xf\n"
"v_and_b32_dpp v21, v41, v29 row_newbcast:1 row_mask:0xf bank_mask:0xf\n"
"v_and_b32_dpp v22, v41, v30 row_newbcast:1 row_mask:0xf bank_mask:0xf\n"
"v_and_b32_dpp v23, v41, v31 row_newbcast:1 row_mask:0xf bank_mask:0xf\n"
"s_waitcnt vmcnt(15)\n"
"s_cmp_eq_u32 s89, 0\n"
"s_cbranch_scc1 .Lst_th_59\n"
"s_waitcnt vmcnt(6)\n"
".Lst_th_59:\n"
"s_add_u32 s20, s18, s93\n"
"s_addc_u32 s21, s19, 0\n"
"s_mov_b64 exec, s[30:31]\n"
"global_load_dwordx4 v[252:255], v1, s[20:21] offset:0 nt\n"
"s_mov_b64 exec, -1\n"
"v_mfma_f32_16x16x32_bf16 v[32:35], v[188:191], v[20:23], v[32:35]\n"
"v_and_b32_dpp v24, v41, v28 row_newbcast:5 row_mask:0xf bank_mask:0xf\n"
"v_and_b32_dpp v25, v41, v29 row_newbcast:5 row_mask:0xf bank_mask:0xf\n"
"v_and_b32_dpp v26, v41, v30 row_newbcast:5 row_mask:0xf bank_mask:0xf\n"
"v_and_b32_dpp v27, v41, v31 row_newbcast:5 row_mask:0xf bank_mask:0xf\n"
"s_waitcnt vmcnt(15)\n"
"s_cmp_eq_u32 s89, 0\n"
"s_cbranch_scc1 .Lst_th_60\n"
"s_waitcnt vmcnt(6)\n"
".Lst_th_60:\n"
"s_add_u32 s16, s16, s68\n"
"s_addc_u32 s17, s17, 0\n"
"s_add_u32 s18, s18, s68\n"
"s_addc_u32 s19, s19, 0\n"
"s_add_u32 s20, s16, 0\n"
"s_addc_u32 s21, s17, 0\n"
"s_mov_b32 m0, s26\n"
"s_add_u32 s26, s26, 1024\n"
"s_mov_b64 exec, s[32:33]\n"
"global_load_lds_dwordx4 v0, s[20:21] nt\n"
"s_mov_b64 exec, -1\n"
"v_mfma_f32_16x16x32_bf16 v[32:35], v[192:195], v[24:27], v[32:35]\n"
"v_and_b32_dpp v20, v41, v28 row_newbcast:9 row_mask:0xf bank_mask:0xf\n"
"v_and_b32_dpp v21, v41, v29 row_newbcast:9 row_mask:0xf bank_mask:0xf\n"
"v_and_b32_dpp v22, v41, v30 row_newbcast:9 row_mask:0xf bank_mask:0xf\n"
"v_and_b32_dpp v23, v41, v31 row_newbcast:9 row_mask:0xf bank_mask:0xf\n"
"s_waitcnt vmcnt(15)\n"
"s_cmp_eq_u32 s89, 0\n"
"s_cbranch_scc1 .Lst_th_61\n"
"s_waitcnt vmcnt(6)\n"
".Lst_th_61:\n"
"s_add_u32 s20, s16, 28672\n"
"s_addc_u32 s21, s17, 0\n"
"s_mov_b32 m0, s26\n"
"s_add_u32 s26, s26, 1024\n"
"s_cmp_eq_u32 s26, s79\n"
"s_cselect_b32 s26, s25, s26\n"
"s_mov_b64 exec, s[32:33]\n"
"global_load_lds_dwordx4 v0, s[20:21] nt\n"
"s_mov_b64 exec, -1\n"
"v_mfma_f32_16x16x32_bf16 v[32:35], v[196:199], v[20:23], v[32:35]\n"
"v_and_b32_dpp v24, v41, v28 row_newbcast:13 row_mask:0xf bank_mask:0xf\n"
"v_and_b32_dpp v25, v41, v29 row_newbcast:13 row_mask:0xf bank_mask:0xf\n"
"v_and_b32_dpp v26, v41, v30 row_newbcast:13 row_mask:0xf bank_mask:0xf\n"
"v_and_b32_dpp v27, v41, v31 row_newbcast:13 row_mask:0xf bank_mask:0xf\n"
"s_waitcnt vmcnt(15)\n"
"s_cmp_eq_u32 s89, 0\n"
"s_cbranch_scc1 .Lst_th_62\n"
"s_waitcnt vmcnt(6)\n"
".Lst_th_62:\n"
"s_add_u32 s20, s16, 57344\n"
"s_addc_u32 s21, s17, 0\n"
"s_mov_b32 m0, s26\n"
"s_add_u32 s26, s26, 1024\n"
"s_mov_b64 exec, s[32:33]\n"
"global_load_lds_dwordx4 v0, s[20:21] nt\n"
"s_mov_b64 exec, -1\n"
"v_mfma_f32_16x16x32_bf16 v[32:35], v[200:203], v[24:27], v[32:35]\n"
"v_and_b32_dpp v20, v41, v28 row_newbcast:2 row_mask:0xf bank_mask:0xf\n"
"v_and_b32_dpp v21, v41, v29 row_newbcast:2 row_mask:0xf bank_mask:0xf\n"
"v_and_b32_dpp v22, v41, v30 row_newbcast:2 row_mask:0xf bank_mask:0xf\n"
"v_and_b32_dpp v23, v41, v31 row_newbcast:2 row_mask:0xf bank_mask:0xf\n"
"s_waitcnt vmcnt(15)\n"
"s_cmp_eq_u32 s89, 0\n"
"s_cbranch_scc1 .Lst_th_63\n"
"s_waitcnt vmcnt(6)\n"
".Lst_th_63:\n"
"s_add_u32 s20, s16, 86016\n"
"s_addc_u32 s21, s17, 0\n"
"s_mov_b32 m0, s26\n"
"s_add_u32 s26, s26, 1024\n"
"s_cmp_eq_u32 s26, s79\n"
"s_cselect_b32 s26, s25, s26\n"
"s_mov_b64 exec, s[32:33]\n"
"global_load_lds_dwordx4 v0, s[20:21] nt\n"
"s_mov_b64 exec, -1\n"
"v_mfma_f32_16x16x32_bf16 v[32:35], v[204:207], v[20:23], v[32:35]\n"
"v_and_b32_dpp v24, v41, v28 row_newbcast:6 row_mask:0xf bank_mask:0xf\n"
"v_and_b32_dpp v25, v41, v29 row_newbcast:6 row_mask:0xf bank_mask:0xf\n"
"v_and_b32_dpp v26, v41, v30 row_newbcast:6 row_mask:0xf bank_mask:0xf\n"
"v_and_b32_dpp v27, v41, v31 row_newbcast:6 row_mask:0xf bank_mask:0xf\n"
"s_waitcnt vmcnt(15)\n"
"s_cmp_eq_u32 s89, 0\n"
"s_cbranch_scc1 .Lst_th_64\n"
"s_waitcnt vmcnt(6)\n"
".Lst_th_64:\n"
"s_add_u32 s20, s16, 114688\n"
"s_addc_u32 s21, s17, 0\n"
"s_mov_b32 m0, s26\n"
"s_add_u32 s26, s26, 1024\n"
"s_mov_b64 exec, s[32:33]\n"
"global_load_lds_dwordx4 v0, s[20:21] nt\n"
"s_mov_b64 exec, -1\n"
"v_mfma_f32_16x16x32_bf16 v[32:35], v[208:211], v[24:27], v[32:35]\n"
"v_and_b32_dpp v20, v41, v28 row_newbcast:10 row_mask:0xf bank_mask:0xf\n"
"v_and_b32_dpp v21, v41, v29 row_newbcast:10 row_mask:0xf bank_mask:0xf\n"
"v_and_b32_dpp v22, v41, v30 row_newbcast:10 row_mask:0xf bank_mask:0xf\n"
"v_and_b32_dpp v23, v41, v31 row_newbcast:10 row_mask:0xf bank_mask:0xf\n"
"s_waitcnt vmcnt(15)\n"
"s_cmp_eq_u32 s89, 0\n"
"s_cbranch_scc1 .Lst_th_65\n"
"s_waitcnt vmcnt(6)\n"
".Lst_th_65:\n"
"s_add_u32 s20, s16, 143360\n"
"s_addc_u32 s21, s17, 0\n"
"s_mov_b32 m0, s26\n"
"s_add_u32 s26, s26, 1024\n"
"s_cmp_eq_u32 s26, s79\n"
"s_cselect_b32 s26, s25, s26\n"
"s_mov_b64 exec, s[32:33]\n"
"global_load_lds_dwordx4 v0, s[20:21] nt\n"
"s_mov_b64 exec, -1\n"
"v_mfma_f32_16x16x32_bf16 v[32:35], v[212:215], v[20:23], v[32:35]\n"
"v_and_b32_dpp v24, v41, v28 row_newbcast:14 row_mask:0xf bank_mask:0xf\n"
"v_and_b32_dpp v25, v41, v29 row_newbcast:14 row_mask:0xf bank_mask:0xf\n"
"v_and_b32_dpp v26, v41, v30 row_newbcast:14 row_mask:0xf bank_mask:0xf\n"
"v_and_b32_dpp v27, v41, v31 row_newbcast:14 row_mask:0xf bank_mask:0xf\n"
"s_waitcnt vmcnt(15)\n"
"s_nop 0\n"
"v_mfma_f32_16x16x32_bf16 v[32:35], v[216:219], v[24:27], v[32:35]\n"
"v_and_b32_dpp v20, v41, v28 row_newbcast:3 row_mask:0xf bank_mask:0xf\n"
"v_and_b32_dpp v21, v41, v29 row_newbcast:3 row_mask:0xf bank_mask:0xf\n"
"v_and_b32_dpp v22, v41, v30 row_newbcast:3 row_mask:0xf bank_mask:0xf\n"
"v_and_b32_dpp v23, v41, v31 row_newbcast:3 row_mask:0xf bank_mask:0xf\n"
"s_waitcnt vmcnt(14)\n"
"s_nop 0\n"
"v_mfma_f32_16x16x32_bf16 v[32:35], v[220:223], v[20:23], v[32:35]\n"
"v_and_b32_dpp v24, v41, v28 row_newbcast:7 row_mask:0xf bank_mask:0xf\n"
"v_and_b32_dpp v25, v41, v29 row_newbcast:7 row_mask:0xf bank_mask:0xf\n"
"v_and_b32_dpp v26, v41, v30 row_newbcast:7 row_mask:0xf bank_mask:0xf\n"
"v_and_b32_dpp v27, v41, v31 row_newbcast:7 row_mask:0xf bank_mask:0xf\n"
"s_waitcnt vmcnt(13)\n"
"s_nop 0\n"
"v_mfma_f32_16x16x32_bf16 v[32:35], v[224:227], v[24:27], v[32:35]\n"
"v_and_b32_dpp v20, v41, v28 row_newbcast:11 row_mask:0xf bank_mask:0xf\n"
"v_and_b32_dpp v21, v41, v29 row_newbcast:11 row_mask:0xf bank_mask:0xf\n"
"v_and_b32_dpp v22, v41, v30 row_newbcast:11 row_mask:0xf bank_mask:0xf\n"
"v_and_b32_dpp v23, v41, v31 row_newbcast:11 row_mask:0xf bank_mask:0xf\n"
"s_waitcnt vmcnt(12)\n"
"s_nop 0\n"
"v_mfma_f32_16x16x32_bf16 v[32:35], v[228:231], v[20:23], v[32:35]\n"
"v_and_b32_dpp v24, v41, v28 row_newbcast:15 row_mask:0xf bank_mask:0xf\n"
"v_and_b32_dpp v25, v41, v29 row_newbcast:15 row_mask:0xf bank_mask:0xf\n"
"v_and_b32_dpp v26, v41, v30 row_newbcast:15 row_mask:0xf bank_mask:0xf\n"
"v_and_b32_dpp v27, v41, v31 row_newbcast:15 row_mask:0xf bank_mask:0xf\n"
"s_waitcnt vmcnt(11)\n"
"s_nop 0\n"
"v_mfma_f32_16x16x32_bf16 v[32:35], v[232:235], v[24:27], v[32:35]\n"
"v_and_b32_dpp v20, v42, v28 row_newbcast:0 row_mask:0xf bank_mask:0xf\n"
"v_and_b32_dpp v21, v42, v29 row_newbcast:0 row_mask:0xf bank_mask:0xf\n"
"v_and_b32_dpp v22, v42, v30 row_newbcast:0 row_mask:0xf bank_mask:0xf\n"
"v_and_b32_dpp v23, v42, v31 row_newbcast:0 row_mask:0xf bank_mask:0xf\n"
"s_waitcnt vmcnt(10)\n"
"s_nop 0\n"
"v_mfma_f32_16x16x32_bf16 v[32:35], v[236:239], v[20:23], v[32:35]\n"
"v_and_b32 v47, 0xff, v0\n"
"v_lshl_add_u32 v47, v47, 1, s57\n"
"global_load_dwordx4 v[4:7], v47, s[22:23] sc1\n"
"global_load_dwordx4 v[8:11], v47, s[22:23] offset:16 sc1\n"
"v_and_b32_dpp v24, v42, v28 row_newbcast:4 row_mask:0xf bank_mask:0xf\n"
"v_and_b32_dpp v25, v42, v29 row_newbcast:4 row_mask:0xf bank_mask:0xf\n"
"v_and_b32_dpp v26, v42, v30 row_newbcast:4 row_mask:0xf bank_mask:0xf\n"
"v_and_b32_dpp v27, v42, v31 row_newbcast:4 row_mask:0xf bank_mask:0xf\n"
"s_waitcnt vmcnt(11)\n"
"s_nop 0\n"
"v_mfma_f32_16x16x32_bf16 v[32:35], v[240:243], v[24:27], v[32:35]\n"
"v_and_b32_dpp v20, v42, v28 row_newbcast:8 row_mask:0xf bank_mask:0xf\n"
"v_and_b32_dpp v21, v42, v29 row_newbcast:8 row_mask:0xf bank_mask:0xf\n"
"v_and_b32_dpp v22, v42, v30 row_newbcast:8 row_mask:0xf bank_mask:0xf\n"
"v_and_b32_dpp v23, v42, v31 row_newbcast:8 row_mask:0xf bank_mask:0xf\n"
"s_waitcnt vmcnt(10)\n"
"s_nop 0\n"
"v_mfma_f32_16x16x32_bf16 v[32:35], v[244:247], v[20:23], v[32:35]\n"
"v_and_b32_dpp v24, v42, v28 row_newbcast:12 row_mask:0xf bank_mask:0xf\n"
"v_and_b32_dpp v25, v42, v29 row_newbcast:12 row_mask:0xf bank_mask:0xf\n"
"v_and_b32_dpp v26, v42, v30 row_newbcast:12 row_mask:0xf bank_mask:0xf\n"
"v_and_b32_dpp v27, v42, v31 row_newbcast:12 row_mask:0xf bank_mask:0xf\n"
"s_waitcnt vmcnt(9)\n"
"s_nop 0\n"
"v_mfma_f32_16x16x32_bf16 v[32:35], v[248:251], v[24:27], v[32:35]\n"
"v_and_b32_dpp v20, v42, v28 row_newbcast:1 row_mask:0xf bank_mask:0xf\n"
"v_and_b32_dpp v21, v42, v29 row_newbcast:1 row_mask:0xf bank_mask:0xf\n"
"v_and_b32_dpp v22, v42, v30 row_newbcast:1 row_mask:0xf bank_mask:0xf\n"
"v_and_b32_dpp v23, v42, v31 row_newbcast:1 row_mask:0xf bank_mask:0xf\n"
"s_add_u32 s63, s62, s61\n"
"v_lshrrev_b32 v2, 8, v1\n"
"v_bfe_u32 v3, v1, 4, 4\n"
"v_lshlrev_b32 v2, 5, v2\n"
"v_add_u32 v2, v2, v3\n"
"v_lshl_add_u32 v2, v2, 2, s63\n"
"v_mov_b32 v3, s63\n"
"s_waitcnt vmcnt(8)\n"
"v_mfma_f32_16x16x32_bf16 v[32:35], v[252:255], v[20:23], v[32:35]\n"
"v_mov_b32 v46, s52\n"
"v_mov_b32 v47, s53\n"
"s_mov_b64 exec, s[48:49]\n"
"ds_write_b64 v3, v[46:47]\n"
"s_nop 3\n"
"ds_write_b32 v2, v32 offset:16\n"
"ds_write_b32 v2, v33 offset:48\n"
"ds_write_b32 v2, v34 offset:80\n"
"ds_write_b32 v2, v35 offset:112\n"
"s_mov_b64 exec, -1\n"
"s_add_u32 s63, s62, s78\n"
"v_mov_b32 v3, s62\n"
"v_bfe_u32 v47, v1, 4, 5\n"
"v_lshl_add_u32 v2, v47, 2, s63\n"
"s_waitcnt lgkmcnt(0)\n"
"s_barrier\n"
"ds_read_b64 v[20:21], v3 offset:0\n"
"ds_read_b64 v[22:23], v3 offset:528\n"
"ds_read_b64 v[24:25], v3 offset:1056\n"
"ds_read_b64 v[26:27], v3 offset:1584\n"
"ds_read_b32 v28, v2 offset:0\n"
"ds_read_b32 v29, v2 offset:528\n"
"ds_read_b32 v30, v2 offset:1056\n"
"ds_read_b32 v31, v2 offset:1584\n"
"s_xor_b32 s62, s62, 2112\n"
"s_add_u32 s63, s58, s72\n"
"s_sub_u32 s70, s73, s72\n"
"v_lshl_add_u32 v47, v47, 2, s63\n"
"v_mov_b32 v46, s70\n"
"v_cndmask_b32_e64 v46, 0, v46, s[66:67]\n"
"v_add_u32 v47, v47, v46\n"
"s_waitcnt lgkmcnt(4)\n"
"v_max3_f32 v2, v20, v22, v24\n"
"v_max_f32 v2, v2, v26\n"
"v_sub_f32 v20, v20, v2\n"
"v_sub_f32 v22, v22, v2\n"
"v_sub_f32 v24, v24, v2\n"
"v_sub_f32 v26, v26, v2\n"
"v_exp_f32 v20, v20\n"
"v_exp_f32 v22, v22\n"
"v_exp_f32 v24, v24\n"
"v_exp_f32 v26, v26\n"
"v_mul_f32 v3, v20, v21\n"
"v_fmac_f32 v3, v22, v23\n"
"v_fmac_f32 v3, v24, v25\n"
"v_fmac_f32 v3, v26, v27\n"
"s_waitcnt lgkmcnt(0)\n"
"v_mul_f32 v21, v20, v28\n"
"v_fmac_f32 v21, v22, v29\n"
"v_fmac_f32 v21, v24, v30\n"
"v_fmac_f32 v21, v26, v31\n"
"v_cndmask_b32_e64 v2, v2, v3, s[34:35]\n"
"v_cndmask_b32_e64 v21, v21, v2, s[66:67]\n"
"v_or_b32 v21, 1, v21\n"
"s_mov_b64 exec, s[50:51]\n"
"global_store_dword v47, v21, s[22:23] sc1\n"
"s_mov_b64 exec, -1\n"
"s_waitcnt vmcnt(1)\n"
"v_bitop3_b32 v2, v4, v5, v6 bitop3:0x80\n"
"v_bitop3_b32 v3, v7, v8, v9 bitop3:0x80\n"
"v_bitop3_b32 v2, v2, v10, v11 bitop3:0x80\n"
"v_bitop3_b32 v2, v2, 1, v3 bitop3:0x80\n"
"v_cmp_ne_u32 vcc, 0, v2\n"
"s_nop 1\n"
"s_cmp_eq_u64 vcc, -1\n"
"s_cbranch_scc0 .Lst_qslow_66\n"
".Lst_qok_67:\n"
"v_perm_b32 v20, v5, v4, s69\n"
"v_perm_b32 v21, v7, v6, s69\n"
"v_perm_b32 v22, v9, v8, s69\n"
"v_perm_b32 v23, v11, v10, s69\n"
"v_cndmask_b32_e64 v4, 0, v20, s[38:39]\n"
"v_cndmask_b32_e64 v5, 0, v21, s[38:39]\n"
"v_cndmask_b32_e64 v6, 0, v22, s[38:39]\n"
"v_cndmask_b32_e64 v7, 0, v23, s[38:39]\n"
"v_cndmask_b32_e64 v8, 0, v20, s[40:41]\n"
"v_cndmask_b32_e64 v9, 0, v21, s[40:41]\n"
"v_cndmask_b32_e64 v10, 0, v22, s[40:41]\n"
"v_cndmask_b32_e64 v11, 0, v23, s[40:41]\n"
"v_cndmask_b32_e64 v12, 0, v20, s[42:43]\n"
"v_cndmask_b32_e64 v13, 0, v21, s[42:43]\n"
"v_cndmask_b32_e64 v14, 0, v22, s[42:43]\n"
"v_cndmask_b32_e64 v15, 0, v23, s[42:43]\n"
"v_cndmask_b32_e64 v16, 0, v20, s[44:45]\n"
"v_cndmask_b32_e64 v17, 0, v21, s[44:45]\n"
"v_cndmask_b32_e64 v18, 0, v22, s[44:45]\n"
"v_cndmask_b32_e64 v19, 0, v23, s[44:45]\n"
"s_mov_b32 s29, s28\n"
"v_add_u32 v47, s29, v1\n"
"ds_read_b128 v[28:31], v47\n"
"s_add_u32 s29, s29, 2048\n"
"s_cmp_eq_u32 s29, s79\n"
"s_cselect_b32 s29, s25, s29\n"
"ds_read_b128 v[36:39], v47 offset:1024\n"
"v_mfma_f32_16x16x32_bf16 v[20:23], a[0:3], v[4:7], 0\n"
"v_mfma_f32_16x16x32_bf16 v[20:23], a[4:7], v[8:11], v[20:23]\n"
"s_waitcnt lgkmcnt(1)\n"
"v_mfma_f32_16x16x32_bf16 v[20:23], v[28:31], v[12:15], v[20:23]\n"
"v_add_u32 v47, s29, v1\n"
"ds_read_b128 v[28:31], v47\n"
"s_add_u32 s29, s29, 2048\n"
"s_cmp_eq_u32 s29, s79\n"
"s_cselect_b32 s29, s25, s29\n"
"s_waitcnt lgkmcnt(1)\n"
"v_mfma_f32_16x16x32_bf16 v[20:23], v[36:39], v[16:19], v[20:23]\n"
"ds_read_b128 v[36:39], v47 offset:1024\n"
"s_add_u32 s20, s16, 172032\n"
"s_addc_u32 s21, s17, 0\n"
"s_mov_b32 m0, s26\n"
"s_add_u32 s26, s26, 1024\n"
"s_mov_b64 exec, s[32:33]\n"
"global_load_lds_dwordx4 v0, s[20:21] nt\n"
"s_mov_b64 exec, -1\n"
"v_mfma_f32_16x16x32_bf16 v[24:27], a[8:11], v[4:7], 0\n"
"v_mfma_f32_16x16x32_bf16 v[24:27], a[12:15], v[8:11], v[24:27]\n"
"s_waitcnt lgkmcnt(1)\n"
"v_mfma_f32_16x16x32_bf16 v[24:27], v[28:31], v[12:15], v[24:27]\n"
"v_add_u32 v47, s29, v1\n"
"ds_read_b128 v[28:31], v47\n"
"s_add_u32 s29, s29, 2048\n"
"s_cmp_eq_u32 s29, s79\n"
"s_cselect_b32 s29, s25, s29\n"
"s_waitcnt lgkmcnt(1)\n"
"v_mfma_f32_16x16x32_bf16 v[24:27], v[36:39], v[16:19], v[24:27]\n"
"ds_read_b128 v[36:39], v47 offset:1024\n"
"v_cndmask_b32_e64 v2, v20, v21, s[34:35]\n"
"v_cndmask_b32_e64 v3, v22, v23, s[34:35]\n"
"v_cndmask_b32_e64 v46, v2, v3, s[36:37]\n"
"s_add_u32 s20, s16, 200704\n"
"s_addc_u32 s21, s17, 0\n"
"s_mov_b32 m0, s26\n"
"s_add_u32 s26, s26, 1024\n"
"s_cmp_eq_u32 s26, s79\n"
"s_cselect_b32 s26, s25, s26\n"
"s_mov_b64 exec, s[32:33]\n"
"global_load_lds_dwordx4 v0, s[20:21] nt\n"
"s_mov_b64 exec, -1\n"
"v_mfma_f32_16x16x32_bf16 v[20:23], a[16:19], v[4:7], 0\n"
"v_mfma_f32_16x16x32_bf16 v[20:23], a[20:23], v[8:11], v[20:23]\n"
"s_waitcnt lgkmcnt(1)\n"
"v_mfma_f32_16x16x32_bf16 v[20:23], v[28:31], v[12:15], v[20:23]\n"
"v_add_u32 v47, s29, v1\n"
"ds_read_b128 v[28:31], v47\n"
"s_add_u32 s29, s29, 2048\n"
"s_cmp_eq_u32 s29, s79\n"
"s_cselect_b32 s29, s25, s29\n"
"s_waitcnt lgkmcnt(1)\n"
"v_mfma_f32_16x16x32_bf16 v[20:23], v[36:39], v[16:19], v[20:23]\n"
"ds_read_b128 v[36:39], v47 offset:1024\n"
"v_cndmask_b32_e64 v2, v24, v25, s[34:35]\n"
"v_cndmask_b32_e64 v3, v26, v27, s[34:35]\n"
"v_cndmask_b32_e64 v2, v2, v3, s[36:37]\n"
"v_cndmask_b32_e64 v3, v2, v46, s[34:35]\n"
"v_cndmask_b32_e64 v46, v46, v2, s[34:35]\n"
"s_nop 0\n"
"v_add_f32_dpp v43, v3, v46 quad_perm:[1,0,3,2] row_mask:0xf bank_mask:0xf\n"
"s_add_u32 s20, s16, 229376\n"
"s_addc_u32 s21, s17, 0\n"
"s_mov_b32 m0, s26\n"
"s_add_u32 s26, s26, 1024\n"
"s_mov_b64 exec, s[32:33]\n"
"global_load_lds_dwordx4 v0, s[20:21] nt\n"
"s_mov_b64 exec, -1\n"
"v_mfma_f32_16x16x32_bf16 v[24:27], a[24:27], v[4:7], 0\n"
"v_mfma_f32_16x16x32_bf16 v[24:27], a[28:31], v[8:11], v[24:27]\n"
"s_waitcnt lgkmcnt(1)\n"
"v_mfma_f32_16x16x32_bf16 v[24:27], v[28:31], v[12:15], v[24:27]\n"
"v_add_u32 v47, s29, v1\n"
"ds_read_b128 v[28:31], v47\n"
"s_add_u32 s29, s29, 2048\n"
"s_cmp_eq_u32 s29, s79\n"
"s_cselect_b32 s29, s25, s29\n"
"s_waitcnt lgkmcnt(1)\n"
"v_mfma_f32_16x16x32_bf16 v[24:27], v[36:39], v[16:19], v[24:27]\n"
"ds_read_b128 v[36:39], v47 offset:1024\n"
"v_cndmask_b32_e64 v2, v20, v21, s[34:35]\n"
"v_cndmask_b32_e64 v3, v22, v23, s[34:35]\n"
"v_cndmask_b32_e64 v46, v2, v3, s[36:37]\n"
"s_add_u32 s20, s16, 258048\n"
"s_addc_u32 s21, s17, 0\n"
"s_mov_b32 m0, s26\n"
"s_add_u32 s26, s26, 1024\n"
"s_cmp_eq_u32 s26, s79\n"
"s_cselect_b32 s26, s25, s26\n"
"s_mov_b64 exec, s[32:33]\n"
"global_load_lds_dwordx4 v0, s[20:21] nt\n"
"s_mov_b64 exec, -1\n"
"v_mfma_f32_16x16x32_bf16 v[20:23], a[32:35], v[4:7], 0\n"
"v_mfma_f32_16x16x32_bf16 v[20:23], a[36:39], v[8:11], v[20:23]\n"
"s_waitcnt lgkmcnt(1)\n"
"v_mfma_f32_16x16x32_bf16 v[20:23], v[28:31], v[12:15], v[20:23]\n"
"v_add_u32 v47, s29, v1\n"
"ds_read_b128 v[28:31], v47\n"
"s_add_u32 s29, s29, 2048\n"
"s_cmp_eq_u32 s29, s79\n"
"s_cselect_b32 s29, s25, s29\n"
"s_waitcnt lgkmcnt(1)\n"
"v_mfma_f32_16x16x32_bf16 v[20:23], v[36:39], v[16:19], v[20:23]\n"
"ds_read_b128 v[36:39], v47 offset:1024\n"
"v_cndmask_b32_e64 v2, v24, v25, s[34:35]\n"
"v_cndmask_b32_e64 v3, v26, v27, s[34:35]\n"
"v_cndmask_b32_e64 v2, v2, v3, s[36:37]\n"
"v_cndmask_b32_e64 v3, v2, v46, s[34:35]\n"
"v_cndmask_b32_e64 v46, v46, v2, s[34:35]\n"
"s_nop 0\n"
"v_add_f32_dpp v46, v3, v46 quad_perm:[1,0,3,2] row_mask:0xf bank_mask:0xf\n"
"v_cndmask_b32_e64 v2, v46, v43, s[36:37]\n"
"v_cndmask_b32_e64 v3, v43, v46, s[36:37]\n"
"s_nop 0\n"
"v_add_f32_dpp v43, v2, v3 quad_perm:[2,3,0,1] row_mask:0xf bank_mask:0xf\n"
"s_add_u32 s20, s16, 286720\n"
"s_addc_u32 s21, s17, 0\n"
"s_mov_b32 m0, s26\n"
"s_add_u32 s26, s26, 1024\n"
"s_mov_b64 exec, s[32:33]\n"
"global_load_lds_dwordx4 v0, s[20:21] nt\n"
"s_mov_b64 exec, -1\n"
"v_mfma_f32_16x16x32_bf16 v[24:27], a[40:43], v[4:7], 0\n"
"v_mfma_f32_16x16x32_bf16 v[24:27], a[44:47], v[8:11], v[24:27]\n"
"s_waitcnt lgkmcnt(1)\n"
"v_mfma_f32_16x16x32_bf16 v[24:27], v[28:31], v[12:15], v[24:27]\n"
"v_add_u32 v47, s29, v1\n"
"ds_read_b128 v[28:31], v47\n"
"s_add_u32 s29, s29, 2048\n"
"s_cmp_eq_u32 s29, s79\n"
"s_cselect_b32 s29, s25, s29\n"
"s_waitcnt lgkmcnt(1)\n"
"v_mfma_f32_16x16x32_bf16 v[24:27], v[36:39], v[16:19], v[24:27]\n"
"ds_read_b128 v[36:39], v47 offset:1024\n"
"v_cndmask_b32_e64 v2, v20, v21, s[34:35]\n"
"v_cndmask_b32_e64 v3, v22, v23, s[34:35]\n"
"v_cndmask_b32_e64 v46, v2, v3, s[36:37]\n"
"s_add_u32 s20, s16, 315392\n"
"s_addc_u32 s21, s17, 0\n"
"s_mov_b32 m0, s26\n"
"s_add_u32 s26, s26, 1024\n"
"s_cmp_eq_u32 s26, s79\n"
"s_cselect_b32 s26, s25, s26\n"
"s_mov_b64 exec, s[32:33]\n"
"global_load_lds_dwordx4 v0, s[20:21] nt\n"
"s_mov_b64 exec, -1\n"
"v_mfma_f32_16x16x32_bf16 v[20:23], a[48:51], v[4:7], 0\n"
"v_mfma_f32_16x16x32_bf16 v[20:23], a[52:55], v[8:11], v[20:23]\n"
"s_waitcnt lgkmcnt(1)\n"
"v_mfma_f32_16x16x32_bf16 v[20:23], v[28:31], v[12:15], v[20:23]\n"
"v_add_u32 v47, s29, v1\n"
"ds_read_b128 v[28:31], v47\n"
"s_add_u32 s29, s29, 2048\n"
"s_cmp_eq_u32 s29, s79\n"
"s_cselect_b32 s29, s25, s29\n"
"s_waitcnt lgkmcnt(1)\n"
"v_mfma_f32_16x16x32_bf16 v[20:23], v[36:39], v[16:19], v[20:23]\n"
"ds_read_b128 v[36:39], v47 offset:1024\n"
"v_cndmask_b32_e64 v2, v24, v25, s[34:35]\n"
"v_cndmask_b32_e64 v3, v26, v27, s[34:35]\n"
"v_cndmask_b32_e64 v2, v2, v3, s[36:37]\n"
"v_cndmask_b32_e64 v3, v2, v46, s[34:35]\n"
"v_cndmask_b32_e64 v46, v46, v2, s[34:35]\n"
"s_nop 0\n"
"v_add_f32_dpp v44, v3, v46 quad_perm:[1,0,3,2] row_mask:0xf bank_mask:0xf\n"
"s_add_u32 s20, s16, 344064\n"
"s_addc_u32 s21, s17, 0\n"
"s_mov_b32 m0, s26\n"
"s_add_u32 s26, s26, 1024\n"
"s_mov_b64 exec, s[32:33]\n"
"global_load_lds_dwordx4 v0, s[20:21] nt\n"
"s_mov_b64 exec, -1\n"
"v_mfma_f32_16x16x32_bf16 v[24:27], a[56:59], v[4:7], 0\n"
"v_mfma_f32_16x16x32_bf16 v[24:27], a[60:63], v[8:11], v[24:27]\n"
"s_waitcnt lgkmcnt(1)\n"
"v_mfma_f32_16x16x32_bf16 v[24:27], v[28:31], v[12:15], v[24:27]\n"
"s_waitcnt lgkmcnt(0)\n"
"v_mfma_f32_16x16x32_bf16 v[24:27], v[36:39], v[16:19], v[24:27]\n"
"v_cndmask_b32_e64 v2, v20, v21, s[34:35]\n"
"v_cndmask_b32_e64 v3, v22, v23, s[34:35]\n"
"v_cndmask_b32_e64 v46, v2, v3, s[36:37]\n"
"s_add_u32 s20, s16, 372736\n"
"s_addc_u32 s21, s17, 0\n"
"s_mov_b32 m0, s26\n"
"s_add_u32 s26, s26, 1024\n"
"s_cmp_eq_u32 s26, s79\n"
"s_cselect_b32 s26, s25, s26\n"
"s_mov_b64 exec, s[32:33]\n"
"global_load_lds_dwordx4 v0, s[20:21] nt\n"
"s_mov_b64 exec, -1\n"
"v_mfma_f32_16x16x32_bf16 v[20:23], a[64:67], v[4:7], 0\n"
"v_mfma_f32_16x16x32_bf16 v[20:23], a[68:71], v[8:11], v[20:23]\n"
"v_mfma_f32_16x16x32_bf16 v[20:23], a[72:75], v[12:15], v[20:23]\n"
"v_mfma_f32_16x16x32_bf16 v[20:23], a[76:79], v[16:19], v[20:23]\n"
"v_cndmask_b32_e64 v2, v24, v25, s[34:35]\n"
"v_cndmask_b32_e64 v3, v26, v27, s[34:35]\n"
"v_cndmask_b32_e64 v2, v2, v3, s[36:37]\n"
"v_cndmask_b32_e64 v3, v2, v46, s[34:35]\n"
"v_cndmask_b32_e64 v46, v46, v2, s[34:35]\n"
"s_nop 0\n"
"v_add_f32_dpp v46, v3, v46 quad_perm:[1,0,3,2] row_mask:0xf bank_mask:0xf\n"
"v_cndmask_b32_e64 v2, v46, v44, s[36:37]\n"
"v_cndmask_b32_e64 v3, v44, v46, s[36:37]\n"
"s_nop 0\n"
"v_add_f32_dpp v44, v2, v3 quad_perm:[2,3,0,1] row_mask:0xf bank_mask:0xf\n"
"v_mfma_f32_16x16x32_bf16 v[24:27], a[80:83], v[4:7], 0\n"
"v_cndmask_b32_e64 v2, v20, v21, s[34:35]\n"
"v_cndmask_b32_e64 v3, v22, v23, s[34:35]\n"
"v_cndmask_b32_e64 v46, v2, v3, s[36:37]\n"
"s_nop 4\n"
"v_cndmask_b32_e64 v2, v24, v25, s[34:35]\n"
"v_cndmask_b32_e64 v3, v26, v27, s[34:35]\n"
"v_cndmask_b32_e64 v2, v2, v3, s[36:37]\n"
"v_cndmask_b32_e64 v3, v2, v46, s[34:35]\n"
"v_cndmask_b32_e64 v46, v46, v2, s[34:35]\n"
"s_nop 0\n"
"v_add_f32_dpp v45, v3, v46 quad_perm:[1,0,3,2] row_mask:0xf bank_mask:0xf\n"
"v_cndmask_b32_e64 v2, v46, v45, s[36:37]\n"
"v_cndmask_b32_e64 v3, v45, v46, s[36:37]\n"
"s_nop 0\n"
"v_add_f32_dpp v45, v2, v3 quad_perm:[2,3,0,1] row_mask:0xf bank_mask:0xf\n"
"v_and_b32 v47, 0xff, v0\n"
"v_lshl_add_u32 v47, v47, 1, s56\n"
"global_load_dwordx4 v[4:7], v47, s[22:23] sc1\n"
"global_load_dwordx4 v[8:11], v47, s[22:23] offset:16 sc1\n"
"v_mov_b32 v2, 0xff800000\n"
"v_cndmask_b32_e64 v45, v2, v45, s[46:47]\n"
"v_max3_f32 v2, v43, v44, v45\n"
"s_nop 1\n"
"v_max_f32_dpp v2, v2, v2 quad_perm:[1,0,3,2] row_mask:0xf bank_mask:0xf\n"
"s_nop 1\n"
"v_max_f32_dpp v2, v2, v2 quad_perm:[2,3,0,1] row_mask:0xf bank_mask:0xf\n"
"s_nop 1\n"
"v_max_f32_dpp v2, v2, v2 row_half_mirror row_mask:0xf bank_mask:0xf\n"
"s_nop 1\n"
"v_max_f32_dpp v2, v2, v2 row_mirror row_mask:0xf bank_mask:0xf\n"
"v_mov_b32 v3, v2\n"
"s_nop 1\n"
"v_permlane16_swap_b32 v3, v2\n"
"v_max_f32 v2, v2, v3\n"
"v_mov_b32 v3, v2\n"
"s_nop 1\n"
"v_permlane32_swap_b32 v3, v2\n"
"v_max_f32 v2, v2, v3\n"
"v_mul_f32 v3, s64, v2\n"
"v_mul_f32 v2, s65, v2\n"
"v_readfirstlane_b32 s54, v3\n"
"v_fma_f32 v43, v43, s64, v2\n"
"v_fma_f32 v44, v44, s64, v2\n"
"v_fma_f32 v45, v45, s64, v2\n"
"v_exp_f32 v43, v43\n"
"v_exp_f32 v44, v44\n"
"v_exp_f32 v45, v45\n"
"v_mov_b32 v3, 0xffff\n"
"v_not_b32 v47, v3\n"
"v_cndmask_b32_e64 v3, v3, v47, s[34:35]\n"
"v_cndmask_b32_e64 v28, 0, v3, s[80:81]\n"
"v_cndmask_b32_e64 v29, 0, v3, s[82:83]\n"
"v_cndmask_b32_e64 v30, 0, v3, s[84:85]\n"
"v_cndmask_b32_e64 v31, 0, v3, s[86:87]\n"
"v_cvt_pk_bf16_f32 v43, v43, v43\n"
"v_cvt_pk_bf16_f32 v44, v44, v44\n"
"v_cvt_pk_bf16_f32 v45, v45, v45\n"
"v_and_b32 v2, 0xffff0000, v43\n"
"v_and_b32 v3, 0xffff0000, v44\n"
"v_add_f32 v2, v2, v3\n"
"v_and_b32 v3, 0xffff0000, v45\n"
"v_add_f32 v2, v2, v3\n"
"s_nop 1\n"
"v_add_f32_dpp v2, v2, v2 quad_perm:[1,0,3,2] row_mask:0xf bank_mask:0xf\n"
"s_nop 1\n"
"v_add_f32_dpp v2, v2, v2 quad_perm:[2,3,0,1] row_mask:0xf bank_mask:0xf\n"
"s_nop 1\n"
"v_add_f32_dpp v2, v2, v2 row_half_mirror row_mask:0xf bank_mask:0xf\n"
"s_nop 1\n"
"v_add_f32_dpp v2, v2, v2 row_mirror row_mask:0xf bank_mask:0xf\n"
"v_mov_b32 v3, v2\n"
"s_nop 1\n"
"v_permlane16_swap_b32 v3, v2\n"
"v_add_f32 v2, v2, v3\n"
"v_mov_b32 v3, v2\n"
"s_nop 1\n"
"v_permlane32_swap_b32 v3, v2\n"
"v_add_f32 v2, v2, v3\n"
"s_nop 0\n"
"v_readfirstlane_b32 s55, v2\n"
"v_and_b32_dpp v20, v43, v28 row_newbcast:8 row_mask:0xf bank_mask:0xf\n"
"v_and_b32_dpp v21, v43, v29 row_newbcast:8 row_mask:0xf bank_mask:0xf\n"
"v_and_b32_dpp v22, v43, v30 row_newbcast:8 row_mask:0xf bank_mask:0xf\n"
"v_and_b32_dpp v23, v43, v31 row_newbcast:8 row_mask:0xf bank_mask:0xf\n"
"v_and_b32_dpp v24, v43, v28 row_newbcast:12 row_mask:0xf bank_mask:0xf\n"
"v_and_b32_dpp v25, v43, v29 row_newbcast:12 row_mask:0xf bank_mask:0xf\n"
"v_and_b32_dpp v26, v43, v30 row_newbcast:12 row_mask:0xf bank_mask:0xf\n"
"v_and_b32_dpp v27, v43, v31 row_newbcast:12 row_mask:0xf bank_mask:0xf\n"
"v_mfma_f32_16x16x32_bf16 v[36:39], a[84:87], v[20:23], 0\n"
"s_add_u32 s20, s16, 401408\n"
"s_addc_u32 s21, s17, 0\n"
"s_mov_b32 m0, s26\n"
"s_add_u32 s26, s26, 1024\n"
"s_mov_b64 exec, s[32:33]\n"
"global_load_lds_dwordx4 v0, s[20:21] nt\n"
"s_mov_b64 exec, -1\n"
"v_and_b32_dpp v20, v43, v28 row_newbcast:9 row_mask:0xf bank_mask:0xf\n"
"v_and_b32_dpp v21, v43, v29 row_newbcast:9 row_mask:0xf bank_mask:0xf\n"
"v_and_b32_dpp v22, v43, v30 row_newbcast:9 row_mask:0xf bank_mask:0xf\n"
"v_and_b32_dpp v23, v43, v31 row_newbcast:9 row_mask:0xf bank_mask:0xf\n"
"v_mfma_f32_16x16x32_bf16 v[36:39], a[88:91], v[24:27], v[36:39]\n"
"v_and_b32_dpp v24, v43, v28 row_newbcast:13 row_mask:0xf bank_mask:0xf\n"
"v_and_b32_dpp v25, v43, v29 row_newbcast:13 row_mask:0xf bank_mask:0xf\n"
"v_and_b32_dpp v26, v43, v30 row_newbcast:13 row_mask:0xf bank_mask:0xf\n"
"v_and_b32_dpp v27, v43, v31 row_newbcast:13 row_mask:0xf bank_mask:0xf\n"
"v_mfma_f32_16x16x32_bf16 v[36:39], a[92:95], v[20:23], v[36:39]\n"
"v_and_b32_dpp v20, v43, v28 row_newbcast:10 row_mask:0xf bank_mask:0xf\n"
"v_and_b32_dpp v21, v43, v29 row_newbcast:10 row_mask:0xf bank_mask:0xf\n"
"v_and_b32_dpp v22, v43, v30 row_newbcast:10 row_mask:0xf bank_mask:0xf\n"
"v_and_b32_dpp v23, v43, v31 row_newbcast:10 row_mask:0xf bank_mask:0xf\n"
"v_mfma_f32_16x16x32_bf16 v[36:39], a[96:99], v[24:27], v[36:39]\n"
"v_and_b32_dpp v24, v43, v28 row_newbcast:14 row_mask:0xf bank_mask:0xf\n"
"v_and_b32_dpp v25, v43, v29 row_newbcast:14 row_mask:0xf bank_mask:0xf\n"
"v_and_b32_dpp v26, v43, v30 row_newbcast:14 row_mask:0xf bank_mask:0xf\n"
"v_and_b32_dpp v27, v43, v31 row_newbcast:14 row_mask:0xf bank_mask:0xf\n"
"v_mfma_f32_16x16x32_bf16 v[36:39], a[100:103], v[20:23], v[36:39]\n"
"s_add_u32 s20, s16, 430080\n"
"s_addc_u32 s21, s17, 0\n"
"s_mov_b32 m0, s26\n"
"s_add_u32 s26, s26, 1024\n"
"s_cmp_eq_u32 s26, s79\n"
"s_cselect_b32 s26, s25, s26\n"
"s_mov_b64 exec, s[32:33]\n"
"global_load_lds_dwordx4 v0, s[20:21] nt\n"
"s_mov_b64 exec, -1\n"
"v_and_b32_dpp v20, v43, v28 row_newbcast:11 row_mask:0xf bank_mask:0xf\n"
"v_and_b32_dpp v21, v43, v29 row_newbcast:11 row_mask:0xf bank_mask:0xf\n"
"v_and_b32_dpp v22, v43, v30 row_newbcast:11 row_mask:0xf bank_mask:0xf\n"
"v_and_b32_dpp v23, v43, v31 row_newbcast:11 row_mask:0xf bank_mask:0xf\n"
"v_mfma_f32_16x16x32_bf16 v[36:39], a[104:107], v[24:27], v[36:39]\n"
"v_and_b32_dpp v24, v43, v28 row_newbcast:15 row_mask:0xf bank_mask:0xf\n"
"v_and_b32_dpp v25, v43, v29 row_newbcast:15 row_mask:0xf bank_mask:0xf\n"
"v_and_b32_dpp v26, v43, v30 row_newbcast:15 row_mask:0xf bank_mask:0xf\n"
"v_and_b32_dpp v27, v43, v31 row_newbcast:15 row_mask:0xf bank_mask:0xf\n"
"v_mfma_f32_16x16x32_bf16 v[36:39], a[108:111], v[20:23], v[36:39]\n"
"v_and_b32_dpp v20, v44, v28 row_newbcast:8 row_mask:0xf bank_mask:0xf\n"
"v_and_b32_dpp v21, v44, v29 row_newbcast:8 row_mask:0xf bank_mask:0xf\n"
"v_and_b32_dpp v22, v44, v30 row_newbcast:8 row_mask:0xf bank_mask:0xf\n"
"v_and_b32_dpp v23, v44, v31 row_newbcast:8 row_mask:0xf bank_mask:0xf\n"
"v_mfma_f32_16x16x32_bf16 v[36:39], a[112:115], v[24:27], v[36:39]\n"
"v_and_b32_dpp v24, v44, v28 row_newbcast:12 row_mask:0xf bank_mask:0xf\n"
"v_and_b32_dpp v25, v44, v29 row_newbcast:12 row_mask:0xf bank_mask:0xf\n"
"v_and_b32_dpp v26, v44, v30 row_newbcast:12 row_mask:0xf bank_mask:0xf\n"
"v_and_b32_dpp v27, v44, v31 row_newbcast:12 row_mask:0xf bank_mask:0xf\n"
"v_mfma_f32_16x16x32_bf16 v[36:39], a[116:119], v[20:23], v[36:39]\n"
"s_add_u32 s20, s16, 458752\n"
"s_addc_u32 s21, s17, 0\n"
"s_mov_b64 exec, s[32:33]\n"
"global_load_dwordx4 a[0:3], v0, s[20:21] offset:0 nt\n"
"s_mov_b64 exec, -1\n"
"v_and_b32_dpp v20, v44, v28 row_newbcast:9 row_mask:0xf bank_mask:0xf\n"
"v_and_b32_dpp v21, v44, v29 row_newbcast:9 row_mask:0xf bank_mask:0xf\n"
"v_and_b32_dpp v22, v44, v30 row_newbcast:9 row_mask:0xf bank_mask:0xf\n"
"v_and_b32_dpp v23, v44, v31 row_newbcast:9 row_mask:0xf bank_mask:0xf\n"
"v_mfma_f32_16x16x32_bf16 v[36:39], a[120:123], v[24:27], v[36:39]\n"
"v_and_b32_dpp v24, v44, v28 row_newbcast:13 row_mask:0xf bank_mask:0xf\n"
"v_and_b32_dpp v25, v44, v29 row_newbcast:13 row_mask:0xf bank_mask:0xf\n"
"v_and_b32_dpp v26, v44, v30 row_newbcast:13 row_mask:0xf bank_mask:0xf\n"
"v_and_b32_dpp v27, v44, v31 row_newbcast:13 row_mask:0xf bank_mask:0xf\n"
"v_mfma_f32_16x16x32_bf16 v[36:39], a[124:127], v[20:23], v[36:39]\n"
"v_and_b32_dpp v20, v44, v28 row_newbcast:10 row_mask:0xf bank_mask:0xf\n"
"v_and_b32_dpp v21, v44, v29 row_newbcast:10 row_mask:0xf bank_mask:0xf\n"
"v_and_b32_dpp v22, v44, v30 row_newbcast:10 row_mask:0xf bank_mask:0xf\n"
"v_and_b32_dpp v23, v44, v31 row_newbcast:10 row_mask:0xf bank_mask:0xf\n"
"v_mfma_f32_16x16x32_bf16 v[36:39], a[128:131], v[24:27], v[36:39]\n"
"v_and_b32_dpp v24, v44, v28 row_newbcast:14 row_mask:0xf bank_mask:0xf\n"
"v_and_b32_dpp v25, v44, v29 row_newbcast:14 row_mask:0xf bank_mask:0xf\n"
"v_and_b32_dpp v26, v44, v30 row_newbcast:14 row_mask:0xf bank_mask:0xf\n"
"v_and_b32_dpp v27, v44, v31 row_newbcast:14 row_mask:0xf bank_mask:0xf\n"
"v_mfma_f32_16x16x32_bf16 v[36:39], a[132:135], v[20:23], v[36:39]\n"
"s_add_u32 s20, s16, 487424\n"
"s_addc_u32 s21, s17, 0\n"
"s_mov_b64 exec, s[32:33]\n"
"global_load_dwordx4 a[4:7], v0, s[20:21] offset:0 nt\n"
"s_mov_b64 exec, -1\n"
"v_and_b32_dpp v20, v44, v28 row_newbcast:11 row_mask:0xf bank_mask:0xf\n"
"v_and_b32_dpp v21, v44, v29 row_newbcast:11 row_mask:0xf bank_mask:0xf\n"
"v_and_b32_dpp v22, v44, v30 row_newbcast:11 row_mask:0xf bank_mask:0xf\n"
"v_and_b32_dpp v23, v44, v31 row_newbcast:11 row_mask:0xf bank_mask:0xf\n"
"v_mfma_f32_16x16x32_bf16 v[36:39], a[136:139], v[24:27], v[36:39]\n"
"v_and_b32_dpp v24, v44, v28 row_newbcast:15 row_mask:0xf bank_mask:0xf\n"
"v_and_b32_dpp v25, v44, v29 row_newbcast:15 row_mask:0xf bank_mask:0xf\n"
"v_and_b32_dpp v26, v44, v30 row_newbcast:15 row_mask:0xf bank_mask:0xf\n"
"v_and_b32_dpp v27, v44, v31 row_newbcast:15 row_mask:0xf bank_mask:0xf\n"
"v_mfma_f32_16x16x32_bf16 v[36:39], a[140:143], v[20:23], v[36:39]\n"
"v_and_b32_dpp v20, v43, v28 row_newbcast:0 row_mask:0xf bank_mask:0xf\n"
"v_and_b32_dpp v21, v43, v29 row_newbcast:0 row_mask:0xf bank_mask:0xf\n"
"v_and_b32_dpp v22, v43, v30 row_newbcast:0 row_mask:0xf bank_mask:0xf\n"
"v_and_b32_dpp v23, v43, v31 row_newbcast:0 row_mask:0xf bank_mask:0xf\n"
"v_mfma_f32_16x16x32_bf16 v[36:39], a[144:147], v[24:27], v[36:39]\n"
"v_and_b32_dpp v24, v43, v28 row_newbcast:4 row_mask:0xf bank_mask:0xf\n"
"v_and_b32_dpp v25, v43, v29 row_newbcast:4 row_mask:0xf bank_mask:0xf\n"
"v_and_b32_dpp v26, v43, v30 row_newbcast:4 row_mask:0xf bank_mask:0xf\n"
"v_and_b32_dpp v27, v43, v31 row_newbcast:4 row_mask:0xf bank_mask:0xf\n"
"v_mfma_f32_16x16x32_bf16 v[36:39], a[148:151], v[20:23], v[36:39]\n"
"s_add_u32 s20, s16, 516096\n"
"s_addc_u32 s21, s17, 0\n"
"s_mov_b64 exec, s[32:33]\n"
"global_load_dwordx4 a[8:11], v0, s[20:21] offset:0 nt\n"
"s_mov_b64 exec, -1\n"
"v_and_b32_dpp v20, v43, v28 row_newbcast:1 row_mask:0xf bank_mask:0xf\n"
"v_and_b32_dpp v21, v43, v29 row_newbcast:1 row_mask:0xf bank_mask:0xf\n"
"v_and_b32_dpp v22, v43, v30 row_newbcast:1 row_mask:0xf bank_mask:0xf\n"
"v_and_b32_dpp v23, v43, v31 row_newbcast:1 row_mask:0xf bank_mask:0xf\n"
"v_mfma_f32_16x16x32_bf16 v[36:39], a[152:155], v[24:27], v[36:39]\n"
"v_and_b32_dpp v24, v43, v28 row_newbcast:5 row_mask:0xf bank_mask:0xf\n"
"v_and_b32_dpp v25, v43, v29 row_newbcast:5 row_mask:0xf bank_mask:0xf\n"
"v_and_b32_dpp v26, v43, v30 row_newbcast:5 row_mask:0xf bank_mask:0xf\n"
"v_and_b32_dpp v27, v43, v31 row_newbcast:5 row_mask:0xf bank_mask:0xf\n"
"v_mfma_f32_16x16x32_bf16 v[36:39], a[156:159], v[20:23], v[36:39]\n"
"v_and_b32_dpp v20, v43, v28 row_newbcast:2 row_mask:0xf bank_mask:0xf\n"
"v_and_b32_dpp v21, v43, v29 row_newbcast:2 row_mask:0xf bank_mask:0xf\n"
"v_and_b32_dpp v22, v43, v30 row_newbcast:2 row_mask:0xf bank_mask:0xf\n"
"v_and_b32_dpp v23, v43, v31 row_newbcast:2 row_mask:0xf bank_mask:0xf\n"
"v_mfma_f32_16x16x32_bf16 v[36:39], a[160:163], v[24:27], v[36:39]\n"
"v_and_b32_dpp v24, v43, v28 row_newbcast:6 row_mask:0xf bank_mask:0xf\n"
"v_and_b32_dpp v25, v43, v29 row_newbcast:6 row_mask:0xf bank_mask:0xf\n"
"v_and_b32_dpp v26, v43, v30 row_newbcast:6 row_mask:0xf bank_mask:0xf\n"
"v_and_b32_dpp v27, v43, v31 row_newbcast:6 row_mask:0xf bank_mask:0xf\n"
"v_mfma_f32_16x16x32_bf16 v[36:39], a[164:167], v[20:23], v[36:39]\n"
"s_add_u32 s20, s16, 544768\n"
"s_addc_u32 s21, s17, 0\n"
"s_mov_b64 exec, s[32:33]\n"
"global_load_dwordx4 a[12:15], v0, s[20:21] offset:0 nt\n"
"s_mov_b64 exec, -1\n"
"v_and_b32_dpp v20, v43, v28 row_newbcast:3 row_mask:0xf bank_mask:0xf\n"
"v_and_b32_dpp v21, v43, v29 row_newbcast:3 row_mask:0xf bank_mask:0xf\n"
"v_and_b32_dpp v22, v43, v30 row_newbcast:3 row_mask:0xf bank_mask:0xf\n"
"v_and_b32_dpp v23, v43, v31 row_newbcast:3 row_mask:0xf bank_mask:0xf\n"
"v_mfma_f32_16x16x32_bf16 v[36:39], a[168:171], v[24:27], v[36:39]\n"
"v_and_b32_dpp v24, v43, v28 row_newbcast:7 row_mask:0xf bank_mask:0xf\n"
"v_and_b32_dpp v25, v43, v29 row_newbcast:7 row_mask:0xf bank_mask:0xf\n"
"v_and_b32_dpp v26, v43, v30 row_newbcast:7 row_mask:0xf bank_mask:0xf\n"
"v_and_b32_dpp v27, v43, v31 row_newbcast:7 row_mask:0xf bank_mask:0xf\n"
"v_mfma_f32_16x16x32_bf16 v[36:39], a[172:175], v[20:23], v[36:39]\n"
"v_and_b32_dpp v20, v44, v28 row_newbcast:0 row_mask:0xf bank_mask:0xf\n"
"v_and_b32_dpp v21, v44, v29 row_newbcast:0 row_mask:0xf bank_mask:0xf\n"
"v_and_b32_dpp v22, v44, v30 row_newbcast:0 row_mask:0xf bank_mask:0xf\n"
"v_and_b32_dpp v23, v44, v31 row_newbcast:0 row_mask:0xf bank_mask:0xf\n"
"v_mfma_f32_16x16x32_bf16 v[36:39], a[176:179], v[24:27], v[36:39]\n"
"v_and_b32_dpp v24, v44, v28 row_newbcast:4 row_mask:0xf bank_mask:0xf\n"
"v_and_b32_dpp v25, v44, v29 row_newbcast:4 row_mask:0xf bank_mask:0xf\n"
"v_and_b32_dpp v26, v44, v30 row_newbcast:4 row_mask:0xf bank_mask:0xf\n"
"v_and_b32_dpp v27, v44, v31 row_newbcast:4 row_mask:0xf bank_mask:0xf\n"
"v_mfma_f32_16x16x32_bf16 v[36:39], a[180:183], v[20:23], v[36:39]\n"
"s_add_u32 s20, s16, 573440\n"
"s_addc_u32 s21, s17, 0\n"
"s_mov_b64 exec, s[32:33]\n"
"global_load_dwordx4 a[16:19], v0, s[20:21] offset:0 nt\n"
"s_mov_b64 exec, -1\n"
"v_and_b32_dpp v20, v44, v28 row_newbcast:1 row_mask:0xf bank_mask:0xf\n"
"v_and_b32_dpp v21, v44, v29 row_newbcast:1 row_mask:0xf bank_mask:0xf\n"
"v_and_b32_dpp v22, v44, v30 row_newbcast:1 row_mask:0xf bank_mask:0xf\n"
"v_and_b32_dpp v23, v44, v31 row_newbcast:1 row_mask:0xf bank_mask:0xf\n"
"v_mfma_f32_16x16x32_bf16 v[36:39], a[184:187], v[24:27], v[36:39]\n"
"v_and_b32_dpp v24, v44, v28 row_newbcast:5 row_mask:0xf bank_mask:0xf\n"
"v_and_b32_dpp v25, v44, v29 row_newbcast:5 row_mask:0xf bank_mask:0xf\n"
"v_and_b32_dpp v26, v44, v30 row_newbcast:5 row_mask:0xf bank_mask:0xf\n"
"v_and_b32_dpp v27, v44, v31 row_newbcast:5 row_mask:0xf bank_mask:0xf\n"
"v_mfma_f32_16x16x32_bf16 v[36:39], a[188:191], v[20:23], v[36:39]\n"
"v_and_b32_dpp v20, v44, v28 row_newbcast:2 row_mask:0xf bank_mask:0xf\n"
"v_and_b32_dpp v21, v44, v29 row_newbcast:2 row_mask:0xf bank_mask:0xf\n"
"v_and_b32_dpp v22, v44, v30 row_newbcast:2 row_mask:0xf bank_mask:0xf\n"
"v_and_b32_dpp v23, v44, v31 row_newbcast:2 row_mask:0xf bank_mask:0xf\n"
"v_mfma_f32_16x16x32_bf16 v[36:39], a[192:195], v[24:27], v[36:39]\n"
"v_and_b32_dpp v24, v44, v28 row_newbcast:6 row_mask:0xf bank_mask:0xf\n"
"v_and_b32_dpp v25, v44, v29 row_newbcast:6 row_mask:0xf bank_mask:0xf\n"
"v_and_b32_dpp v26, v44, v30 row_newbcast:6 row_mask:0xf bank_mask:0xf\n"
"v_and_b32_dpp v27, v44, v31 row_newbcast:6 row_mask:0xf bank_mask:0xf\n"
"v_mfma_f32_16x16x32_bf16 v[36:39], a[196:199], v[20:23], v[36:39]\n"
"s_add_u32 s20, s16, 602112\n"
"s_addc_u32 s21, s17, 0\n"
"s_mov_b64 exec, s[32:33]\n"
"global_load_dwordx4 a[20:23], v0, s[20:21] offset:0 nt\n"
"s_mov_b64 exec, -1\n"
"v_and_b32_dpp v20, v44, v28 row_newbcast:3 row_mask:0xf bank_mask:0xf\n"
"v_and_b32_dpp v21, v44, v29 row_newbcast:3 row_mask:0xf bank_mask:0xf\n"
"v_and_b32_dpp v22, v44, v30 row_newbcast:3 row_mask:0xf bank_mask:0xf\n"
"v_and_b32_dpp v23, v44, v31 row_newbcast:3 row_mask:0xf bank_mask:0xf\n"
"v_mfma_f32_16x16x32_bf16 v[36:39], a[200:203], v[24:27], v[36:39]\n"
"v_and_b32_dpp v24, v44, v28 row_newbcast:7 row_mask:0xf bank_mask:0xf\n"
"v_and_b32_dpp v25, v44, v29 row_newbcast:7 row_mask:0xf bank_mask:0xf\n"
"v_and_b32_dpp v26, v44, v30 row_newbcast:7 row_mask:0xf bank_mask:0xf\n"
"v_and_b32_dpp v27, v44, v31 row_newbcast:7 row_mask:0xf bank_mask:0xf\n"
"v_mfma_f32_16x16x32_bf16 v[36:39], a[204:207], v[20:23], v[36:39]\n"
"v_and_b32_dpp v20, v45, v28 row_newbcast:0 row_mask:0xf bank_mask:0xf\n"
"v_and_b32_dpp v21, v45, v29 row_newbcast:0 row_mask:0xf bank_mask:0xf\n"
"v_and_b32_dpp v22, v45, v30 row_newbcast:0 row_mask:0xf bank_mask:0xf\n"
"v_and_b32_dpp v23, v45, v31 row_newbcast:0 row_mask:0xf bank_mask:0xf\n"
"v_mfma_f32_16x16x32_bf16 v[36:39], a[208:211], v[24:27], v[36:39]\n"
"v_and_b32_dpp v24, v45, v28 row_newbcast:4 row_mask:0xf bank_mask:0xf\n"
"v_and_b32_dpp v25, v45, v29 row_newbcast:4 row_mask:0xf bank_mask:0xf\n"
"v_and_b32_dpp v26, v45, v30 row_newbcast:4 row_mask:0xf bank_mask:0xf\n"
"v_and_b32_dpp v27, v45, v31 row_newbcast:4 row_mask:0xf bank_mask:0xf\n"
"v_mfma_f32_16x16x32_bf16 v[36:39], a[212:215], v[20:23], v[36:39]\n"
"s_add_u32 s20, s16, 630784\n"
"s_addc_u32 s21, s17, 0\n"
"s_mov_b64 exec, s[32:33]\n"
"global_load_dwordx4 a[24:27], v0, s[20:21] offset:0 nt\n"
"s_mov_b64 exec, -1\n"
"v_and_b32_dpp v20, v45, v28 row_newbcast:8 row_mask:0xf bank_mask:0xf\n"
"v_and_b32_dpp v21, v45, v29 row_newbcast:8 row_mask:0xf bank_mask:0xf\n"
"v_and_b32_dpp v22, v45, v30 row_newbcast:8 row_mask:0xf bank_mask:0xf\n"
"v_and_b32_dpp v23, v45, v31 row_newbcast:8 row_mask:0xf bank_mask:0xf\n"
"v_mfma_f32_16x16x32_bf16 v[36:39], a[216:219], v[24:27], v[36:39]\n"
"v_and_b32_dpp v24, v45, v28 row_newbcast:12 row_mask:0xf bank_mask:0xf\n"
"v_and_b32_dpp v25, v45, v29 row_newbcast:12 row_mask:0xf bank_mask:0xf\n"
"v_and_b32_dpp v26, v45, v30 row_newbcast:12 row_mask:0xf bank_mask:0xf\n"
"v_and_b32_dpp v27, v45, v31 row_newbcast:12 row_mask:0xf bank_mask:0xf\n"
"v_mfma_f32_16x16x32_bf16 v[36:39], a[220:223], v[20:23], v[36:39]\n"
"v_and_b32_dpp v20, v45, v28 row_newbcast:1 row_mask:0xf bank_mask:0xf\n"
"v_and_b32_dpp v21, v45, v29 row_newbcast:1 row_mask:0xf bank_mask:0xf\n"
"v_and_b32_dpp v22, v45, v30 row_newbcast:1 row_mask:0xf bank_mask:0xf\n"
"v_and_b32_dpp v23, v45, v31 row_newbcast:1 row_mask:0xf bank_mask:0xf\n"
"v_mfma_f32_16x16x32_bf16 v[36:39], a[224:227], v[24:27], v[36:39]\n"
"s_add_u32 s63, s62, s61\n"
"v_lshrrev_b32 v2, 8, v1\n"
"v_bfe_u32 v3, v1, 4, 4\n"
"v_lshlrev_b32 v2, 5, v2\n"
"v_add_u32 v2, v2, v3\n"
"v_lshl_add_u32 v2, v2, 2, s63\n"
"v_mov_b32 v3, s63\n"
"v_mfma_f32_16x16x32_bf16 v[36:39], a[228:231], v[20:23], v[36:39]\n"
"v_mov_b32 v46, s54\n"
"v_mov_b32 v47, s55\n"
"s_mov_b64 exec, s[48:49]\n"
"ds_write_b64 v3, v[46:47]\n"
"s_nop 3\n"
"ds_write_b32 v2, v36 offset:16\n"
"ds_write_b32 v2, v37 offset:48\n"
"ds_write_b32 v2, v38 offset:80\n"
"ds_write_b32 v2, v39 offset:112\n"
"s_mov_b64 exec, -1\n"
"s_add_u32 s63, s62, s78\n"
"v_mov_b32 v3, s62\n"
"v_bfe_u32 v47, v1, 4, 5\n"
"v_lshl_add_u32 v2, v47, 2, s63\n"
"s_waitcnt lgkmcnt(0)\n"
"s_barrier\n"
"ds_read_b64 v[20:21], v3 offset:0\n"
"ds_read_b64 v[22:23], v3 offset:528\n"
"ds_read_b64 v[24:25], v3 offset:1056\n"
"ds_read_b64 v[26:27], v3 offset:1584\n"
"ds_read_b32 v28, v2 offset:0\n"
"ds_read_b32 v29, v2 offset:528\n"
"ds_read_b32 v30, v2 offset:1056\n"
"ds_read_b32 v31, v2 offset:1584\n"
"s_xor_b32 s62, s62, 2112\n"
"s_add_u32 s63, s59, s72\n"
"s_sub_u32 s70, s73, s72\n"
"v_lshl_add_u32 v47, v47, 2, s63\n"
"v_mov_b32 v46, s70\n"
"v_cndmask_b32_e64 v46, 0, v46, s[66:67]\n"
"v_add_u32 v47, v47, v46\n"
"s_waitcnt lgkmcnt(4)\n"
"v_max3_f32 v2, v20, v22, v24\n"
"v_max_f32 v2, v2, v26\n"
"v_sub_f32 v20, v20, v2\n"
"v_sub_f32 v22, v22, v2\n"
"v_sub_f32 v24, v24, v2\n"
"v_sub_f32 v26, v26, v2\n"
"v_exp_f32 v20, v20\n"
"v_exp_f32 v22, v22\n"
"v_exp_f32 v24, v24\n"
"v_exp_f32 v26, v26\n"
"v_mul_f32 v3, v20, v21\n"
"v_fmac_f32 v3, v22, v23\n"
"v_fmac_f32 v3, v24, v25\n"
"v_fmac_f32 v3, v26, v27\n"
"s_waitcnt lgkmcnt(0)\n"
"v_mul_f32 v21, v20, v28\n"
"v_fmac_f32 v21, v22, v29\n"
"v_fmac_f32 v21, v24, v30\n"
"v_fmac_f32 v21, v26, v31\n"
"v_cndmask_b32_e64 v2, v2, v3, s[34:35]\n"
"v_cndmask_b32_e64 v21, v21, v2, s[66:67]\n"
"v_or_b32 v21, 1, v21\n"
"s_mov_b64 exec, s[50:51]\n"
"global_store_dword v47, v21, s[22:23] sc1\n"
"s_mov_b64 exec, -1\n"
"s_add_u32 s24, s24, 1\n"
"s_mov_b32 s28, s27\n"
"s_add_u32 s27, s27, 16384\n"
"s_sub_u32 s63, s27, 38912\n"
"s_cmp_ge_u32 s27, s79\n"
"s_cselect_b32 s27, s63, s27\n"
"s_sub_u32 s57, s57, 16384\n"
"s_add_u32 s58, s58, 262144\n"
"s_add_u32 s59, s59, 262144\n"
"s_cmp_eq_u32 s24, 8\n"
"s_nop 7\n"
"s_nop 3\n"
"s_cbranch_scc1 .Lst_epi\n"
"s_cmp_lt_u32 s24, 7\n"
"s_cselect_b64 s[32:33], -1, 1\n"
"s_cselect_b32 s68, 0x2000000, 0\n"
"s_cselect_b32 s89, 1, 0\n"
"s_waitcnt vmcnt(10)\n"
"v_bitop3_b32 v2, v4, v5, v6 bitop3:0x80\n"
"v_bitop3_b32 v3, v7, v8, v9 bitop3:0x80\n"
"v_bitop3_b32 v2, v2, v10, v11 bitop3:0x80\n"
"v_bitop3_b32 v2, v2, 1, v3 bitop3:0x80\n"
"v_cmp_ne_u32 vcc, 0, v2\n"
"s_nop 1\n"
"s_cmp_eq_u64 vcc, -1\n"
"s_cbranch_scc0 .Lst_qslow_70\n"
".Lst_qok_71:\n"
"v_perm_b32 v20, v5, v4, s69\n"
"v_perm_b32 v21, v7, v6, s69\n"
"v_perm_b32 v22, v9, v8, s69\n"
"v_perm_b32 v23, v11, v10, s69\n"
"v_cndmask_b32_e64 v4, 0, v20, s[38:39]\n"
"v_cndmask_b32_e64 v5, 0, v21, s[38:39]\n"
"v_cndmask_b32_e64 v6, 0, v22, s[38:39]\n"
"v_cndmask_b32_e64 v7, 0, v23, s[38:39]\n"
"v_cndmask_b32_e64 v8, 0, v20, s[40:41]\n"
"v_cndmask_b32_e64 v9, 0, v21, s[40:41]\n"
"v_cndmask_b32_e64 v10, 0, v22, s[40:41]\n"
"v_cndmask_b32_e64 v11, 0, v23, s[40:41]\n"
"v_cndmask_b32_e64 v12, 0, v20, s[42:43]\n"
"v_cndmask_b32_e64 v13, 0, v21, s[42:43]\n"
"v_cndmask_b32_e64 v14, 0, v22, s[42:43]\n"
"v_cndmask_b32_e64 v15, 0, v23, s[42:43]\n"
"v_cndmask_b32_e64 v16, 0, v20, s[44:45]\n"
"v_cndmask_b32_e64 v17, 0, v21, s[44:45]\n"
"v_cndmask_b32_e64 v18, 0, v22, s[44:45]\n"
"v_cndmask_b32_e64 v19, 0, v23, s[44:45]\n"
"s_cmp_lt_u32 s24, 7\n"
"s_cselect_b32 s63, 16384, 0\n"
"s_sub_u32 s56, s56, s63\n"
"s_mov_b32 s29, s27\n"
"s_waitcnt vmcnt(28)\n"
"v_add_u32 v47, s29, v1\n"
"ds_read_b128 v[28:31], v47\n"
"s_add_u32 s29, s29, 2048\n"
"s_cmp_eq_u32 s29, s79\n"
"s_cselect_b32 s29, s25, s29\n"
"s_cmp_eq_u32 s89, 0\n"
"s_cbranch_scc1 .Lst_th_74\n"
"s_waitcnt vmcnt(6)\n"
".Lst_th_74:\n"
"s_add_u32 s20, s16, 659456\n"
"s_addc_u32 s21, s17, 0\n"
"global_load_dwordx4 a[28:31], v0, s[20:21] offset:0 nt\n"
"s_waitcnt lgkmcnt(0)\n"
"v_mfma_f32_16x16x32_bf16 v[20:23], v[28:31], v[4:7], 0\n"
"s_waitcnt vmcnt(28)\n"
"ds_read_b128 v[36:39], v47 offset:1024\n"
"s_waitcnt lgkmcnt(0)\n"
"v_mfma_f32_16x16x32_bf16 v[20:23], v[36:39], v[8:11], v[20:23]\n"
"s_waitcnt vmcnt(27)\n"
"v_add_u32 v47, s29, v1\n"
"ds_read_b128 v[28:31], v47\n"
"s_add_u32 s29, s29, 2048\n"
"s_cmp_eq_u32 s29, s79\n"
"s_cselect_b32 s29, s25, s29\n"
"s_cmp_eq_u32 s89, 0\n"
"s_cbranch_scc1 .Lst_th_75\n"
"s_waitcnt vmcnt(6)\n"
".Lst_th_75:\n"
"s_add_u32 s20, s16, 688128\n"
"s_addc_u32 s21, s17, 0\n"
"global_load_dwordx4 a[32:35], v0, s[20:21] offset:0 nt\n"
"s_waitcnt lgkmcnt(0)\n"
"v_mfma_f32_16x16x32_bf16 v[20:23], v[28:31], v[12:15], v[20:23]\n"
"s_waitcnt vmcnt(27)\n"
"ds_read_b128 v[36:39], v47 offset:1024\n"
"s_waitcnt lgkmcnt(0)\n"
"v_mfma_f32_16x16x32_bf16 v[20:23], v[36:39], v[16:19], v[20:23]\n"
"s_nop 7\n"
"v_cndmask_b32_e64 v2, v20, v21, s[34:35]\n"
"v_cndmask_b32_e64 v3, v22, v23, s[34:35]\n"
"v_cndmask_b32_e64 v46, v2, v3, s[36:37]\n"
"s_waitcnt vmcnt(26)\n"
"v_add_u32 v47, s29, v1\n"
"ds_read_b128 v[28:31], v47\n"
"s_add_u32 s29, s29, 2048\n"
"s_cmp_eq_u32 s29, s79\n"
"s_cselect_b32 s29, s25, s29\n"
"s_cmp_eq_u32 s89, 0\n"
"s_cbranch_scc1 .Lst_th_76\n"
"s_waitcnt vmcnt(6)\n"
".Lst_th_76:\n"
"s_add_u32 s20, s16, 716800\n"
"s_addc_u32 s21, s17, 0\n"
"global_load_dwordx4 a[36:39], v0, s[20:21] offset:0 nt\n"
"s_waitcnt lgkmcnt(0)\n"
"v_mfma_f32_16x16x32_bf16 v[24:27], v[28:31], v[4:7], 0\n"
"s_waitcnt vmcnt(26)\n"
"ds_read_b128 v[36:39], v47 offset:1024\n"
"s_waitcnt lgkmcnt(0)\n"
"v_mfma_f32_16x16x32_bf16 v[24:27], v[36:39], v[8:11], v[24:27]\n"
"s_waitcnt vmcnt(22)\n"
"v_add_u32 v47, s29, v1\n"
"ds_read_b128 v[28:31], v47\n"
"s_add_u32 s29, s29, 2048\n"
"s_cmp_eq_u32 s29, s79\n"
"s_cselect_b32 s29, s25, s29\n"
"s_cmp_eq_u32 s89, 0\n"
"s_cbranch_scc1 .Lst_th_77\n"
"s_waitcnt vmcnt(6)\n"
".Lst_th_77:\n"
"s_add_u32 s20, s16, 745472\n"
"s_addc_u32 s21, s17, 0\n"
"global_load_dwordx4 a[40:43], v0, s[20:21] offset:0 nt\n"
"s_waitcnt lgkmcnt(0)\n"
"v_mfma_f32_16x16x32_bf16 v[24:27], v[28:31], v[12:15], v[24:27]\n"
"s_waitcnt vmcnt(22)\n"
"ds_read_b128 v[36:39], v47 offset:1024\n"
"s_waitcnt lgkmcnt(0)\n"
"v_mfma_f32_16x16x32_bf16 v[24:27], v[36:39], v[16:19], v[24:27]\n"
"s_nop 7\n"
"v_cndmask_b32_e64 v2, v24, v25, s[34:35]\n"
"v_cndmask_b32_e64 v3, v26, v27, s[34:35]\n"
"v_cndmask_b32_e64 v2, v2, v3, s[36:37]\n"
"v_cndmask_b32_e64 v3, v2, v46, s[34:35]\n"
"v_cndmask_b32_e64 v46, v46, v2, s[34:35]\n"
"s_nop 0\n"
"v_add_f32_dpp v40, v3, v46 quad_perm:[1,0,3,2] row_mask:0xf bank_mask:0xf\n"
"s_waitcnt vmcnt(21)\n"
"v_add_u32 v47, s29, v1\n"
"ds_read_b128 v[28:31], v47\n"
"s_add_u32 s29, s29, 2048\n"
"s_cmp_eq_u32 s29, s79\n"
"s_cselect_b32 s29, s25, s29\n"
"s_cmp_eq_u32 s89, 0\n"
"s_cbranch_scc1 .Lst_th_78\n"
"s_waitcnt vmcnt(6)\n"
".Lst_th_78:\n"
"s_add_u32 s20, s16, 774144\n"
"s_addc_u32 s21, s17, 0\n"
"global_load_dwordx4 a[44:47], v0, s[20:21] offset:0 nt\n"
"s_waitcnt lgkmcnt(0)\n"
"v_mfma_f32_16x16x32_bf16 v[20:23], v[28:31], v[4:7], 0\n"
"s_waitcnt vmcnt(21)\n"
"ds_read_b128 v[36:39], v47 offset:1024\n"
"s_waitcnt lgkmcnt(0)\n"
"v_mfma_f32_16x16x32_bf16 v[20:23], v[36:39], v[8:11], v[20:23]\n"
"s_waitcnt vmcnt(20)\n"
"v_add_u32 v47, s29, v1\n"
"ds_read_b128 v[28:31], v47\n"
"s_add_u32 s29, s29, 2048\n"
"s_cmp_eq_u32 s29, s79\n"
"s_cselect_b32 s29, s25, s29\n"
"s_cmp_eq_u32 s89, 0\n"
"s_cbranch_scc1 .Lst_th_79\n"
"s_waitcnt vmcnt(6)\n"
".Lst_th_79:\n"
"s_add_u32 s20, s16, 802816\n"
"s_addc_u32 s21, s17, 0\n"
"global_load_dwordx4 a[48:51], v0, s[20:21] offset:0 nt\n"
"s_waitcnt lgkmcnt(0)\n"
"v_mfma_f32_16x16x32_bf16 v[20:23], v[28:31], v[12:15], v[20:23]\n"
"s_waitcnt vmcnt(20)\n"
"ds_read_b128 v[36:39], v47 offset:1024\n"
"s_waitcnt lgkmcnt(0)\n"
"v_mfma_f32_16x16x32_bf16 v[20:23], v[36:39], v[16:19], v[20:23]\n"
"s_nop 7\n"
"v_cndmask_b32_e64 v2, v20, v21, s[34:35]\n"
"v_cndmask_b32_e64 v3, v22, v23, s[34:35]\n"
"v_cndmask_b32_e64 v46, v2, v3, s[36:37]\n"
"s_waitcnt vmcnt(19)\n"
"v_add_u32 v47, s29, v1\n"
"ds_read_b128 v[28:31], v47\n"
"s_add_u32 s29, s29, 2048\n"
"s_cmp_eq_u32 s29, s79\n"
"s_cselect_b32 s29, s25, s29\n"
"s_cmp_eq_u32 s89, 0\n"
"s_cbranch_scc1 .Lst_th_80\n"
"s_waitcnt vmcnt(6)\n"
".Lst_th_80:\n"
"s_add_u32 s20, s16, 831488\n"
"s_addc_u32 s21, s17, 0\n"
"global_load_dwordx4 a[52:55], v0, s[20:21] offset:0 nt\n"
"s_waitcnt lgkmcnt(0)\n"
"v_mfma_f32_16x16x32_bf16 v[24:27], v[28:31], v[4:7], 0\n"
"s_waitcnt vmcnt(19)\n"
"ds_read_b128 v[36:39], v47 offset:1024\n"
"s_waitcnt lgkmcnt(0)\n"
"v_mfma_f32_16x16x32_bf16 v[24:27], v[36:39], v[8:11], v[24:27]\n"
"s_waitcnt vmcnt(16)\n"
"v_add_u32 v47, s29, v1\n"
"ds_read_b128 v[28:31], v47\n"
"s_add_u32 s29, s29, 2048\n"
"s_cmp_eq_u32 s29, s79\n"
"s_cselect_b32 s29, s25, s29\n"
"s_cmp_eq_u32 s89, 0\n"
"s_cbranch_scc1 .Lst_th_81\n"
"s_waitcnt vmcnt(6)\n"
".Lst_th_81:\n"
"s_add_u32 s20, s16, 860160\n"
"s_addc_u32 s21, s17, 0\n"
"global_load_dwordx4 a[56:59], v0, s[20:21] offset:0 nt\n"
"s_waitcnt lgkmcnt(0)\n"
"v_mfma_f32_16x16x32_bf16 v[24:27], v[28:31], v[12:15], v[24:27]\n"
"s_waitcnt vmcnt(16)\n"
"ds_read_b128 v[36:39], v47 offset:1024\n"
"s_cmp_eq_u32 s89, 0\n"
"s_cbranch_scc1 .Lst_th_82\n"
"s_waitcnt vmcnt(6)\n"
".Lst_th_82:\n"
"s_add_u32 s20, s16, 888832\n"
"s_addc_u32 s21, s17, 0\n"
"global_load_dwordx4 a[60:63], v0, s[20:21] offset:0 nt\n"
"s_waitcnt lgkmcnt(0)\n"
"v_mfma_f32_16x16x32_bf16 v[24:27], v[36:39], v[16:19], v[24:27]\n"
"s_nop 7\n"
"v_cndmask_b32_e64 v2, v24, v25, s[34:35]\n"
"v_cndmask_b32_e64 v3, v26, v27, s[34:35]\n"
"v_cndmask_b32_e64 v2, v2, v3, s[36:37]\n"
"v_cndmask_b32_e64 v3, v2, v46, s[34:35]\n"
"v_cndmask_b32_e64 v46, v46, v2, s[34:35]\n"
"s_nop 0\n"
"v_add_f32_dpp v46, v3, v46 quad_perm:[1,0,3,2] row_mask:0xf bank_mask:0xf\n"
"v_cndmask_b32_e64 v2, v46, v40, s[36:37]\n"
"v_cndmask_b32_e64 v3, v40, v46, s[36:37]\n"
"s_nop 0\n"
"v_add_f32_dpp v40, v2, v3 quad_perm:[2,3,0,1] row_mask:0xf bank_mask:0xf\n"
"s_waitcnt vmcnt(16)\n"
"s_cmp_eq_u32 s89, 0\n"
"s_cbranch_scc1 .Lst_th_83\n"
"s_waitcnt vmcnt(6)\n"
".Lst_th_83:\n"
"s_add_u32 s20, s16, 917504\n"
"s_addc_u32 s21, s17, 0\n"
"global_load_dwordx4 a[64:67], v0, s[20:21] offset:0 nt\n"
"v_mfma_f32_16x16x32_bf16 v[20:23], a[0:3], v[4:7], 0\n"
"s_waitcnt vmcnt(16)\n"
"s_cmp_eq_u32 s89, 0\n"
"s_cbranch_scc1 .Lst_th_84\n"
"s_waitcnt vmcnt(6)\n"
".Lst_th_84:\n"
"s_add_u32 s20, s16, 946176\n"
"s_addc_u32 s21, s17, 0\n"
"global_load_dwordx4 a[68:71], v0, s[20:21] offset:0 nt\n"
"v_mfma_f32_16x16x32_bf16 v[20:23], a[4:7], v[8:11], v[20:23]\n"
"s_waitcnt vmcnt(16)\n"
"s_cmp_eq_u32 s89, 0\n"
"s_cbranch_scc1 .Lst_th_85\n"
"s_waitcnt vmcnt(6)\n"
".Lst_th_85:\n"
"s_add_u32 s20, s16, 974848\n"
"s_addc_u32 s21, s17, 0\n"
"global_load_dwordx4 a[72:75], v0, s[20:21] offset:0 nt\n"
"v_mfma_f32_16x16x32_bf16 v[20:23], a[8:11], v[12:15], v[20:23]\n"
"s_waitcnt vmcnt(16)\n"
"s_cmp_eq_u32 s89, 0\n"
"s_cbranch_scc1 .Lst_th_86\n"
"s_waitcnt vmcnt(6)\n"
".Lst_th_86:\n"
"s_add_u32 s20, s16, 1003520\n"
"s_addc_u32 s21, s17, 0\n"
"global_load_dwordx4 a[76:79], v0, s[20:21] offset:0 nt\n"
"v_mfma_f32_16x16x32_bf16 v[20:23], a[12:15], v[16:19], v[20:23]\n"
"s_nop 7\n"
"v_cndmask_b32_e64 v2, v20, v21, s[34:35]\n"
"v_cndmask_b32_e64 v3, v22, v23, s[34:35]\n"
"v_cndmask_b32_e64 v46, v2, v3, s[36:37]\n"
"s_waitcnt vmcnt(16)\n"
"s_cmp_eq_u32 s89, 0\n"
"s_cbranch_scc1 .Lst_th_87\n"
"s_waitcnt vmcnt(6)\n"
".Lst_th_87:\n"
"s_add_u32 s20, s16, s93\n"
"s_addc_u32 s21, s17, 0\n"
"s_mov_b64 exec, s[30:31]\n"
"global_load_dwordx4 a[80:83], v0, s[20:21] offset:0 nt\n"
"s_mov_b64 exec, -1\n"
"v_mfma_f32_16x16x32_bf16 v[24:27], a[16:19], v[4:7], 0\n"
"s_waitcnt vmcnt(16)\n"
"s_cmp_eq_u32 s89, 0\n"
"s_cbranch_scc1 .Lst_th_88\n"
"s_waitcnt vmcnt(6)\n"
".Lst_th_88:\n"
"s_add_u32 s20, s18, 0\n"
"s_addc_u32 s21, s19, 0\n"
"global_load_dwordx4 a[84:87], v1, s[20:21] offset:0 nt\n"
"v_mfma_f32_16x16x32_bf16 v[24:27], a[20:23], v[8:11], v[24:27]\n"
"s_waitcnt vmcnt(16)\n"
"s_cmp_eq_u32 s89, 0\n"
"s_cbranch_scc1 .Lst_th_89\n"
"s_waitcnt vmcnt(6)\n"
".Lst_th_89:\n"
"s_add_u32 s20, s18, 28672\n"
"s_addc_u32 s21, s19, 0\n"
"global_load_dwordx4 a[88:91], v1, s[20:21] offset:0 nt\n"
"v_mfma_f32_16x16x32_bf16 v[24:27], a[24:27], v[12:15], v[24:27]\n"
"s_waitcnt vmcnt(15)\n"
"s_cmp_eq_u32 s89, 0\n"
"s_cbranch_scc1 .Lst_th_90\n"
"s_waitcnt vmcnt(6)\n"
".Lst_th_90:\n"
"s_add_u32 s20, s18, 57344\n"
"s_addc_u32 s21, s19, 0\n"
"global_load_dwordx4 a[92:95], v1, s[20:21] offset:0 nt\n"
"v_mfma_f32_16x16x32_bf16 v[24:27], a[28:31], v[16:19], v[24:27]\n"
"s_nop 7\n"
"v_cndmask_b32_e64 v2, v24, v25, s[34:35]\n"
"v_cndmask_b32_e64 v3, v26, v27, s[34:35]\n"
"v_cndmask_b32_e64 v2, v2, v3, s[36:37]\n"
"v_cndmask_b32_e64 v3, v2, v46, s[34:35]\n"
"v_cndmask_b32_e64 v46, v46, v2, s[34:35]\n"
"s_nop 0\n"
"v_add_f32_dpp v41, v3, v46 quad_perm:[1,0,3,2] row_mask:0xf bank_mask:0xf\n"
"s_waitcnt vmcnt(15)\n"
"s_cmp_eq_u32 s89, 0\n"
"s_cbranch_scc1 .Lst_th_91\n"
"s_waitcnt vmcnt(6)\n"
".Lst_th_91:\n"
"s_add_u32 s20, s18, 86016\n"
"s_addc_u32 s21, s19, 0\n"
"global_load_dwordx4 a[96:99], v1, s[20:21] offset:0 nt\n"
"v_mfma_f32_16x16x32_bf16 v[20:23], a[32:35], v[4:7], 0\n"
"s_waitcnt vmcnt(15)\n"
"s_cmp_eq_u32 s89, 0\n"
"s_cbranch_scc1 .Lst_th_92\n"
"s_waitcnt vmcnt(6)\n"
".Lst_th_92:\n"
"s_add_u32 s20, s18, 114688\n"
"s_addc_u32 s21, s19, 0\n"
"global_load_dwordx4 a[100:103], v1, s[20:21] offset:0 nt\n"
"v_mfma_f32_16x16x32_bf16 v[20:23], a[36:39], v[8:11], v[20:23]\n"
"s_waitcnt vmcnt(15)\n"
"s_cmp_eq_u32 s89, 0\n"
"s_cbranch_scc1 .Lst_th_93\n"
"s_waitcnt vmcnt(6)\n"
".Lst_th_93:\n"
"s_add_u32 s20, s18, 143360\n"
"s_addc_u32 s21, s19, 0\n"
"global_load_dwordx4 a[104:107], v1, s[20:21] offset:0 nt\n"
"v_mfma_f32_16x16x32_bf16 v[20:23], a[40:43], v[12:15], v[20:23]\n"
"s_waitcnt vmcnt(15)\n"
"s_cmp_eq_u32 s89, 0\n"
"s_cbranch_scc1 .Lst_th_94\n"
"s_waitcnt vmcnt(6)\n"
".Lst_th_94:\n"
"s_add_u32 s20, s18, 172032\n"
"s_addc_u32 s21, s19, 0\n"
"global_load_dwordx4 a[108:111], v1, s[20:21] offset:0 nt\n"
"v_mfma_f32_16x16x32_bf16 v[20:23], a[44:47], v[16:19], v[20:23]\n"
"s_nop 7\n"
"v_cndmask_b32_e64 v2, v20, v21, s[34:35]\n"
"v_cndmask_b32_e64 v3, v22, v23, s[34:35]\n"
"v_cndmask_b32_e64 v46, v2, v3, s[36:37]\n"
"s_waitcnt vmcnt(15)\n"
"s_cmp_eq_u32 s89, 0\n"
"s_cbranch_scc1 .Lst_th_95\n"
"s_waitcnt vmcnt(6)\n"
".Lst_th_95:\n"
"s_add_u32 s20, s18, 200704\n"
"s_addc_u32 s21, s19, 0\n"
"global_load_dwordx4 a[112:115], v1, s[20:21] offset:0 nt\n"
"v_mfma_f32_16x16x32_bf16 v[24:27], a[48:51], v[4:7], 0\n"
"s_waitcnt vmcnt(15)\n"
"s_cmp_eq_u32 s89, 0\n"
"s_cbranch_scc1 .Lst_th_96\n"
"s_waitcnt vmcnt(6)\n"
".Lst_th_96:\n"
"s_add_u32 s20, s18, 229376\n"
"s_addc_u32 s21, s19, 0\n"
"global_load_dwordx4 a[116:119], v1, s[20:21] offset:0 nt\n"
"v_mfma_f32_16x16x32_bf16 v[24:27], a[52:55], v[8:11], v[24:27]\n"
"s_waitcnt vmcnt(15)\n"
"s_cmp_eq_u32 s89, 0\n"
"s_cbranch_scc1 .Lst_th_97\n"
"s_waitcnt vmcnt(6)\n"
".Lst_th_97:\n"
"s_add_u32 s20, s18, 258048\n"
"s_addc_u32 s21, s19, 0\n"
"global_load_dwordx4 a[120:123], v1, s[20:21] offset:0 nt\n"
"v_mfma_f32_16x16x32_bf16 v[24:27], a[56:59], v[12:15], v[24:27]\n"
"s_waitcnt vmcnt(15)\n"
"s_cmp_eq_u32 s89, 0\n"
"s_cbranch_scc1 .Lst_th_98\n"
"s_waitcnt vmcnt(6)\n"
".Lst_th_98:\n"
"s_add_u32 s20, s18, 286720\n"
"s_addc_u32 s21, s19, 0\n"
"global_load_dwordx4 a[124:127], v1, s[20:21] offset:0 nt\n"
"v_mfma_f32_16x16x32_bf16 v[24:27], a[60:63], v[16:19], v[24:27]\n"
"s_nop 7\n"
"v_cndmask_b32_e64 v2, v24, v25, s[34:35]\n"
"v_cndmask_b32_e64 v3, v26, v27, s[34:35]\n"
"v_cndmask_b32_e64 v2, v2, v3, s[36:37]\n"
"v_cndmask_b32_e64 v3, v2, v46, s[34:35]\n"
"v_cndmask_b32_e64 v46, v46, v2, s[34:35]\n"
"s_nop 0\n"
"v_add_f32_dpp v46, v3, v46 quad_perm:[1,0,3,2] row_mask:0xf bank_mask:0xf\n"
"v_cndmask_b32_e64 v2, v46, v41, s[36:37]\n"
"v_cndmask_b32_e64 v3, v41, v46, s[36:37]\n"
"s_nop 0\n"
"v_add_f32_dpp v41, v2, v3 quad_perm:[2,3,0,1] row_mask:0xf bank_mask:0xf\n"
"s_waitcnt vmcnt(15)\n"
"s_cmp_eq_u32 s89, 0\n"
"s_cbranch_scc1 .Lst_th_99\n"
"s_waitcnt vmcnt(6)\n"
".Lst_th_99:\n"
"s_add_u32 s20, s18, 315392\n"
"s_addc_u32 s21, s19, 0\n"
"global_load_dwordx4 a[128:131], v1, s[20:21] offset:0 nt\n"
"v_mfma_f32_16x16x32_bf16 v[20:23], a[64:67], v[4:7], 0\n"
"s_waitcnt vmcnt(15)\n"
"s_cmp_eq_u32 s89, 0\n"
"s_cbranch_scc1 .Lst_th_100\n"
"s_waitcnt vmcnt(6)\n"
".Lst_th_100:\n"
"s_add_u32 s20, s18, 344064\n"
"s_addc_u32 s21, s19, 0\n"
"global_load_dwordx4 a[132:135], v1, s[20:21] offset:0 nt\n"
"v_mfma_f32_16x16x32_bf16 v[20:23], a[68:71], v[8:11], v[20:23]\n"
"s_waitcnt vmcnt(15)\n"
"s_cmp_eq_u32 s89, 0\n"
"s_cbranch_scc1 .Lst_th_101\n"
"s_waitcnt vmcnt(6)\n"
".Lst_th_101:\n"
"s_add_u32 s20, s18, 372736\n"
"s_addc_u32 s21, s19, 0\n"
"global_load_dwordx4 a[136:139], v1, s[20:21] offset:0 nt\n"
"v_mfma_f32_16x16x32_bf16 v[20:23], a[72:75], v[12:15], v[20:23]\n"
"s_waitcnt vmcnt(15)\n"
"s_cmp_eq_u32 s89, 0\n"
"s_cbranch_scc1 .Lst_th_102\n"
"s_waitcnt vmcnt(6)\n"
".Lst_th_102:\n"
"s_add_u32 s20, s18, 401408\n"
"s_addc_u32 s21, s19, 0\n"
"global_load_dwordx4 a[140:143], v1, s[20:21] offset:0 nt\n"
"v_mfma_f32_16x16x32_bf16 v[20:23], a[76:79], v[16:19], v[20:23]\n"
"s_nop 7\n"
"v_cndmask_b32_e64 v2, v20, v21, s[34:35]\n"
"v_cndmask_b32_e64 v3, v22, v23, s[34:35]\n"
"v_cndmask_b32_e64 v46, v2, v3, s[36:37]\n"
"s_waitcnt vmcnt(15)\n"
"s_cmp_eq_u32 s89, 0\n"
"s_cbranch_scc1 .Lst_th_103\n"
"s_waitcnt vmcnt(6)\n"
".Lst_th_103:\n"
"s_add_u32 s20, s18, 430080\n"
"s_addc_u32 s21, s19, 0\n"
"global_load_dwordx4 a[144:147], v1, s[20:21] offset:0 nt\n"
"v_mfma_f32_16x16x32_bf16 v[24:27], a[80:83], v[4:7], 0\n"
"s_nop 7\n"
"v_cndmask_b32_e64 v2, v24, v25, s[34:35]\n"
"v_cndmask_b32_e64 v3, v26, v27, s[34:35]\n"
"v_cndmask_b32_e64 v2, v2, v3, s[36:37]\n"
"v_cndmask_b32_e64 v3, v2, v46, s[34:35]\n"
"v_cndmask_b32_e64 v46, v46, v2, s[34:35]\n"
"s_nop 0\n"
"v_add_f32_dpp v42, v3, v46 quad_perm:[1,0,3,2] row_mask:0xf bank_mask:0xf\n"
"v_cndmask_b32_e64 v2, v46, v42, s[36:37]\n"
"v_cndmask_b32_e64 v3, v42, v46, s[36:37]\n"
"s_nop 0\n"
"v_add_f32_dpp v42, v2, v3 quad_perm:[2,3,0,1] row_mask:0xf bank_mask:0xf\n"
"v_mov_b32 v2, 0xff800000\n"
"v_cndmask_b32_e64 v42, v2, v42, s[46:47]\n"
"v_max3_f32 v2, v40, v41, v42\n"
"s_nop 1\n"
"v_max_f32_dpp v2, v2, v2 quad_perm:[1,0,3,2] row_mask:0xf bank_mask:0xf\n"
"s_nop 1\n"
"v_max_f32_dpp v2, v2, v2 quad_perm:[2,3,0,1] row_mask:0xf bank_mask:0xf\n"
"s_nop 1\n"
"v_max_f32_dpp v2, v2, v2 row_half_mirror row_mask:0xf bank_mask:0xf\n"
"s_nop 1\n"
"v_max_f32_dpp v2, v2, v2 row_mirror row_mask:0xf bank_mask:0xf\n"
"v_mov_b32 v3, v2\n"
"s_nop 1\n"
"v_permlane16_swap_b32 v3, v2\n"
"v_max_f32 v2, v2, v3\n"
"v_mov_b32 v3, v2\n"
"s_nop 1\n"
"v_permlane32_swap_b32 v3, v2\n"
"v_max_f32 v2, v2, v3\n"
"v_mul_f32 v3, s64, v2\n"
"v_mul_f32 v2, s65, v2\n"
"v_readfirstlane_b32 s52, v3\n"
"v_fma_f32 v40, v40, s64, v2\n"
"v_fma_f32 v41, v41, s64, v2\n"
"v_fma_f32 v42, v42, s64, v2\n"
"v_exp_f32 v40, v40\n"
"v_exp_f32 v41, v41\n"
"v_exp_f32 v42, v42\n"
"v_mov_b32 v3, 0xffff\n"
"v_not_b32 v47, v3\n"
"v_cndmask_b32_e64 v3, v3, v47, s[34:35]\n"
"v_cndmask_b32_e64 v28, 0, v3, s[80:81]\n"
"v_cndmask_b32_e64 v29, 0, v3, s[82:83]\n"
"v_cndmask_b32_e64 v30, 0, v3, s[84:85]\n"
"v_cndmask_b32_e64 v31, 0, v3, s[86:87]\n"
"v_cvt_pk_bf16_f32 v40, v40, v40\n"
"v_cvt_pk_bf16_f32 v41, v41, v41\n"
"v_cvt_pk_bf16_f32 v42, v42, v42\n"
"v_and_b32 v2, 0xffff0000, v40\n"
"v_and_b32 v3, 0xffff0000, v41\n"
"v_add_f32 v2, v2, v3\n"
"v_and_b32 v3, 0xffff0000, v42\n"
"v_add_f32 v2, v2, v3\n"
"s_nop 1\n"
"v_add_f32_dpp v2, v2, v2 quad_perm:[1,0,3,2] row_mask:0xf bank_mask:0xf\n"
"s_nop 1\n"
"v_add_f32_dpp v2, v2, v2 quad_perm:[2,3,0,1] row_mask:0xf bank_mask:0xf\n"
"s_nop 1\n"
"v_add_f32_dpp v2, v2, v2 row_half_mirror row_mask:0xf bank_mask:0xf\n"
"s_nop 1\n"
"v_add_f32_dpp v2, v2, v2 row_mirror row_mask:0xf bank_mask:0xf\n"
"v_mov_b32 v3, v2\n"
"s_nop 1\n"
"v_permlane16_swap_b32 v3, v2\n"
"v_add_f32 v2, v2, v3\n"
"v_mov_b32 v3, v2\n"
"s_nop 1\n"
"v_permlane32_swap_b32 v3, v2\n"
"v_add_f32 v2, v2, v3\n"
"s_nop 0\n"
"v_readfirstlane_b32 s53, v2\n"
"v_and_b32_dpp v20, v40, v28 row_newbcast:0 row_mask:0xf bank_mask:0xf\n"
"v_and_b32_dpp v21, v40, v29 row_newbcast:0 row_mask:0xf bank_mask:0xf\n"
"v_and_b32_dpp v22, v40, v30 row_newbcast:0 row_mask:0xf bank_mask:0xf\n"
"v_and_b32_dpp v23, v40, v31 row_newbcast:0 row_mask:0xf bank_mask:0xf\n"
"s_waitcnt vmcnt(15)\n"
"s_cmp_eq_u32 s89, 0\n"
"s_cbranch_scc1 .Lst_th_104\n"
"s_waitcnt vmcnt(6)\n"
".Lst_th_104:\n"
"s_add_u32 s20, s18, 458752\n"
"s_addc_u32 s21, s19, 0\n"
"global_load_dwordx4 a[148:151], v1, s[20:21] offset:0 nt\n"
"v_mfma_f32_16x16x32_bf16 v[32:35], a[84:87], v[20:23], 0\n"
"v_and_b32_dpp v24, v40, v28 row_newbcast:4 row_mask:0xf bank_mask:0xf\n"
"v_and_b32_dpp v25, v40, v29 row_newbcast:4 row_mask:0xf bank_mask:0xf\n"
"v_and_b32_dpp v26, v40, v30 row_newbcast:4 row_mask:0xf bank_mask:0xf\n"
"v_and_b32_dpp v27, v40, v31 row_newbcast:4 row_mask:0xf bank_mask:0xf\n"
"s_waitcnt vmcnt(15)\n"
"s_cmp_eq_u32 s89, 0\n"
"s_cbranch_scc1 .Lst_th_105\n"
"s_waitcnt vmcnt(6)\n"
".Lst_th_105:\n"
"s_add_u32 s20, s18, 487424\n"
"s_addc_u32 s21, s19, 0\n"
"global_load_dwordx4 a[152:155], v1, s[20:21] offset:0 nt\n"
"v_mfma_f32_16x16x32_bf16 v[32:35], a[88:91], v[24:27], v[32:35]\n"
"v_and_b32_dpp v20, v40, v28 row_newbcast:8 row_mask:0xf bank_mask:0xf\n"
"v_and_b32_dpp v21, v40, v29 row_newbcast:8 row_mask:0xf bank_mask:0xf\n"
"v_and_b32_dpp v22, v40, v30 row_newbcast:8 row_mask:0xf bank_mask:0xf\n"
"v_and_b32_dpp v23, v40, v31 row_newbcast:8 row_mask:0xf bank_mask:0xf\n"
"s_waitcnt vmcnt(15)\n"
"s_cmp_eq_u32 s89, 0\n"
"s_cbranch_scc1 .Lst_th_106\n"
"s_waitcnt vmcnt(6)\n"
".Lst_th_106:\n"
"s_add_u32 s20, s18, 516096\n"
"s_addc_u32 s21, s19, 0\n"
"global_load_dwordx4 a[156:159], v1, s[20:21] offset:0 nt\n"
"v_mfma_f32_16x16x32_bf16 v[32:35], a[92:95], v[20:23], v[32:35]\n"
"v_and_b32_dpp v24, v40, v28 row_newbcast:12 row_mask:0xf bank_mask:0xf\n"
"v_and_b32_dpp v25, v40, v29 row_newbcast:12 row_mask:0xf bank_mask:0xf\n"
"v_and_b32_dpp v26, v40, v30 row_newbcast:12 row_mask:0xf bank_mask:0xf\n"
"v_and_b32_dpp v27, v40, v31 row_newbcast:12 row_mask:0xf bank_mask:0xf\n"
"s_waitcnt vmcnt(15)\n"
"s_cmp_eq_u32 s89, 0\n"
"s_cbranch_scc1 .Lst_th_107\n"
"s_waitcnt vmcnt(6)\n"
".Lst_th_107:\n"
"s_add_u32 s20, s18, 544768\n"
"s_addc_u32 s21, s19, 0\n"
"global_load_dwordx4 a[160:163], v1, s[20:21] offset:0 nt\n"
"v_mfma_f32_16x16x32_bf16 v[32:35], a[96:99], v[24:27], v[32:35]\n"
"v_and_b32_dpp v20, v40, v28 row_newbcast:1 row_mask:0xf bank_mask:0xf\n"
"v_and_b32_dpp v21, v40, v29 row_newbcast:1 row_mask:0xf bank_mask:0xf\n"
"v_and_b32_dpp v22, v40, v30 row_newbcast:1 row_mask:0xf bank_mask:0xf\n"
"v_and_b32_dpp v23, v40, v31 row_newbcast:1 row_mask:0xf bank_mask:0xf\n"
"s_waitcnt vmcnt(15)\n"
"s_cmp_eq_u32 s89, 0\n"
"s_cbranch_scc1 .Lst_th_108\n"
"s_waitcnt vmcnt(6)\n"
".Lst_th_108:\n"
"s_add_u32 s20, s18, 573440\n"
"s_addc_u32 s21, s19, 0\n"
"global_load_dwordx4 a[164:167], v1, s[20:21] offset:0 nt\n"
"v_mfma_f32_16x16x32_bf16 v[32:35], a[100:103], v[20:23], v[32:35]\n"
"v_and_b32_dpp v24, v40, v28 row_newbcast:5 row_mask:0xf bank_mask:0xf\n"
"v_and_b32_dpp v25, v40, v29 row_newbcast:5 row_mask:0xf bank_mask:0xf\n"
"v_and_b32_dpp v26, v40, v30 row_newbcast:5 row_mask:0xf bank_mask:0xf\n"
"v_and_b32_dpp v27, v40, v31 row_newbcast:5 row_mask:0xf bank_mask:0xf\n"
"s_waitcnt vmcnt(15)\n"
"s_cmp_eq_u32 s89, 0\n"
"s_cbranch_scc1 .Lst_th_109\n"
"s_waitcnt vmcnt(6)\n"
".Lst_th_109:\n"
"s_add_u32 s20, s18, 602112\n"
"s_addc_u32 s21, s19, 0\n"
"global_load_dwordx4 a[168:171], v1, s[20:21] offset:0 nt\n"
"v_mfma_f32_16x16x32_bf16 v[32:35], a[104:107], v[24:27], v[32:35]\n"
"v_and_b32_dpp v20, v40, v28 row_newbcast:9 row_mask:0xf bank_mask:0xf\n"
"v_and_b32_dpp v21, v40, v29 row_newbcast:9 row_mask:0xf bank_mask:0xf\n"
"v_and_b32_dpp v22, v40, v30 row_newbcast:9 row_mask:0xf bank_mask:0xf\n"
"v_and_b32_dpp v23, v40, v31 row_newbcast:9 row_mask:0xf bank_mask:0xf\n"
"s_waitcnt vmcnt(15)\n"
"s_cmp_eq_u32 s89, 0\n"
"s_cbranch_scc1 .Lst_th_110\n"
"s_waitcnt vmcnt(6)\n"
".Lst_th_110:\n"
"s_add_u32 s20, s18, 630784\n"
"s_addc_u32 s21, s19, 0\n"
"global_load_dwordx4 a[172:175], v1, s[20:21] offset:0 nt\n"
"v_mfma_f32_16x16x32_bf16 v[32:35], a[108:111], v[20:23], v[32:35]\n"
"v_and_b32_dpp v24, v40, v28 row_newbcast:13 row_mask:0xf bank_mask:0xf\n"
"v_and_b32_dpp v25, v40, v29 row_newbcast:13 row_mask:0xf bank_mask:0xf\n"
"v_and_b32_dpp v26, v40, v30 row_newbcast:13 row_mask:0xf bank_mask:0xf\n"
"v_and_b32_dpp v27, v40, v31 row_newbcast:13 row_mask:0xf bank_mask:0xf\n"
"s_waitcnt vmcnt(15)\n"
"s_cmp_eq_u32 s89, 0\n"
"s_cbranch_scc1 .Lst_th_111\n"
"s_waitcnt vmcnt(6)\n"
".Lst_th_111:\n"
"s_add_u32 s20, s18, 659456\n"
"s_addc_u32 s21, s19, 0\n"
"global_load_dwordx4 a[176:179], v1, s[20:21] offset:0 nt\n"
"v_mfma_f32_16x16x32_bf16 v[32:35], a[112:115], v[24:27], v[32:35]\n"
"v_and_b32_dpp v20, v40, v28 row_newbcast:2 row_mask:0xf bank_mask:0xf\n"
"v_and_b32_dpp v21, v40, v29 row_newbcast:2 row_mask:0xf bank_mask:0xf\n"
"v_and_b32_dpp v22, v40, v30 row_newbcast:2 row_mask:0xf bank_mask:0xf\n"
"v_and_b32_dpp v23, v40, v31 row_newbcast:2 row_mask:0xf bank_mask:0xf\n"
"s_waitcnt vmcnt(15)\n"
"s_cmp_eq_u32 s89, 0\n"
"s_cbranch_scc1 .Lst_th_112\n"
"s_waitcnt vmcnt(6)\n"
".Lst_th_112:\n"
"s_add_u32 s20, s18, 688128\n"
"s_addc_u32 s21, s19, 0\n"
"global_load_dwordx4 a[180:183], v1, s[20:21] offset:0 nt\n"
"v_mfma_f32_16x16x32_bf16 v[32:35], a[116:119], v[20:23], v[32:35]\n"
"v_and_b32_dpp v24, v40, v28 row_newbcast:6 row_mask:0xf bank_mask:0xf\n"
"v_and_b32_dpp v25, v40, v29 row_newbcast:6 row_mask:0xf bank_mask:0xf\n"
"v_and_b32_dpp v26, v40, v30 row_newbcast:6 row_mask:0xf bank_mask:0xf\n"
"v_and_b32_dpp v27, v40, v31 row_newbcast:6 row_mask:0xf bank_mask:0xf\n"
"s_waitcnt vmcnt(15)\n"
"s_cmp_eq_u32 s89, 0\n"
"s_cbranch_scc1 .Lst_th_113\n"
"s_waitcnt vmcnt(6)\n"
".Lst_th_113:\n"
"s_add_u32 s20, s18, 716800\n"
"s_addc_u32 s21, s19, 0\n"
"global_load_dwordx4 a[184:187], v1, s[20:21] offset:0 nt\n"
"v_mfma_f32_16x16x32_bf16 v[32:35], a[120:123], v[24:27], v[32:35]\n"
"v_and_b32_dpp v20, v40, v28 row_newbcast:10 row_mask:0xf bank_mask:0xf\n"
"v_and_b32_dpp v21, v40, v29 row_newbcast:10 row_mask:0xf bank_mask:0xf\n"
"v_and_b32_dpp v22, v40, v30 row_newbcast:10 row_mask:0xf bank_mask:0xf\n"
"v_and_b32_dpp v23, v40, v31 row_newbcast:10 row_mask:0xf bank_mask:0xf\n"
"s_waitcnt vmcnt(15)\n"
"s_cmp_eq_u32 s89, 0\n"
"s_cbranch_scc1 .Lst_th_114\n"
"s_waitcnt vmcnt(6)\n"
".Lst_th_114:\n"
"s_add_u32 s20, s18, 745472\n"
"s_addc_u32 s21, s19, 0\n"
"global_load_dwordx4 a[188:191], v1, s[20:21] offset:0 nt\n"
"v_mfma_f32_16x16x32_bf16 v[32:35], a[124:127], v[20:23], v[32:35]\n"
"v_and_b32_dpp v24, v40, v28 row_newbcast:14 row_mask:0xf bank_mask:0xf\n"
"v_and_b32_dpp v25, v40, v29 row_newbcast:14 row_mask:0xf bank_mask:0xf\n"
"v_and_b32_dpp v26, v40, v30 row_newbcast:14 row_mask:0xf bank_mask:0xf\n"
"v_and_b32_dpp v27, v40, v31 row_newbcast:14 row_mask:0xf bank_mask:0xf\n"
"s_waitcnt vmcnt(15)\n"
"s_cmp_eq_u32 s89, 0\n"
"s_cbranch_scc1 .Lst_th_115\n"
"s_waitcnt vmcnt(6)\n"
".Lst_th_115:\n"
"s_add_u32 s20, s18, 774144\n"
"s_addc_u32 s21, s19, 0\n"
"global_load_dwordx4 a[192:195], v1, s[20:21] offset:0 nt\n"
"v_mfma_f32_16x16x32_bf16 v[32:35], a[128:131], v[24:27], v[32:35]\n"
"v_and_b32_dpp v20, v40, v28 row_newbcast:3 row_mask:0xf bank_mask:0xf\n"
"v_and_b32_dpp v21, v40, v29 row_newbcast:3 row_mask:0xf bank_mask:0xf\n"
"v_and_b32_dpp v22, v40, v30 row_newbcast:3 row_mask:0xf bank_mask:0xf\n"
"v_and_b32_dpp v23, v40, v31 row_newbcast:3 row_mask:0xf bank_mask:0xf\n"
"s_waitcnt vmcnt(15)\n"
"s_cmp_eq_u32 s89, 0\n"
"s_cbranch_scc1 .Lst_th_116\n"
"s_waitcnt vmcnt(6)\n"
".Lst_th_116:\n"
"s_add_u32 s20, s18, 802816\n"
"s_addc_u32 s21, s19, 0\n"
"global_load_dwordx4 a[196:199], v1, s[20:21] offset:0 nt\n"
"v_mfma_f32_16x16x32_bf16 v[32:35], a[132:135], v[20:23], v[32:35]\n"
"v_and_b32_dpp v24, v40, v28 row_newbcast:7 row_mask:0xf bank_mask:0xf\n"
"v_and_b32_dpp v25, v40, v29 row_newbcast:7 row_mask:0xf bank_mask:0xf\n"
"v_and_b32_dpp v26, v40, v30 row_newbcast:7 row_mask:0xf bank_mask:0xf\n"
"v_and_b32_dpp v27, v40, v31 row_newbcast:7 row_mask:0xf bank_mask:0xf\n"
"s_waitcnt vmcnt(15)\n"
"s_cmp_eq_u32 s89, 0\n"
"s_cbranch_scc1 .Lst_th_117\n"
"s_waitcnt vmcnt(6)\n"
".Lst_th_117:\n"
"s_add_u32 s20, s18, 831488\n"
"s_addc_u32 s21, s19, 0\n"
"global_load_dwordx4 a[200:203], v1, s[20:21] offset:0 nt\n"
"v_mfma_f32_16x16x32_bf16 v[32:35], a[136:139], v[24:27], v[32:35]\n"
"v_and_b32_dpp v20, v40, v28 row_newbcast:11 row_mask:0xf bank_mask:0xf\n"
"v_and_b32_dpp v21, v40, v29 row_newbcast:11 row_mask:0xf bank_mask:0xf\n"
"v_and_b32_dpp v22, v40, v30 row_newbcast:11 row_mask:0xf bank_mask:0xf\n"
"v_and_b32_dpp v23, v40, v31 row_newbcast:11 row_mask:0xf bank_mask:0xf\n"
"s_waitcnt vmcnt(15)\n"
"s_cmp_eq_u32 s89, 0\n"
"s_cbranch_scc1 .Lst_th_118\n"
"s_waitcnt vmcnt(6)\n"
".Lst_th_118:\n"
"s_add_u32 s20, s18, 860160\n"
"s_addc_u32 s21, s19, 0\n"
"global_load_dwordx4 a[204:207], v1, s[20:21] offset:0 nt\n"
"v_mfma_f32_16x16x32_bf16 v[32:35], a[140:143], v[20:23], v[32:35]\n"
"v_and_b32_dpp v24, v40, v28 row_newbcast:15 row_mask:0xf bank_mask:0xf\n"
"v_and_b32_dpp v25, v40, v29 row_newbcast:15 row_mask:0xf bank_mask:0xf\n"
"v_and_b32_dpp v26, v40, v30 row_newbcast:15 row_mask:0xf bank_mask:0xf\n"
"v_and_b32_dpp v27, v40, v31 row_newbcast:15 row_mask:0xf bank_mask:0xf\n"
"s_waitcnt vmcnt(15)\n"
"s_cmp_eq_u32 s89, 0\n"
"s_cbranch_scc1 .Lst_th_119\n"
"s_waitcnt vmcnt(6)\n"
".Lst_th_119:\n"
"s_add_u32 s20, s18, 888832\n"
"s_addc_u32 s21, s19, 0\n"
"global_load_dwordx4 a[208:211], v1, s[20:21] offset:0 nt\n"
"v_mfma_f32_16x16x32_bf16 v[32:35], a[144:147], v[24:27], v[32:35]\n"
"v_and_b32_dpp v20, v41, v28 row_newbcast:0 row_mask:0xf bank_mask:0xf\n"
"v_and_b32_dpp v21, v41, v29 row_newbcast:0 row_mask:0xf bank_mask:0xf\n"
"v_and_b32_dpp v22, v41, v30 row_newbcast:0 row_mask:0xf bank_mask:0xf\n"
"v_and_b32_dpp v23, v41, v31 row_newbcast:0 row_mask:0xf bank_mask:0xf\n"
"s_waitcnt vmcnt(15)\n"
"s_cmp_eq_u32 s89, 0\n"
"s_cbranch_scc1 .Lst_th_120\n"
"s_waitcnt vmcnt(6)\n"
".Lst_th_120:\n"
"s_add_u32 s20, s18, 917504\n"
"s_addc_u32 s21, s19, 0\n"
"global_load_dwordx4 a[212:215], v1, s[20:21] offset:0 nt\n"
"v_mfma_f32_16x16x32_bf16 v[32:35], a[148:151], v[20:23], v[32:35]\n"
"v_and_b32_dpp v24, v41, v28 row_newbcast:4 row_mask:0xf bank_mask:0xf\n"
"v_and_b32_dpp v25, v41, v29 row_newbcast:4 row_mask:0xf bank_mask:0xf\n"
"v_and_b32_dpp v26, v41, v30 row_newbcast:4 row_mask:0xf bank_mask:0xf\n"
"v_and_b32_dpp v27, v41, v31 row_newbcast:4 row_mask:0xf bank_mask:0xf\n"
"s_waitcnt vmcnt(15)\n"
"s_cmp_eq_u32 s89, 0\n"
"s_cbranch_scc1 .Lst_th_121\n"
"s_waitcnt vmcnt(6)\n"
".Lst_th_121:\n"
"s_add_u32 s20, s18, 946176\n"
"s_addc_u32 s21, s19, 0\n"
"global_load_dwordx4 a[216:219], v1, s[20:21] offset:0 nt\n"
"v_mfma_f32_16x16x32_bf16 v[32:35], a[152:155], v[24:27], v[32:35]\n"
"v_and_b32_dpp v20, v41, v28 row_newbcast:8 row_mask:0xf bank_mask:0xf\n"
"v_and_b32_dpp v21, v41, v29 row_newbcast:8 row_mask:0xf bank_mask:0xf\n"
"v_and_b32_dpp v22, v41, v30 row_newbcast:8 row_mask:0xf bank_mask:0xf\n"
"v_and_b32_dpp v23, v41, v31 row_newbcast:8 row_mask:0xf bank_mask:0xf\n"
"s_waitcnt vmcnt(15)\n"
"s_cmp_eq_u32 s89, 0\n"
"s_cbranch_scc1 .Lst_th_122\n"
"s_waitcnt vmcnt(6)\n"
".Lst_th_122:\n"
"s_add_u32 s20, s18, 974848\n"
"s_addc_u32 s21, s19, 0\n"
"global_load_dwordx4 a[220:223], v1, s[20:21] offset:0 nt\n"
"v_mfma_f32_16x16x32_bf16 v[32:35], a[156:159], v[20:23], v[32:35]\n"
"v_and_b32_dpp v24, v41, v28 row_newbcast:12 row_mask:0xf bank_mask:0xf\n"
"v_and_b32_dpp v25, v41, v29 row_newbcast:12 row_mask:0xf bank_mask:0xf\n"
"v_and_b32_dpp v26, v41, v30 row_newbcast:12 row_mask:0xf bank_mask:0xf\n"
"v_and_b32_dpp v27, v41, v31 row_newbcast:12 row_mask:0xf bank_mask:0xf\n"
"s_waitcnt vmcnt(15)\n"
"s_cmp_eq_u32 s89, 0\n"
"s_cbranch_scc1 .Lst_th_123\n"
"s_waitcnt vmcnt(6)\n"
".Lst_th_123:\n"
"s_add_u32 s20, s18, 1003520\n"
"s_addc_u32 s21, s19, 0\n"
"global_load_dwordx4 a[224:227], v1, s[20:21] offset:0 nt\n"
"v_mfma_f32_16x16x32_bf16 v[32:35], a[160:163], v[24:27], v[32:35]\n"
"v_and_b32_dpp v20, v41, v28 row_newbcast:1 row_mask:0xf bank_mask:0xf\n"
"v_and_b32_dpp v21, v41, v29 row_newbcast:1 row_mask:0xf bank_mask:0xf\n"
"v_and_b32_dpp v22, v41, v30 row_newbcast:1 row_mask:0xf bank_mask:0xf\n"
"v_and_b32_dpp v23, v41, v31 row_newbcast:1 row_mask:0xf bank_mask:0xf\n"
"s_waitcnt vmcnt(15)\n"
"s_cmp_eq_u32 s89, 0\n"
"s_cbranch_scc1 .Lst_th_124\n"
"s_waitcnt vmcnt(6)\n"
".Lst_th_124:\n"
"s_add_u32 s20, s18, s93\n"
"s_addc_u32 s21, s19, 0\n"
"s_mov_b64 exec, s[30:31]\n"
"global_load_dwordx4 a[228:231], v1, s[20:21] offset:0 nt\n"
"s_mov_b64 exec, -1\n"
"v_mfma_f32_16x16x32_bf16 v[32:35], a[164:167], v[20:23], v[32:35]\n"
"v_and_b32_dpp v24, v41, v28 row_newbcast:5 row_mask:0xf bank_mask:0xf\n"
"v_and_b32_dpp v25, v41, v29 row_newbcast:5 row_mask:0xf bank_mask:0xf\n"
"v_and_b32_dpp v26, v41, v30 row_newbcast:5 row_mask:0xf bank_mask:0xf\n"
"v_and_b32_dpp v27, v41, v31 row_newbcast:5 row_mask:0xf bank_mask:0xf\n"
"s_waitcnt vmcnt(15)\n"
"s_cmp_eq_u32 s89, 0\n"
"s_cbranch_scc1 .Lst_th_125\n"
"s_waitcnt vmcnt(6)\n"
".Lst_th_125:\n"
"s_add_u32 s16, s16, s68\n"
"s_addc_u32 s17, s17, 0\n"
"s_add_u32 s18, s18, s68\n"
"s_addc_u32 s19, s19, 0\n"
"s_add_u32 s20, s16, 0\n"
"s_addc_u32 s21, s17, 0\n"
"s_mov_b32 m0, s26\n"
"s_add_u32 s26, s26, 1024\n"
"s_mov_b64 exec, s[32:33]\n"
"global_load_lds_dwordx4 v0, s[20:21] nt\n"
"s_mov_b64 exec, -1\n"
"v_mfma_f32_16x16x32_bf16 v[32:35], a[168:171], v[24:27], v[32:35]\n"
"v_and_b32_dpp v20, v41, v28 row_newbcast:9 row_mask:0xf bank_mask:0xf\n"
"v_and_b32_dpp v21, v41, v29 row_newbcast:9 row_mask:0xf bank_mask:0xf\n"
"v_and_b32_dpp v22, v41, v30 row_newbcast:9 row_mask:0xf bank_mask:0xf\n"
"v_and_b32_dpp v23, v41, v31 row_newbcast:9 row_mask:0xf bank_mask:0xf\n"
"s_waitcnt vmcnt(15)\n"
"s_cmp_eq_u32 s89, 0\n"
"s_cbranch_scc1 .Lst_th_126\n"
"s_waitcnt vmcnt(6)\n"
".Lst_th_126:\n"
"s_add_u32 s20, s16, 28672\n"
"s_addc_u32 s21, s17, 0\n"
"s_mov_b32 m0, s26\n"
"s_add_u32 s26, s26, 1024\n"
"s_cmp_eq_u32 s26, s79\n"
"s_cselect_b32 s26, s25, s26\n"
"s_mov_b64 exec, s[32:33]\n"
"global_load_lds_dwordx4 v0, s[20:21] nt\n"
"s_mov_b64 exec, -1\n"
"v_mfma_f32_16x16x32_bf16 v[32:35], a[172:175], v[20:23], v[32:35]\n"
"v_and_b32_dpp v24, v41, v28 row_newbcast:13 row_mask:0xf bank_mask:0xf\n"
"v_and_b32_dpp v25, v41, v29 row_newbcast:13 row_mask:0xf bank_mask:0xf\n"
"v_and_b32_dpp v26, v41, v30 row_newbcast:13 row_mask:0xf bank_mask:0xf\n"
"v_and_b32_dpp v27, v41, v31 row_newbcast:13 row_mask:0xf bank_mask:0xf\n"
"s_waitcnt vmcnt(15)\n"
"s_cmp_eq_u32 s89, 0\n"
"s_cbranch_scc1 .Lst_th_127\n"
"s_waitcnt vmcnt(6)\n"
".Lst_th_127:\n"
"s_add_u32 s20, s16, 57344\n"
"s_addc_u32 s21, s17, 0\n"
"s_mov_b32 m0, s26\n"
"s_add_u32 s26, s26, 1024\n"
"s_mov_b64 exec, s[32:33]\n"
"global_load_lds_dwordx4 v0, s[20:21] nt\n"
"s_mov_b64 exec, -1\n"
"v_mfma_f32_16x16x32_bf16 v[32:35], a[176:179], v[24:27], v[32:35]\n"
"v_and_b32_dpp v20, v41, v28 row_newbcast:2 row_mask:0xf bank_mask:0xf\n"
"v_and_b32_dpp v21, v41, v29 row_newbcast:2 row_mask:0xf bank_mask:0xf\n"
"v_and_b32_dpp v22, v41, v30 row_newbcast:2 row_mask:0xf bank_mask:0xf\n"
"v_and_b32_dpp v23, v41, v31 row_newbcast:2 row_mask:0xf bank_mask:0xf\n"
"s_waitcnt vmcnt(15)\n"
"s_cmp_eq_u32 s89, 0\n"
"s_cbranch_scc1 .Lst_th_128\n"
"s_waitcnt vmcnt(6)\n"
".Lst_th_128:\n"
"s_add_u32 s20, s16, 86016\n"
"s_addc_u32 s21, s17, 0\n"
"s_mov_b32 m0, s26\n"
"s_add_u32 s26, s26, 1024\n"
"s_cmp_eq_u32 s26, s79\n"
"s_cselect_b32 s26, s25, s26\n"
"s_mov_b64 exec, s[32:33]\n"
"global_load_lds_dwordx4 v0, s[20:21] nt\n"
"s_mov_b64 exec, -1\n"
"v_mfma_f32_16x16x32_bf16 v[32:35], a[180:183], v[20:23], v[32:35]\n"
"v_and_b32_dpp v24, v41, v28 row_newbcast:6 row_mask:0xf bank_mask:0xf\n"
"v_and_b32_dpp v25, v41, v29 row_newbcast:6 row_mask:0xf bank_mask:0xf\n"
"v_and_b32_dpp v26, v41, v30 row_newbcast:6 row_mask:0xf bank_mask:0xf\n"
"v_and_b32_dpp v27, v41, v31 row_newbcast:6 row_mask:0xf bank_mask:0xf\n"
"s_waitcnt vmcnt(15)\n"
"s_cmp_eq_u32 s89, 0\n"
"s_cbranch_scc1 .Lst_th_129\n"
"s_waitcnt vmcnt(6)\n"
".Lst_th_129:\n"
"s_add_u32 s20, s16, 114688\n"
"s_addc_u32 s21, s17, 0\n"
"s_mov_b32 m0, s26\n"
"s_add_u32 s26, s26, 1024\n"
"s_mov_b64 exec, s[32:33]\n"
"global_load_lds_dwordx4 v0, s[20:21] nt\n"
"s_mov_b64 exec, -1\n"
"v_mfma_f32_16x16x32_bf16 v[32:35], a[184:187], v[24:27], v[32:35]\n"
"v_and_b32_dpp v20, v41, v28 row_newbcast:10 row_mask:0xf bank_mask:0xf\n"
"v_and_b32_dpp v21, v41, v29 row_newbcast:10 row_mask:0xf bank_mask:0xf\n"
"v_and_b32_dpp v22, v41, v30 row_newbcast:10 row_mask:0xf bank_mask:0xf\n"
"v_and_b32_dpp v23, v41, v31 row_newbcast:10 row_mask:0xf bank_mask:0xf\n"
"s_waitcnt vmcnt(15)\n"
"s_cmp_eq_u32 s89, 0\n"
"s_cbranch_scc1 .Lst_th_130\n"
"s_waitcnt vmcnt(6)\n"
".Lst_th_130:\n"
"s_add_u32 s20, s16, 143360\n"
"s_addc_u32 s21, s17, 0\n"
"s_mov_b32 m0, s26\n"
"s_add_u32 s26, s26, 1024\n"
"s_cmp_eq_u32 s26, s79\n"
"s_cselect_b32 s26, s25, s26\n"
"s_mov_b64 exec, s[32:33]\n"
"global_load_lds_dwordx4 v0, s[20:21] nt\n"
"s_mov_b64 exec, -1\n"
"v_mfma_f32_16x16x32_bf16 v[32:35], a[188:191], v[20:23], v[32:35]\n"
"v_and_b32_dpp v24, v41, v28 row_newbcast:14 row_mask:0xf bank_mask:0xf\n"
"v_and_b32_dpp v25, v41, v29 row_newbcast:14 row_mask:0xf bank_mask:0xf\n"
"v_and_b32_dpp v26, v41, v30 row_newbcast:14 row_mask:0xf bank_mask:0xf\n"
"v_and_b32_dpp v27, v41, v31 row_newbcast:14 row_mask:0xf bank_mask:0xf\n"
"s_waitcnt vmcnt(15)\n"
"s_nop 0\n"
"v_mfma_f32_16x16x32_bf16 v[32:35], a[192:195], v[24:27], v[32:35]\n"
"v_and_b32_dpp v20, v41, v28 row_newbcast:3 row_mask:0xf bank_mask:0xf\n"
"v_and_b32_dpp v21, v41, v29 row_newbcast:3 row_mask:0xf bank_mask:0xf\n"
"v_and_b32_dpp v22, v41, v30 row_newbcast:3 row_mask:0xf bank_mask:0xf\n"
"v_and_b32_dpp v23, v41, v31 row_newbcast:3 row_mask:0xf bank_mask:0xf\n"
"s_waitcnt vmcnt(14)\n"
"s_nop 0\n"
"v_mfma_f32_16x16x32_bf16 v[32:35], a[196:199], v[20:23], v[32:35]\n"
"v_and_b32_dpp v24, v41, v28 row_newbcast:7 row_mask:0xf bank_mask:0xf\n"
"v_and_b32_dpp v25, v41, v29 row_newbcast:7 row_mask:0xf bank_mask:0xf\n"
"v_and_b32_dpp v26, v41, v30 row_newbcast:7 row_mask:0xf bank_mask:0xf\n"
"v_and_b32_dpp v27, v41, v31 row_newbcast:7 row_mask:0xf bank_mask:0xf\n"
"s_waitcnt vmcnt(13)\n"
"s_nop 0\n"
"v_mfma_f32_16x16x32_bf16 v[32:35], a[200:203], v[24:27], v[32:35]\n"
"v_and_b32_dpp v20, v41, v28 row_newbcast:11 row_mask:0xf bank_mask:0xf\n"
"v_and_b32_dpp v21, v41, v29 row_newbcast:11 row_mask:0xf bank_mask:0xf\n"
"v_and_b32_dpp v22, v41, v30 row_newbcast:11 row_mask:0xf bank_mask:0xf\n"
"v_and_b32_dpp v23, v41, v31 row_newbcast:11 row_mask:0xf bank_mask:0xf\n"
"s_waitcnt vmcnt(12)\n"
"s_nop 0\n"
"v_mfma_f32_16x16x32_bf16 v[32:35], a[204:207], v[20:23], v[32:35]\n"
"v_and_b32_dpp v24, v41, v28 row_newbcast:15 row_mask:0xf bank_mask:0xf\n"
"v_and_b32_dpp v25, v41, v29 row_newbcast:15 row_mask:0xf bank_mask:0xf\n"
"v_and_b32_dpp v26, v41, v30 row_newbcast:15 row_mask:0xf bank_mask:0xf\n"
"v_and_b32_dpp v27, v41, v31 row_newbcast:15 row_mask:0xf bank_mask:0xf\n"
"s_waitcnt vmcnt(11)\n"
"s_nop 0\n"
"v_mfma_f32_16x16x32_bf16 v[32:35], a[208:211], v[24:27], v[32:35]\n"
"v_and_b32_dpp v20, v42, v28 row_newbcast:0 row_mask:0xf bank_mask:0xf\n"
"v_and_b32_dpp v21, v42, v29 row_newbcast:0 row_mask:0xf bank_mask:0xf\n"
"v_and_b32_dpp v22, v42, v30 row_newbcast:0 row_mask:0xf bank_mask:0xf\n"
"v_and_b32_dpp v23, v42, v31 row_newbcast:0 row_mask:0xf bank_mask:0xf\n"
"s_waitcnt vmcnt(10)\n"
"s_nop 0\n"
"v_mfma_f32_16x16x32_bf16 v[32:35], a[212:215], v[20:23], v[32:35]\n"
"v_and_b32 v47, 0xff, v0\n"
"v_lshl_add_u32 v47, v47, 1, s57\n"
"global_load_dwordx4 v[4:7], v47, s[22:23] sc1\n"
"global_load_dwordx4 v[8:11], v47, s[22:23] offset:16 sc1\n"
"v_and_b32_dpp v24, v42, v28 row_newbcast:4 row_mask:0xf bank_mask:0xf\n"
"v_and_b32_dpp v25, v42, v29 row_newbcast:4 row_mask:0xf bank_mask:0xf\n"
"v_and_b32_dpp v26, v42, v30 row_newbcast:4 row_mask:0xf bank_mask:0xf\n"
"v_and_b32_dpp v27, v42, v31 row_newbcast:4 row_mask:0xf bank_mask:0xf\n"
"s_waitcnt vmcnt(11)\n"
"s_nop 0\n"
"v_mfma_f32_16x16x32_bf16 v[32:35], a[216:219], v[24:27], v[32:35]\n"
"v_and_b32_dpp v20, v42, v28 row_newbcast:8 row_mask:0xf bank_mask:0xf\n"
"v_and_b32_dpp v21, v42, v29 row_newbcast:8 row_mask:0xf bank_mask:0xf\n"
"v_and_b32_dpp v22, v42, v30 row_newbcast:8 row_mask:0xf bank_mask:0xf\n"
"v_and_b32_dpp v23, v42, v31 row_newbcast:8 row_mask:0xf bank_mask:0xf\n"
"s_waitcnt vmcnt(10)\n"
"s_nop 0\n"
"v_mfma_f32_16x16x32_bf16 v[32:35], a[220:223], v[20:23], v[32:35]\n"
"v_and_b32_dpp v24, v42, v28 row_newbcast:12 row_mask:0xf bank_mask:0xf\n"
"v_and_b32_dpp v25, v42, v29 row_newbcast:12 row_mask:0xf bank_mask:0xf\n"
"v_and_b32_dpp v26, v42, v30 row_newbcast:12 row_mask:0xf bank_mask:0xf\n"
"v_and_b32_dpp v27, v42, v31 row_newbcast:12 row_mask:0xf bank_mask:0xf\n"
"s_waitcnt vmcnt(9)\n"
"s_nop 0\n"
"v_mfma_f32_16x16x32_bf16 v[32:35], a[224:227], v[24:27], v[32:35]\n"
"v_and_b32_dpp v20, v42, v28 row_newbcast:1 row_mask:0xf bank_mask:0xf\n"
"v_and_b32_dpp v21, v42, v29 row_newbcast:1 row_mask:0xf bank_mask:0xf\n"
"v_and_b32_dpp v22, v42, v30 row_newbcast:1 row_mask:0xf bank_mask:0xf\n"
"v_and_b32_dpp v23, v42, v31 row_newbcast:1 row_mask:0xf bank_mask:0xf\n"
"s_add_u32 s63, s62, s61\n"
"v_lshrrev_b32 v2, 8, v1\n"
"v_bfe_u32 v3, v1, 4, 4\n"
"v_lshlrev_b32 v2, 5, v2\n"
"v_add_u32 v2, v2, v3\n"
"v_lshl_add_u32 v2, v2, 2, s63\n"
"v_mov_b32 v3, s63\n"
"s_waitcnt vmcnt(8)\n"
"v_mfma_f32_16x16x32_bf16 v[32:35], a[228:231], v[20:23], v[32:35]\n"
"v_mov_b32 v46, s52\n"
"v_mov_b32 v47, s53\n"
"s_mov_b64 exec, s[48:49]\n"
"ds_write_b64 v3, v[46:47]\n"
"s_nop 3\n"
"ds_write_b32 v2, v32 offset:16\n"
"ds_write_b32 v2, v33 offset:48\n"
"ds_write_b32 v2, v34 offset:80\n"
"ds_write_b32 v2, v35 offset:112\n"
"s_mov_b64 exec, -1\n"
"s_add_u32 s63, s62, s78\n"
"v_mov_b32 v3, s62\n"
"v_bfe_u32 v47, v1, 4, 5\n"
"v_lshl_add_u32 v2, v47, 2, s63\n"
"s_waitcnt lgkmcnt(0)\n"
"s_barrier\n"
"ds_read_b64 v[20:21], v3 offset:0\n"
"ds_read_b64 v[22:23], v3 offset:528\n"
"ds_read_b64 v[24:25], v3 offset:1056\n"
"ds_read_b64 v[26:27], v3 offset:1584\n"
"ds_read_b32 v28, v2 offset:0\n"
"ds_read_b32 v29, v2 offset:528\n"
"ds_read_b32 v30, v2 offset:1056\n"
"ds_read_b32 v31, v2 offset:1584\n"
"s_xor_b32 s62, s62, 2112\n"
"s_add_u32 s63, s58, s72\n"
"s_sub_u32 s70, s73, s72\n"
"v_lshl_add_u32 v47, v47, 2, s63\n"
"v_mov_b32 v46, s70\n"
"v_cndmask_b32_e64 v46, 0, v46, s[66:67]\n"
"v_add_u32 v47, v47, v46\n"
"s_waitcnt lgkmcnt(4)\n"
"v_max3_f32 v2, v20, v22, v24\n"
"v_max_f32 v2, v2, v26\n"
"v_sub_f32 v20, v20, v2\n"
"v_sub_f32 v22, v22, v2\n"
"v_sub_f32 v24, v24, v2\n"
"v_sub_f32 v26, v26, v2\n"
"v_exp_f32 v20, v20\n"
"v_exp_f32 v22, v22\n"
"v_exp_f32 v24, v24\n"
"v_exp_f32 v26, v26\n"
"v_mul_f32 v3, v20, v21\n"
"v_fmac_f32 v3, v22, v23\n"
"v_fmac_f32 v3, v24, v25\n"
"v_fmac_f32 v3, v26, v27\n"
"s_waitcnt lgkmcnt(0)\n"
"v_mul_f32 v21, v20, v28\n"
"v_fmac_f32 v21, v22, v29\n"
"v_fmac_f32 v21, v24, v30\n"
"v_fmac_f32 v21, v26, v31\n"
"v_cndmask_b32_e64 v2, v2, v3, s[34:35]\n"
"v_cndmask_b32_e64 v21, v21, v2, s[66:67]\n"
"v_or_b32 v21, 1, v21\n"
"s_mov_b64 exec, s[50:51]\n"
"global_store_dword v47, v21, s[22:23] sc1\n"
"s_mov_b64 exec, -1\n"
"s_waitcnt vmcnt(1)\n"
"v_bitop3_b32 v2, v4, v5, v6 bitop3:0x80\n"
"v_bitop3_b32 v3, v7, v8, v9 bitop3:0x80\n"
"v_bitop3_b32 v2, v2, v10, v11 bitop3:0x80\n"
"v_bitop3_b32 v2, v2, 1, v3 bitop3:0x80\n"
"v_cmp_ne_u32 vcc, 0, v2\n"
"s_nop 1\n"
"s_cmp_eq_u64 vcc, -1\n"
"s_cbranch_scc0 .Lst_qslow_131\n"
".Lst_qok_132:\n"
"v_perm_b32 v20, v5, v4, s69\n"
"v_perm_b32 v21, v7, v6, s69\n"
"v_perm_b32 v22, v9, v8, s69\n"
"v_perm_b32 v23, v11, v10, s69\n"
"v_cndmask_b32_e64 v4, 0, v20, s[38:39]\n"
"v_cndmask_b32_e64 v5, 0, v21, s[38:39]\n"
"v_cndmask_b32_e64 v6, 0, v22, s[38:39]\n"
"v_cndmask_b32_e64 v7, 0, v23, s[38:39]\n"
"v_cndmask_b32_e64 v8, 0, v20, s[40:41]\n"
"v_cndmask_b32_e64 v9, 0, v21, s[40:41]\n"
"v_cndmask_b32_e64 v10, 0, v22, s[40:41]\n"
"v_cndmask_b32_e64 v11, 0, v23, s[40:41]\n"
"v_cndmask_b32_e64 v12, 0, v20, s[42:43]\n"
"v_cndmask_b32_e64 v13, 0, v21, s[42:43]\n"
"v_cndmask_b32_e64 v14, 0, v22, s[42:43]\n"
"v_cndmask_b32_e64 v15, 0, v23, s[42:43]\n"
"v_cndmask_b32_e64 v16, 0, v20, s[44:45]\n"
"v_cndmask_b32_e64 v17, 0, v21, s[44:45]\n"
"v_cndmask_b32_e64 v18, 0, v22, s[44:45]\n"
"v_cndmask_b32_e64 v19, 0, v23, s[44:45]\n"
"s_mov_b32 s29, s28\n"
"v_add_u32 v47, s29, v1\n"
"ds_read_b128 v[28:31], v47\n"
"s_add_u32 s29, s29, 2048\n"
"s_cmp_eq_u32 s29, s79\n"
"s_cselect_b32 s29, s25, s29\n"
"ds_read_b128 v[36:39], v47 offset:1024\n"
"v_mfma_f32_16x16x32_bf16 v[20:23], a[232:235], v[4:7], 0\n"
"v_mfma_f32_16x16x32_bf16 v[20:23], a[236:239], v[8:11], v[20:23]\n"
"s_waitcnt lgkmcnt(1)\n"
"v_mfma_f32_16x16x32_bf16 v[20:23], v[28:31], v[12:15], v[20:23]\n"
"v_add_u32 v47, s29, v1\n"
"ds_read_b128 v[28:31], v47\n"
"s_add_u32 s29, s29, 2048\n"
"s_cmp_eq_u32 s29, s79\n"
"s_cselect_b32 s29, s25, s29\n"
"s_waitcnt lgkmcnt(1)\n"
"v_mfma_f32_16x16x32_bf16 v[20:23], v[36:39], v[16:19], v[20:23]\n"
"ds_read_b128 v[36:39], v47 offset:1024\n"
"s_add_u32 s20, s16, 172032\n"
"s_addc_u32 s21, s17, 0\n"
"s_mov_b32 m0, s26\n"
"s_add_u32 s26, s26, 1024\n"
"s_mov_b64 exec, s[32:33]\n"
"global_load_lds_dwordx4 v0, s[20:21] nt\n"
"s_mov_b64 exec, -1\n"
"v_mfma_f32_16x16x32_bf16 v[24:27], a[240:243], v[4:7], 0\n"
"v_mfma_f32_16x16x32_bf16 v[24:27], a[244:247], v[8:11], v[24:27]\n"
"s_waitcnt lgkmcnt(1)\n"
"v_mfma_f32_16x16x32_bf16 v[24:27], v[28:31], v[12:15], v[24:27]\n"
"v_add_u32 v47, s29, v1\n"
"ds_read_b128 v[28:31], v47\n"
"s_add_u32 s29, s29, 2048\n"
"s_cmp_eq_u32 s29, s79\n"
"s_cselect_b32 s29, s25, s29\n"
"s_waitcnt lgkmcnt(1)\n"
"v_mfma_f32_16x16x32_bf16 v[24:27], v[36:39], v[16:19], v[24:27]\n"
"ds_read_b128 v[36:39], v47 offset:1024\n"
"v_cndmask_b32_e64 v2, v20, v21, s[34:35]\n"
"v_cndmask_b32_e64 v3, v22, v23, s[34:35]\n"
"v_cndmask_b32_e64 v46, v2, v3, s[36:37]\n"
"s_add_u32 s20, s16, 200704\n"
"s_addc_u32 s21, s17, 0\n"
"s_mov_b32 m0, s26\n"
"s_add_u32 s26, s26, 1024\n"
"s_cmp_eq_u32 s26, s79\n"
"s_cselect_b32 s26, s25, s26\n"
"s_mov_b64 exec, s[32:33]\n"
"global_load_lds_dwordx4 v0, s[20:21] nt\n"
"s_mov_b64 exec, -1\n"
"v_mfma_f32_16x16x32_bf16 v[20:23], a[248:251], v[4:7], 0\n"
"v_mfma_f32_16x16x32_bf16 v[20:23], a[252:255], v[8:11], v[20:23]\n"
"s_waitcnt lgkmcnt(1)\n"
"v_mfma_f32_16x16x32_bf16 v[20:23], v[28:31], v[12:15], v[20:23]\n"
"v_add_u32 v47, s29, v1\n"
"ds_read_b128 v[28:31], v47\n"
"s_add_u32 s29, s29, 2048\n"
"s_cmp_eq_u32 s29, s79\n"
"s_cselect_b32 s29, s25, s29\n"
"s_waitcnt lgkmcnt(1)\n"
"v_mfma_f32_16x16x32_bf16 v[20:23], v[36:39], v[16:19], v[20:23]\n"
"ds_read_b128 v[36:39], v47 offset:1024\n"
"v_cndmask_b32_e64 v2, v24, v25, s[34:35]\n"
"v_cndmask_b32_e64 v3, v26, v27, s[34:35]\n"
"v_cndmask_b32_e64 v2, v2, v3, s[36:37]\n"
"v_cndmask_b32_e64 v3, v2, v46, s[34:35]\n"
"v_cndmask_b32_e64 v46, v46, v2, s[34:35]\n"
"s_nop 0\n"
"v_add_f32_dpp v43, v3, v46 quad_perm:[1,0,3,2] row_mask:0xf bank_mask:0xf\n"
"s_add_u32 s20, s16, 229376\n"
"s_addc_u32 s21, s17, 0\n"
"s_mov_b32 m0, s26\n"
"s_add_u32 s26, s26, 1024\n"
"s_mov_b64 exec, s[32:33]\n"
"global_load_lds_dwordx4 v0, s[20:21] nt\n"
"s_mov_b64 exec, -1\n"
"v_mfma_f32_16x16x32_bf16 v[24:27], v[48:51], v[4:7], 0\n"
"v_mfma_f32_16x16x32_bf16 v[24:27], v[52:55], v[8:11], v[24:27]\n"
"s_waitcnt lgkmcnt(1)\n"
"v_mfma_f32_16x16x32_bf16 v[24:27], v[28:31], v[12:15], v[24:27]\n"
"v_add_u32 v47, s29, v1\n"
"ds_read_b128 v[28:31], v47\n"
"s_add_u32 s29, s29, 2048\n"
"s_cmp_eq_u32 s29, s79\n"
"s_cselect_b32 s29, s25, s29\n"
"s_waitcnt lgkmcnt(1)\n"
"v_mfma_f32_16x16x32_bf16 v[24:27], v[36:39], v[16:19], v[24:27]\n"
"ds_read_b128 v[36:39], v47 offset:1024\n"
"v_cndmask_b32_e64 v2, v20, v21, s[34:35]\n"
"v_cndmask_b32_e64 v3, v22, v23, s[34:35]\n"
"v_cndmask_b32_e64 v46, v2, v3, s[36:37]\n"
"s_add_u32 s20, s16, 258048\n"
"s_addc_u32 s21, s17, 0\n"
"s_mov_b32 m0, s26\n"
"s_add_u32 s26, s26, 1024\n"
"s_cmp_eq_u32 s26, s79\n"
"s_cselect_b32 s26, s25, s26\n"
"s_mov_b64 exec, s[32:33]\n"
"global_load_lds_dwordx4 v0, s[20:21] nt\n"
"s_mov_b64 exec, -1\n"
"v_mfma_f32_16x16x32_bf16 v[20:23], v[56:59], v[4:7], 0\n"
"v_mfma_f32_16x16x32_bf16 v[20:23], v[60:63], v[8:11], v[20:23]\n"
"s_waitcnt lgkmcnt(1)\n"
"v_mfma_f32_16x16x32_bf16 v[20:23], v[28:31], v[12:15], v[20:23]\n"
"v_add_u32 v47, s29, v1\n"
"ds_read_b128 v[28:31], v47\n"
"s_add_u32 s29, s29, 2048\n"
"s_cmp_eq_u32 s29, s79\n"
"s_cselect_b32 s29, s25, s29\n"
"s_waitcnt lgkmcnt(1)\n"
"v_mfma_f32_16x16x32_bf16 v[20:23], v[36:39], v[16:19], v[20:23]\n"
"ds_read_b128 v[36:39], v47 offset:1024\n"
"v_cndmask_b32_e64 v2, v24, v25, s[34:35]\n"
"v_cndmask_b32_e64 v3, v26, v27, s[34:35]\n"
"v_cndmask_b32_e64 v2, v2, v3, s[36:37]\n"
"v_cndmask_b32_e64 v3, v2, v46, s[34:35]\n"
"v_cndmask_b32_e64 v46, v46, v2, s[34:35]\n"
"s_nop 0\n"
"v_add_f32_dpp v46, v3, v46 quad_perm:[1,0,3,2] row_mask:0xf bank_mask:0xf\n"
"v_cndmask_b32_e64 v2, v46, v43, s[36:37]\n"
"v_cndmask_b32_e64 v3, v43, v46, s[36:37]\n"
"s_nop 0\n"
"v_add_f32_dpp v43, v2, v3 quad_perm:[2,3,0,1] row_mask:0xf bank_mask:0xf\n"
"s_add_u32 s20, s16, 286720\n"
"s_addc_u32 s21, s17, 0\n"
"s_mov_b32 m0, s26\n"
"s_add_u32 s26, s26, 1024\n"
"s_mov_b64 exec, s[32:33]\n"
"global_load_lds_dwordx4 v0, s[20:21] nt\n"
"s_mov_b64 exec, -1\n"
"v_mfma_f32_16x16x32_bf16 v[24:27], v[64:67], v[4:7], 0\n"
"v_mfma_f32_16x16x32_bf16 v[24:27], v[68:71], v[8:11], v[24:27]\n"
"s_waitcnt lgkmcnt(1)\n"
"v_mfma_f32_16x16x32_bf16 v[24:27], v[28:31], v[12:15], v[24:27]\n"
"v_add_u32 v47, s29, v1\n"
"ds_read_b128 v[28:31], v47\n"
"s_add_u32 s29, s29, 2048\n"
"s_cmp_eq_u32 s29, s79\n"
"s_cselect_b32 s29, s25, s29\n"
"s_waitcnt lgkmcnt(1)\n"
"v_mfma_f32_16x16x32_bf16 v[24:27], v[36:39], v[16:19], v[24:27]\n"
"ds_read_b128 v[36:39], v47 offset:1024\n"
"v_cndmask_b32_e64 v2, v20, v21, s[34:35]\n"
"v_cndmask_b32_e64 v3, v22, v23, s[34:35]\n"
"v_cndmask_b32_e64 v46, v2, v3, s[36:37]\n"
"s_add_u32 s20, s16, 315392\n"
"s_addc_u32 s21, s17, 0\n"
"s_mov_b32 m0, s26\n"
"s_add_u32 s26, s26, 1024\n"
"s_cmp_eq_u32 s26, s79\n"
"s_cselect_b32 s26, s25, s26\n"
"s_mov_b64 exec, s[32:33]\n"
"global_load_lds_dwordx4 v0, s[20:21] nt\n"
"s_mov_b64 exec, -1\n"
"v_mfma_f32_16x16x32_bf16 v[20:23], v[72:75], v[4:7], 0\n"
"v_mfma_f32_16x16x32_bf16 v[20:23], v[76:79], v[8:11], v[20:23]\n"
"s_waitcnt lgkmcnt(1)\n"
"v_mfma_f32_16x16x32_bf16 v[20:23], v[28:31], v[12:15], v[20:23]\n"
"v_add_u32 v47, s29, v1\n"
"ds_read_b128 v[28:31], v47\n"
"s_add_u32 s29, s29, 2048\n"
"s_cmp_eq_u32 s29, s79\n"
"s_cselect_b32 s29, s25, s29\n"
"s_waitcnt lgkmcnt(1)\n"
"v_mfma_f32_16x16x32_bf16 v[20:23], v[36:39], v[16:19], v[20:23]\n"
"ds_read_b128 v[36:39], v47 offset:1024\n"
"v_cndmask_b32_e64 v2, v24, v25, s[34:35]\n"
"v_cndmask_b32_e64 v3, v26, v27, s[34:35]\n"
"v_cndmask_b32_e64 v2, v2, v3, s[36:37]\n"
"v_cndmask_b32_e64 v3, v2, v46, s[34:35]\n"
"v_cndmask_b32_e64 v46, v46, v2, s[34:35]\n"
"s_nop 0\n"
"v_add_f32_dpp v44, v3, v46 quad_perm:[1,0,3,2] row_mask:0xf bank_mask:0xf\n"
"s_add_u32 s20, s16, 344064\n"
"s_addc_u32 s21, s17, 0\n"
"s_mov_b32 m0, s26\n"
"s_add_u32 s26, s26, 1024\n"
"s_mov_b64 exec, s[32:33]\n"
"global_load_lds_dwordx4 v0, s[20:21] nt\n"
"s_mov_b64 exec, -1\n"
"v_mfma_f32_16x16x32_bf16 v[24:27], v[80:83], v[4:7], 0\n"
"v_mfma_f32_16x16x32_bf16 v[24:27], v[84:87], v[8:11], v[24:27]\n"
"s_waitcnt lgkmcnt(1)\n"
"v_mfma_f32_16x16x32_bf16 v[24:27], v[28:31], v[12:15], v[24:27]\n"
"s_waitcnt lgkmcnt(0)\n"
"v_mfma_f32_16x16x32_bf16 v[24:27], v[36:39], v[16:19], v[24:27]\n"
"v_cndmask_b32_e64 v2, v20, v21, s[34:35]\n"
"v_cndmask_b32_e64 v3, v22, v23, s[34:35]\n"
"v_cndmask_b32_e64 v46, v2, v3, s[36:37]\n"
"s_add_u32 s20, s16, 372736\n"
"s_addc_u32 s21, s17, 0\n"
"s_mov_b32 m0, s26\n"
"s_add_u32 s26, s26, 1024\n"
"s_cmp_eq_u32 s26, s79\n"
"s_cselect_b32 s26, s25, s26\n"
"s_mov_b64 exec, s[32:33]\n"
"global_load_lds_dwordx4 v0, s[20:21] nt\n"
"s_mov_b64 exec, -1\n"
"v_mfma_f32_16x16x32_bf16 v[20:23], v[88:91], v[4:7], 0\n"
"v_mfma_f32_16x16x32_bf16 v[20:23], v[92:95], v[8:11], v[20:23]\n"
"v_mfma_f32_16x16x32_bf16 v[20:23], v[96:99], v[12:15], v[20:23]\n"
"v_mfma_f32_16x16x32_bf16 v[20:23], v[100:103], v[16:19], v[20:23]\n"
"v_cndmask_b32_e64 v2, v24, v25, s[34:35]\n"
"v_cndmask_b32_e64 v3, v26, v27, s[34:35]\n"
"v_cndmask_b32_e64 v2, v2, v3, s[36:37]\n"
"v_cndmask_b32_e64 v3, v2, v46, s[34:35]\n"
"v_cndmask_b32_e64 v46, v46, v2, s[34:35]\n"
"s_nop 0\n"
"v_add_f32_dpp v46, v3, v46 quad_perm:[1,0,3,2] row_mask:0xf bank_mask:0xf\n"
"v_cndmask_b32_e64 v2, v46, v44, s[36:37]\n"
"v_cndmask_b32_e64 v3, v44, v46, s[36:37]\n"
"s_nop 0\n"
"v_add_f32_dpp v44, v2, v3 quad_perm:[2,3,0,1] row_mask:0xf bank_mask:0xf\n"
"v_mfma_f32_16x16x32_bf16 v[24:27], v[104:107], v[4:7], 0\n"
"v_cndmask_b32_e64 v2, v20, v21, s[34:35]\n"
"v_cndmask_b32_e64 v3, v22, v23, s[34:35]\n"
"v_cndmask_b32_e64 v46, v2, v3, s[36:37]\n"
"s_nop 4\n"
"v_cndmask_b32_e64 v2, v24, v25, s[34:35]\n"
"v_cndmask_b32_e64 v3, v26, v27, s[34:35]\n"
"v_cndmask_b32_e64 v2, v2, v3, s[36:37]\n"
"v_cndmask_b32_e64 v3, v2, v46, s[34:35]\n"
"v_cndmask_b32_e64 v46, v46, v2, s[34:35]\n"
"s_nop 0\n"
"v_add_f32_dpp v45, v3, v46 quad_perm:[1,0,3,2] row_mask:0xf bank_mask:0xf\n"
"v_cndmask_b32_e64 v2, v46, v45, s[36:37]\n"
"v_cndmask_b32_e64 v3, v45, v46, s[36:37]\n"
"s_nop 0\n"
"v_add_f32_dpp v45, v2, v3 quad_perm:[2,3,0,1] row_mask:0xf bank_mask:0xf\n"
"v_and_b32 v47, 0xff, v0\n"
"v_lshl_add_u32 v47, v47, 1, s56\n"
"global_load_dwordx4 v[4:7], v47, s[22:23] sc1\n"
"global_load_dwordx4 v[8:11], v47, s[22:23] offset:16 sc1\n"
"v_mov_b32 v2, 0xff800000\n"
"v_cndmask_b32_e64 v45, v2, v45, s[46:47]\n"
"v_max3_f32 v2, v43, v44, v45\n"
"s_nop 1\n"
"v_max_f32_dpp v2, v2, v2 quad_perm:[1,0,3,2] row_mask:0xf bank_mask:0xf\n"
"s_nop 1\n"
"v_max_f32_dpp v2, v2, v2 quad_perm:[2,3,0,1] row_mask:0xf bank_mask:0xf\n"
"s_nop 1\n"
"v_max_f32_dpp v2, v2, v2 row_half_mirror row_mask:0xf bank_mask:0xf\n"
"s_nop 1\n"
"v_max_f32_dpp v2, v2, v2 row_mirror row_mask:0xf bank_mask:0xf\n"
"v_mov_b32 v3, v2\n"
"s_nop 1\n"
"v_permlane16_swap_b32 v3, v2\n"
"v_max_f32 v2, v2, v3\n"
"v_mov_b32 v3, v2\n"
"s_nop 1\n"
"v_permlane32_swap_b32 v3, v2\n"
"v_max_f32 v2, v2, v3\n"
"v_mul_f32 v3, s64, v2\n"
"v_mul_f32 v2, s65, v2\n"
"v_readfirstlane_b32 s54, v3\n"
"v_fma_f32 v43, v43, s64, v2\n"
"v_fma_f32 v44, v44, s64, v2\n"
"v_fma_f32 v45, v45, s64, v2\n"
"v_exp_f32 v43, v43\n"
"v_exp_f32 v44, v44\n"
"v_exp_f32 v45, v45\n"
"v_mov_b32 v3, 0xffff\n"
"v_not_b32 v47, v3\n"
"v_cndmask_b32_e64 v3, v3, v47, s[34:35]\n"
"v_cndmask_b32_e64 v28, 0, v3, s[80:81]\n"
"v_cndmask_b32_e64 v29, 0, v3, s[82:83]\n"
"v_cndmask_b32_e64 v30, 0, v3, s[84:85]\n"
"v_cndmask_b32_e64 v31, 0, v3, s[86:87]\n"
"v_cvt_pk_bf16_f32 v43, v43, v43\n"
"v_cvt_pk_bf16_f32 v44, v44, v44\n"
"v_cvt_pk_bf16_f32 v45, v45, v45\n"
"v_and_b32 v2, 0xffff0000, v43\n"
"v_and_b32 v3, 0xffff0000, v44\n"
"v_add_f32 v2, v2, v3\n"
"v_and_b32 v3, 0xffff0000, v45\n"
"v_add_f32 v2, v2, v3\n"
"s_nop 1\n"
"v_add_f32_dpp v2, v2, v2 quad_perm:[1,0,3,2] row_mask:0xf bank_mask:0xf\n"
"s_nop 1\n"
"v_add_f32_dpp v2, v2, v2 quad_perm:[2,3,0,1] row_mask:0xf bank_mask:0xf\n"
"s_nop 1\n"
"v_add_f32_dpp v2, v2, v2 row_half_mirror row_mask:0xf bank_mask:0xf\n"
"s_nop 1\n"
"v_add_f32_dpp v2, v2, v2 row_mirror row_mask:0xf bank_mask:0xf\n"
"v_mov_b32 v3, v2\n"
"s_nop 1\n"
"v_permlane16_swap_b32 v3, v2\n"
"v_add_f32 v2, v2, v3\n"
"v_mov_b32 v3, v2\n"
"s_nop 1\n"
"v_permlane32_swap_b32 v3, v2\n"
"v_add_f32 v2, v2, v3\n"
"s_nop 0\n"
"v_readfirstlane_b32 s55, v2\n"
"v_and_b32_dpp v20, v43, v28 row_newbcast:8 row_mask:0xf bank_mask:0xf\n"
"v_and_b32_dpp v21, v43, v29 row_newbcast:8 row_mask:0xf bank_mask:0xf\n"
"v_and_b32_dpp v22, v43, v30 row_newbcast:8 row_mask:0xf bank_mask:0xf\n"
"v_and_b32_dpp v23, v43, v31 row_newbcast:8 row_mask:0xf bank_mask:0xf\n"
"v_and_b32_dpp v24, v43, v28 row_newbcast:12 row_mask:0xf bank_mask:0xf\n"
"v_and_b32_dpp v25, v43, v29 row_newbcast:12 row_mask:0xf bank_mask:0xf\n"
"v_and_b32_dpp v26, v43, v30 row_newbcast:12 row_mask:0xf bank_mask:0xf\n"
"v_and_b32_dpp v27, v43, v31 row_newbcast:12 row_mask:0xf bank_mask:0xf\n"
"v_mfma_f32_16x16x32_bf16 v[36:39], v[108:111], v[20:23], 0\n"
"s_add_u32 s20, s16, 401408\n"
"s_addc_u32 s21, s17, 0\n"
"s_mov_b32 m0, s26\n"
"s_add_u32 s26, s26, 1024\n"
"s_mov_b64 exec, s[32:33]\n"
"global_load_lds_dwordx4 v0, s[20:21] nt\n"
"s_mov_b64 exec, -1\n"
"v_and_b32_dpp v20, v43, v28 row_newbcast:9 row_mask:0xf bank_mask:0xf\n"
"v_and_b32_dpp v21, v43, v29 row_newbcast:9 row_mask:0xf bank_mask:0xf\n"
"v_and_b32_dpp v22, v43, v30 row_newbcast:9 row_mask:0xf bank_mask:0xf\n"
"v_and_b32_dpp v23, v43, v31 row_newbcast:9 row_mask:0xf bank_mask:0xf\n"
"v_mfma_f32_16x16x32_bf16 v[36:39], v[112:115], v[24:27], v[36:39]\n"
"v_and_b32_dpp v24, v43, v28 row_newbcast:13 row_mask:0xf bank_mask:0xf\n"
"v_and_b32_dpp v25, v43, v29 row_newbcast:13 row_mask:0xf bank_mask:0xf\n"
"v_and_b32_dpp v26, v43, v30 row_newbcast:13 row_mask:0xf bank_mask:0xf\n"
"v_and_b32_dpp v27, v43, v31 row_newbcast:13 row_mask:0xf bank_mask:0xf\n"
"v_mfma_f32_16x16x32_bf16 v[36:39], v[116:119], v[20:23], v[36:39]\n"
"v_and_b32_dpp v20, v43, v28 row_newbcast:10 row_mask:0xf bank_mask:0xf\n"
"v_and_b32_dpp v21, v43, v29 row_newbcast:10 row_mask:0xf bank_mask:0xf\n"
"v_and_b32_dpp v22, v43, v30 row_newbcast:10 row_mask:0xf bank_mask:0xf\n"
"v_and_b32_dpp v23, v43, v31 row_newbcast:10 row_mask:0xf bank_mask:0xf\n"
"v_mfma_f32_16x16x32_bf16 v[36:39], v[120:123], v[24:27], v[36:39]\n"
"v_and_b32_dpp v24, v43, v28 row_newbcast:14 row_mask:0xf bank_mask:0xf\n"
"v_and_b32_dpp v25, v43, v29 row_newbcast:14 row_mask:0xf bank_mask:0xf\n"
"v_and_b32_dpp v26, v43, v30 row_newbcast:14 row_mask:0xf bank_mask:0xf\n"
"v_and_b32_dpp v27, v43, v31 row_newbcast:14 row_mask:0xf bank_mask:0xf\n"
"v_mfma_f32_16x16x32_bf16 v[36:39], v[124:127], v[20:23], v[36:39]\n"
"s_add_u32 s20, s16, 430080\n"
"s_addc_u32 s21, s17, 0\n"
"s_mov_b32 m0, s26\n"
"s_add_u32 s26, s26, 1024\n"
"s_cmp_eq_u32 s26, s79\n"
"s_cselect_b32 s26, s25, s26\n"
"s_mov_b64 exec, s[32:33]\n"
"global_load_lds_dwordx4 v0, s[20:21] nt\n"
"s_mov_b64 exec, -1\n"
"v_and_b32_dpp v20, v43, v28 row_newbcast:11 row_mask:0xf bank_mask:0xf\n"
"v_and_b32_dpp v21, v43, v29 row_newbcast:11 row_mask:0xf bank_mask:0xf\n"
"v_and_b32_dpp v22, v43, v30 row_newbcast:11 row_mask:0xf bank_mask:0xf\n"
"v_and_b32_dpp v23, v43, v31 row_newbcast:11 row_mask:0xf bank_mask:0xf\n"
"v_mfma_f32_16x16x32_bf16 v[36:39], v[128:131], v[24:27], v[36:39]\n"
"v_and_b32_dpp v24, v43, v28 row_newbcast:15 row_mask:0xf bank_mask:0xf\n"
"v_and_b32_dpp v25, v43, v29 row_newbcast:15 row_mask:0xf bank_mask:0xf\n"
"v_and_b32_dpp v26, v43, v30 row_newbcast:15 row_mask:0xf bank_mask:0xf\n"
"v_and_b32_dpp v27, v43, v31 row_newbcast:15 row_mask:0xf bank_mask:0xf\n"
"v_mfma_f32_16x16x32_bf16 v[36:39], v[132:135], v[20:23], v[36:39]\n"
"v_and_b32_dpp v20, v44, v28 row_newbcast:8 row_mask:0xf bank_mask:0xf\n"
"v_and_b32_dpp v21, v44, v29 row_newbcast:8 row_mask:0xf bank_mask:0xf\n"
"v_and_b32_dpp v22, v44, v30 row_newbcast:8 row_mask:0xf bank_mask:0xf\n"
"v_and_b32_dpp v23, v44, v31 row_newbcast:8 row_mask:0xf bank_mask:0xf\n"
"v_mfma_f32_16x16x32_bf16 v[36:39], v[136:139], v[24:27], v[36:39]\n"
"v_and_b32_dpp v24, v44, v28 row_newbcast:12 row_mask:0xf bank_mask:0xf\n"
"v_and_b32_dpp v25, v44, v29 row_newbcast:12 row_mask:0xf bank_mask:0xf\n"
"v_and_b32_dpp v26, v44, v30 row_newbcast:12 row_mask:0xf bank_mask:0xf\n"
"v_and_b32_dpp v27, v44, v31 row_newbcast:12 row_mask:0xf bank_mask:0xf\n"
"v_mfma_f32_16x16x32_bf16 v[36:39], v[140:143], v[20:23], v[36:39]\n"
"s_add_u32 s20, s16, 458752\n"
"s_addc_u32 s21, s17, 0\n"
"s_mov_b64 exec, s[32:33]\n"
"global_load_dwordx4 a[232:235], v0, s[20:21] offset:0 nt\n"
"s_mov_b64 exec, -1\n"
"v_and_b32_dpp v20, v44, v28 row_newbcast:9 row_mask:0xf bank_mask:0xf\n"
"v_and_b32_dpp v21, v44, v29 row_newbcast:9 row_mask:0xf bank_mask:0xf\n"
"v_and_b32_dpp v22, v44, v30 row_newbcast:9 row_mask:0xf bank_mask:0xf\n"
"v_and_b32_dpp v23, v44, v31 row_newbcast:9 row_mask:0xf bank_mask:0xf\n"
"v_mfma_f32_16x16x32_bf16 v[36:39], v[144:147], v[24:27], v[36:39]\n"
"v_and_b32_dpp v24, v44, v28 row_newbcast:13 row_mask:0xf bank_mask:0xf\n"
"v_and_b32_dpp v25, v44, v29 row_newbcast:13 row_mask:0xf bank_mask:0xf\n"
"v_and_b32_dpp v26, v44, v30 row_newbcast:13 row_mask:0xf bank_mask:0xf\n"
"v_and_b32_dpp v27, v44, v31 row_newbcast:13 row_mask:0xf bank_mask:0xf\n"
"v_mfma_f32_16x16x32_bf16 v[36:39], v[148:151], v[20:23], v[36:39]\n"
"v_and_b32_dpp v20, v44, v28 row_newbcast:10 row_mask:0xf bank_mask:0xf\n"
"v_and_b32_dpp v21, v44, v29 row_newbcast:10 row_mask:0xf bank_mask:0xf\n"
"v_and_b32_dpp v22, v44, v30 row_newbcast:10 row_mask:0xf bank_mask:0xf\n"
"v_and_b32_dpp v23, v44, v31 row_newbcast:10 row_mask:0xf bank_mask:0xf\n"
"v_mfma_f32_16x16x32_bf16 v[36:39], v[152:155], v[24:27], v[36:39]\n"
"v_and_b32_dpp v24, v44, v28 row_newbcast:14 row_mask:0xf bank_mask:0xf\n"
"v_and_b32_dpp v25, v44, v29 row_newbcast:14 row_mask:0xf bank_mask:0xf\n"
"v_and_b32_dpp v26, v44, v30 row_newbcast:14 row_mask:0xf bank_mask:0xf\n"
"v_and_b32_dpp v27, v44, v31 row_newbcast:14 row_mask:0xf bank_mask:0xf\n"
"v_mfma_f32_16x16x32_bf16 v[36:39], v[156:159], v[20:23], v[36:39]\n"
"s_add_u32 s20, s16, 487424\n"
"s_addc_u32 s21, s17, 0\n"
"s_mov_b64 exec, s[32:33]\n"
"global_load_dwordx4 a[236:239], v0, s[20:21] offset:0 nt\n"
"s_mov_b64 exec, -1\n"
"v_and_b32_dpp v20, v44, v28 row_newbcast:11 row_mask:0xf bank_mask:0xf\n"
"v_and_b32_dpp v21, v44, v29 row_newbcast:11 row_mask:0xf bank_mask:0xf\n"
"v_and_b32_dpp v22, v44, v30 row_newbcast:11 row_mask:0xf bank_mask:0xf\n"
"v_and_b32_dpp v23, v44, v31 row_newbcast:11 row_mask:0xf bank_mask:0xf\n"
"v_mfma_f32_16x16x32_bf16 v[36:39], v[160:163], v[24:27], v[36:39]\n"
"v_and_b32_dpp v24, v44, v28 row_newbcast:15 row_mask:0xf bank_mask:0xf\n"
"v_and_b32_dpp v25, v44, v29 row_newbcast:15 row_mask:0xf bank_mask:0xf\n"
"v_and_b32_dpp v26, v44, v30 row_newbcast:15 row_mask:0xf bank_mask:0xf\n"
"v_and_b32_dpp v27, v44, v31 row_newbcast:15 row_mask:0xf bank_mask:0xf\n"
"v_mfma_f32_16x16x32_bf16 v[36:39], v[164:167], v[20:23], v[36:39]\n"
"v_and_b32_dpp v20, v43, v28 row_newbcast:0 row_mask:0xf bank_mask:0xf\n"
"v_and_b32_dpp v21, v43, v29 row_newbcast:0 row_mask:0xf bank_mask:0xf\n"
"v_and_b32_dpp v22, v43, v30 row_newbcast:0 row_mask:0xf bank_mask:0xf\n"
"v_and_b32_dpp v23, v43, v31 row_newbcast:0 row_mask:0xf bank_mask:0xf\n"
"v_mfma_f32_16x16x32_bf16 v[36:39], v[168:171], v[24:27], v[36:39]\n"
"v_and_b32_dpp v24, v43, v28 row_newbcast:4 row_mask:0xf bank_mask:0xf\n"
"v_and_b32_dpp v25, v43, v29 row_newbcast:4 row_mask:0xf bank_mask:0xf\n"
"v_and_b32_dpp v26, v43, v30 row_newbcast:4 row_mask:0xf bank_mask:0xf\n"
"v_and_b32_dpp v27, v43, v31 row_newbcast:4 row_mask:0xf bank_mask:0xf\n"
"v_mfma_f32_16x16x32_bf16 v[36:39], v[172:175], v[20:23], v[36:39]\n"
"s_add_u32 s20, s16, 516096\n"
"s_addc_u32 s21, s17, 0\n"
"s_mov_b64 exec, s[32:33]\n"
"global_load_dwordx4 a[240:243], v0, s[20:21] offset:0 nt\n"
"s_mov_b64 exec, -1\n"
"v_and_b32_dpp v20, v43, v28 row_newbcast:1 row_mask:0xf bank_mask:0xf\n"
"v_and_b32_dpp v21, v43, v29 row_newbcast:1 row_mask:0xf bank_mask:0xf\n"
"v_and_b32_dpp v22, v43, v30 row_newbcast:1 row_mask:0xf bank_mask:0xf\n"
"v_and_b32_dpp v23, v43, v31 row_newbcast:1 row_mask:0xf bank_mask:0xf\n"
"v_mfma_f32_16x16x32_bf16 v[36:39], v[176:179], v[24:27], v[36:39]\n"
"v_and_b32_dpp v24, v43, v28 row_newbcast:5 row_mask:0xf bank_mask:0xf\n"
"v_and_b32_dpp v25, v43, v29 row_newbcast:5 row_mask:0xf bank_mask:0xf\n"
"v_and_b32_dpp v26, v43, v30 row_newbcast:5 row_mask:0xf bank_mask:0xf\n"
"v_and_b32_dpp v27, v43, v31 row_newbcast:5 row_mask:0xf bank_mask:0xf\n"
"v_mfma_f32_16x16x32_bf16 v[36:39], v[180:183], v[20:23], v[36:39]\n"
"v_and_b32_dpp v20, v43, v28 row_newbcast:2 row_mask:0xf bank_mask:0xf\n"
"v_and_b32_dpp v21, v43, v29 row_newbcast:2 row_mask:0xf bank_mask:0xf\n"
"v_and_b32_dpp v22, v43, v30 row_newbcast:2 row_mask:0xf bank_mask:0xf\n"
"v_and_b32_dpp v23, v43, v31 row_newbcast:2 row_mask:0xf bank_mask:0xf\n"
"v_mfma_f32_16x16x32_bf16 v[36:39], v[184:187], v[24:27], v[36:39]\n"
"v_and_b32_dpp v24, v43, v28 row_newbcast:6 row_mask:0xf bank_mask:0xf\n"
"v_and_b32_dpp v25, v43, v29 row_newbcast:6 row_mask:0xf bank_mask:0xf\n"
"v_and_b32_dpp v26, v43, v30 row_newbcast:6 row_mask:0xf bank_mask:0xf\n"
"v_and_b32_dpp v27, v43, v31 row_newbcast:6 row_mask:0xf bank_mask:0xf\n"
"v_mfma_f32_16x16x32_bf16 v[36:39], v[188:191], v[20:23], v[36:39]\n"
"s_add_u32 s20, s16, 544768\n"
"s_addc_u32 s21, s17, 0\n"
"s_mov_b64 exec, s[32:33]\n"
"global_load_dwordx4 a[244:247], v0, s[20:21] offset:0 nt\n"
"s_mov_b64 exec, -1\n"
"v_and_b32_dpp v20, v43, v28 row_newbcast:3 row_mask:0xf bank_mask:0xf\n"
"v_and_b32_dpp v21, v43, v29 row_newbcast:3 row_mask:0xf bank_mask:0xf\n"
"v_and_b32_dpp v22, v43, v30 row_newbcast:3 row_mask:0xf bank_mask:0xf\n"
"v_and_b32_dpp v23, v43, v31 row_newbcast:3 row_mask:0xf bank_mask:0xf\n"
"v_mfma_f32_16x16x32_bf16 v[36:39], v[192:195], v[24:27], v[36:39]\n"
"v_and_b32_dpp v24, v43, v28 row_newbcast:7 row_mask:0xf bank_mask:0xf\n"
"v_and_b32_dpp v25, v43, v29 row_newbcast:7 row_mask:0xf bank_mask:0xf\n"
"v_and_b32_dpp v26, v43, v30 row_newbcast:7 row_mask:0xf bank_mask:0xf\n"
"v_and_b32_dpp v27, v43, v31 row_newbcast:7 row_mask:0xf bank_mask:0xf\n"
"v_mfma_f32_16x16x32_bf16 v[36:39], v[196:199], v[20:23], v[36:39]\n"
"v_and_b32_dpp v20, v44, v28 row_newbcast:0 row_mask:0xf bank_mask:0xf\n"
"v_and_b32_dpp v21, v44, v29 row_newbcast:0 row_mask:0xf bank_mask:0xf\n"
"v_and_b32_dpp v22, v44, v30 row_newbcast:0 row_mask:0xf bank_mask:0xf\n"
"v_and_b32_dpp v23, v44, v31 row_newbcast:0 row_mask:0xf bank_mask:0xf\n"
"v_mfma_f32_16x16x32_bf16 v[36:39], v[200:203], v[24:27], v[36:39]\n"
"v_and_b32_dpp v24, v44, v28 row_newbcast:4 row_mask:0xf bank_mask:0xf\n"
"v_and_b32_dpp v25, v44, v29 row_newbcast:4 row_mask:0xf bank_mask:0xf\n"
"v_and_b32_dpp v26, v44, v30 row_newbcast:4 row_mask:0xf bank_mask:0xf\n"
"v_and_b32_dpp v27, v44, v31 row_newbcast:4 row_mask:0xf bank_mask:0xf\n"
"v_mfma_f32_16x16x32_bf16 v[36:39], v[204:207], v[20:23], v[36:39]\n"
"s_add_u32 s20, s16, 573440\n"
"s_addc_u32 s21, s17, 0\n"
"s_mov_b64 exec, s[32:33]\n"
"global_load_dwordx4 a[248:251], v0, s[20:21] offset:0 nt\n"
"s_mov_b64 exec, -1\n"
"v_and_b32_dpp v20, v44, v28 row_newbcast:1 row_mask:0xf bank_mask:0xf\n"
"v_and_b32_dpp v21, v44, v29 row_newbcast:1 row_mask:0xf bank_mask:0xf\n"
"v_and_b32_dpp v22, v44, v30 row_newbcast:1 row_mask:0xf bank_mask:0xf\n"
"v_and_b32_dpp v23, v44, v31 row_newbcast:1 row_mask:0xf bank_mask:0xf\n"
"v_mfma_f32_16x16x32_bf16 v[36:39], v[208:211], v[24:27], v[36:39]\n"
"v_and_b32_dpp v24, v44, v28 row_newbcast:5 row_mask:0xf bank_mask:0xf\n"
"v_and_b32_dpp v25, v44, v29 row_newbcast:5 row_mask:0xf bank_mask:0xf\n"
"v_and_b32_dpp v26, v44, v30 row_newbcast:5 row_mask:0xf bank_mask:0xf\n"
"v_and_b32_dpp v27, v44, v31 row_newbcast:5 row_mask:0xf bank_mask:0xf\n"
"v_mfma_f32_16x16x32_bf16 v[36:39], v[212:215], v[20:23], v[36:39]\n"
"v_and_b32_dpp v20, v44, v28 row_newbcast:2 row_mask:0xf bank_mask:0xf\n"
"v_and_b32_dpp v21, v44, v29 row_newbcast:2 row_mask:0xf bank_mask:0xf\n"
"v_and_b32_dpp v22, v44, v30 row_newbcast:2 row_mask:0xf bank_mask:0xf\n"
"v_and_b32_dpp v23, v44, v31 row_newbcast:2 row_mask:0xf bank_mask:0xf\n"
"v_mfma_f32_16x16x32_bf16 v[36:39], v[216:219], v[24:27], v[36:39]\n"
"v_and_b32_dpp v24, v44, v28 row_newbcast:6 row_mask:0xf bank_mask:0xf\n"
"v_and_b32_dpp v25, v44, v29 row_newbcast:6 row_mask:0xf bank_mask:0xf\n"
"v_and_b32_dpp v26, v44, v30 row_newbcast:6 row_mask:0xf bank_mask:0xf\n"
"v_and_b32_dpp v27, v44, v31 row_newbcast:6 row_mask:0xf bank_mask:0xf\n"
"v_mfma_f32_16x16x32_bf16 v[36:39], v[220:223], v[20:23], v[36:39]\n"
"s_add_u32 s20, s16, 602112\n"
"s_addc_u32 s21, s17, 0\n"
"s_mov_b64 exec, s[32:33]\n"
"global_load_dwordx4 a[252:255], v0, s[20:21] offset:0 nt\n"
"s_mov_b64 exec, -1\n"
"v_and_b32_dpp v20, v44, v28 row_newbcast:3 row_mask:0xf bank_mask:0xf\n"
"v_and_b32_dpp v21, v44, v29 row_newbcast:3 row_mask:0xf bank_mask:0xf\n"
"v_and_b32_dpp v22, v44, v30 row_newbcast:3 row_mask:0xf bank_mask:0xf\n"
"v_and_b32_dpp v23, v44, v31 row_newbcast:3 row_mask:0xf bank_mask:0xf\n"
"v_mfma_f32_16x16x32_bf16 v[36:39], v[224:227], v[24:27], v[36:39]\n"
"v_and_b32_dpp v24, v44, v28 row_newbcast:7 row_mask:0xf bank_mask:0xf\n"
"v_and_b32_dpp v25, v44, v29 row_newbcast:7 row_mask:0xf bank_mask:0xf\n"
"v_and_b32_dpp v26, v44, v30 row_newbcast:7 row_mask:0xf bank_mask:0xf\n"
"v_and_b32_dpp v27, v44, v31 row_newbcast:7 row_mask:0xf bank_mask:0xf\n"
"v_mfma_f32_16x16x32_bf16 v[36:39], v[228:231], v[20:23], v[36:39]\n"
"v_and_b32_dpp v20, v45, v28 row_newbcast:0 row_mask:0xf bank_mask:0xf\n"
"v_and_b32_dpp v21, v45, v29 row_newbcast:0 row_mask:0xf bank_mask:0xf\n"
"v_and_b32_dpp v22, v45, v30 row_newbcast:0 row_mask:0xf bank_mask:0xf\n"
"v_and_b32_dpp v23, v45, v31 row_newbcast:0 row_mask:0xf bank_mask:0xf\n"
"v_mfma_f32_16x16x32_bf16 v[36:39], v[232:235], v[24:27], v[36:39]\n"
"v_and_b32_dpp v24, v45, v28 row_newbcast:4 row_mask:0xf bank_mask:0xf\n"
"v_and_b32_dpp v25, v45, v29 row_newbcast:4 row_mask:0xf bank_mask:0xf\n"
"v_and_b32_dpp v26, v45, v30 row_newbcast:4 row_mask:0xf bank_mask:0xf\n"
"v_and_b32_dpp v27, v45, v31 row_newbcast:4 row_mask:0xf bank_mask:0xf\n"
"v_mfma_f32_16x16x32_bf16 v[36:39], v[236:239], v[20:23], v[36:39]\n"
"s_add_u32 s20, s16, 630784\n"
"s_addc_u32 s21, s17, 0\n"
"s_mov_b64 exec, s[32:33]\n"
"global_load_dwordx4 v[48:51], v0, s[20:21] offset:0 nt\n"
"s_mov_b64 exec, -1\n"
"v_and_b32_dpp v20, v45, v28 row_newbcast:8 row_mask:0xf bank_mask:0xf\n"
"v_and_b32_dpp v21, v45, v29 row_newbcast:8 row_mask:0xf bank_mask:0xf\n"
"v_and_b32_dpp v22, v45, v30 row_newbcast:8 row_mask:0xf bank_mask:0xf\n"
"v_and_b32_dpp v23, v45, v31 row_newbcast:8 row_mask:0xf bank_mask:0xf\n"
"v_mfma_f32_16x16x32_bf16 v[36:39], v[240:243], v[24:27], v[36:39]\n"
"v_and_b32_dpp v24, v45, v28 row_newbcast:12 row_mask:0xf bank_mask:0xf\n"
"v_and_b32_dpp v25, v45, v29 row_newbcast:12 row_mask:0xf bank_mask:0xf\n"
"v_and_b32_dpp v26, v45, v30 row_newbcast:12 row_mask:0xf bank_mask:0xf\n"
"v_and_b32_dpp v27, v45, v31 row_newbcast:12 row_mask:0xf bank_mask:0xf\n"
"v_mfma_f32_16x16x32_bf16 v[36:39], v[244:247], v[20:23], v[36:39]\n"
"v_and_b32_dpp v20, v45, v28 row_newbcast:1 row_mask:0xf bank_mask:0xf\n"
"v_and_b32_dpp v21, v45, v29 row_newbcast:1 row_mask:0xf bank_mask:0xf\n"
"v_and_b32_dpp v22, v45, v30 row_newbcast:1 row_mask:0xf bank_mask:0xf\n"
"v_and_b32_dpp v23, v45, v31 row_newbcast:1 row_mask:0xf bank_mask:0xf\n"
"v_mfma_f32_16x16x32_bf16 v[36:39], v[248:251], v[24:27], v[36:39]\n"
"s_add_u32 s63, s62, s61\n"
"v_lshrrev_b32 v2, 8, v1\n"
"v_bfe_u32 v3, v1, 4, 4\n"
"v_lshlrev_b32 v2, 5, v2\n"
"v_add_u32 v2, v2, v3\n"
"v_lshl_add_u32 v2, v2, 2, s63\n"
"v_mov_b32 v3, s63\n"
"v_mfma_f32_16x16x32_bf16 v[36:39], v[252:255], v[20:23], v[36:39]\n"
"v_mov_b32 v46, s54\n"
"v_mov_b32 v47, s55\n"
"s_mov_b64 exec, s[48:49]\n"
"ds_write_b64 v3, v[46:47]\n"
"s_nop 3\n"
"ds_write_b32 v2, v36 offset:16\n"
"ds_write_b32 v2, v37 offset:48\n"
"ds_write_b32 v2, v38 offset:80\n"
"ds_write_b32 v2, v39 offset:112\n"
"s_mov_b64 exec, -1\n"
"s_add_u32 s63, s62, s78\n"
"v_mov_b32 v3, s62\n"
"v_bfe_u32 v47, v1, 4, 5\n"
"v_lshl_add_u32 v2, v47, 2, s63\n"
"s_waitcnt lgkmcnt(0)\n"
"s_barrier\n"
"ds_read_b64 v[20:21], v3 offset:0\n"
"ds_read_b64 v[22:23], v3 offset:528\n"
"ds_read_b64 v[24:25], v3 offset:1056\n"
"ds_read_b64 v[26:27], v3 offset:1584\n"
"ds_read_b32 v28, v2 offset:0\n"
"ds_read_b32 v29, v2 offset:528\n"
"ds_read_b32 v30, v2 offset:1056\n"
"ds_read_b32 v31, v2 offset:1584\n"
"s_xor_b32 s62, s62, 2112\n"
"s_add_u32 s63, s59, s72\n"
"s_sub_u32 s70, s73, s72\n"
"v_lshl_add_u32 v47, v47, 2, s63\n"
"v_mov_b32 v46, s70\n"
"v_cndmask_b32_e64 v46, 0, v46, s[66:67]\n"
"v_add_u32 v47, v47, v46\n"
"s_waitcnt lgkmcnt(4)\n"
"v_max3_f32 v2, v20, v22, v24\n"
"v_max_f32 v2, v2, v26\n"
"v_sub_f32 v20, v20, v2\n"
"v_sub_f32 v22, v22, v2\n"
"v_sub_f32 v24, v24, v2\n"
"v_sub_f32 v26, v26, v2\n"
"v_exp_f32 v20, v20\n"
"v_exp_f32 v22, v22\n"
"v_exp_f32 v24, v24\n"
"v_exp_f32 v26, v26\n"
"v_mul_f32 v3, v20, v21\n"
"v_fmac_f32 v3, v22, v23\n"
"v_fmac_f32 v3, v24, v25\n"
"v_fmac_f32 v3, v26, v27\n"
"s_waitcnt lgkmcnt(0)\n"
"v_mul_f32 v21, v20, v28\n"
"v_fmac_f32 v21, v22, v29\n"
"v_fmac_f32 v21, v24, v30\n"
"v_fmac_f32 v21, v26, v31\n"
"v_cndmask_b32_e64 v2, v2, v3, s[34:35]\n"
"v_cndmask_b32_e64 v21, v21, v2, s[66:67]\n"
"v_or_b32 v21, 1, v21\n"
"s_mov_b64 exec, s[50:51]\n"
"global_store_dword v47, v21, s[22:23] sc1\n"
"s_mov_b64 exec, -1\n"
"s_add_u32 s24, s24, 1\n"
"s_mov_b32 s28, s27\n"
"s_add_u32 s27, s27, 16384\n"
"s_sub_u32 s63, s27, 38912\n"
"s_cmp_ge_u32 s27, s79\n"
"s_cselect_b32 s27, s63, s27\n"
"s_sub_u32 s57, s57, 16384\n"
"s_add_u32 s58, s58, 262144\n"
"s_add_u32 s59, s59, 262144\n"
"s_nop 7\n"
"s_nop 3\n"
"s_branch .Lst_loop\n"
".Lst_epi:\n"
"s_branch .Lst_qslow_135\n"
".Lst_qok_136:\n"
"v_perm_b32 v20, v5, v4, s69\n"
"v_perm_b32 v21, v7, v6, s69\n"
"v_perm_b32 v22, v9, v8, s69\n"
"v_perm_b32 v23, v11, v10, s69\n"
"v_cndmask_b32_e64 v4, 0, v20, s[38:39]\n"
"v_cndmask_b32_e64 v5, 0, v21, s[38:39]\n"
"v_cndmask_b32_e64 v6, 0, v22, s[38:39]\n"
"v_cndmask_b32_e64 v7, 0, v23, s[38:39]\n"
"v_cndmask_b32_e64 v8, 0, v20, s[40:41]\n"
"v_cndmask_b32_e64 v9, 0, v21, s[40:41]\n"
"v_cndmask_b32_e64 v10, 0, v22, s[40:41]\n"
"v_cndmask_b32_e64 v11, 0, v23, s[40:41]\n"
"v_cndmask_b32_e64 v12, 0, v20, s[42:43]\n"
"v_cndmask_b32_e64 v13, 0, v21, s[42:43]\n"
"v_cndmask_b32_e64 v14, 0, v22, s[42:43]\n"
"v_cndmask_b32_e64 v15, 0, v23, s[42:43]\n"
"v_cndmask_b32_e64 v16, 0, v20, s[44:45]\n"
"v_cndmask_b32_e64 v17, 0, v21, s[44:45]\n"
"v_cndmask_b32_e64 v18, 0, v22, s[44:45]\n"
"v_cndmask_b32_e64 v19, 0, v23, s[44:45]\n"
"s_mov_b32 s29, s28\n"
"v_add_u32 v47, s29, v1\n"
"ds_read_b128 v[28:31], v47\n"
"s_add_u32 s29, s29, 2048\n"
"s_cmp_eq_u32 s29, s79\n"
"s_cselect_b32 s29, s25, s29\n"
"ds_read_b128 v[36:39], v47 offset:1024\n"
"v_mfma_f32_16x16x32_bf16 v[20:23], a[232:235], v[4:7], 0\n"
"v_mfma_f32_16x16x32_bf16 v[20:23], a[236:239], v[8:11], v[20:23]\n"
"s_waitcnt lgkmcnt(1)\n"
"v_mfma_f32_16x16x32_bf16 v[20:23], v[28:31], v[12:15], v[20:23]\n"
"v_add_u32 v47, s29, v1\n"
"ds_read_b128 v[28:31], v47\n"
"s_add_u32 s29, s29, 2048\n"
"s_cmp_eq_u32 s29, s79\n"
"s_cselect_b32 s29, s25, s29\n"
"s_waitcnt lgkmcnt(1)\n"
"v_mfma_f32_16x16x32_bf16 v[20:23], v[36:39], v[16:19], v[20:23]\n"
"ds_read_b128 v[36:39], v47 offset:1024\n"
"v_mfma_f32_16x16x32_bf16 v[24:27], a[240:243], v[4:7], 0\n"
"v_mfma_f32_16x16x32_bf16 v[24:27], a[244:247], v[8:11], v[24:27]\n"
"s_waitcnt lgkmcnt(1)\n"
"v_mfma_f32_16x16x32_bf16 v[24:27], v[28:31], v[12:15], v[24:27]\n"
"v_add_u32 v47, s29, v1\n"
"ds_read_b128 v[28:31], v47\n"
"s_add_u32 s29, s29, 2048\n"
"s_cmp_eq_u32 s29, s79\n"
"s_cselect_b32 s29, s25, s29\n"
"s_waitcnt lgkmcnt(1)\n"
"v_mfma_f32_16x16x32_bf16 v[24:27], v[36:39], v[16:19], v[24:27]\n"
"ds_read_b128 v[36:39], v47 offset:1024\n"
"v_cndmask_b32_e64 v2, v20, v21, s[34:35]\n"
"v_cndmask_b32_e64 v3, v22, v23, s[34:35]\n"
"v_cndmask_b32_e64 v46, v2, v3, s[36:37]\n"
"v_mfma_f32_16x16x32_bf16 v[20:23], a[248:251], v[4:7], 0\n"
"v_mfma_f32_16x16x32_bf16 v[20:23], a[252:255], v[8:11], v[20:23]\n"
"s_waitcnt lgkmcnt(1)\n"
"v_mfma_f32_16x16x32_bf16 v[20:23], v[28:31], v[12:15], v[20:23]\n"
"v_add_u32 v47, s29, v1\n"
"ds_read_b128 v[28:31], v47\n"
"s_add_u32 s29, s29, 2048\n"
"s_cmp_eq_u32 s29, s79\n"
"s_cselect_b32 s29, s25, s29\n"
"s_waitcnt lgkmcnt(1)\n"
"v_mfma_f32_16x16x32_bf16 v[20:23], v[36:39], v[16:19], v[20:23]\n"
"ds_read_b128 v[36:39], v47 offset:1024\n"
"v_cndmask_b32_e64 v2, v24, v25, s[34:35]\n"
"v_cndmask_b32_e64 v3, v26, v27, s[34:35]\n"
"v_cndmask_b32_e64 v2, v2, v3, s[36:37]\n"
"v_cndmask_b32_e64 v3, v2, v46, s[34:35]\n"
"v_cndmask_b32_e64 v46, v46, v2, s[34:35]\n"
"s_nop 0\n"
"v_add_f32_dpp v43, v3, v46 quad_perm:[1,0,3,2] row_mask:0xf bank_mask:0xf\n"
"v_mfma_f32_16x16x32_bf16 v[24:27], v[48:51], v[4:7], 0\n"
"v_mfma_f32_16x16x32_bf16 v[24:27], v[52:55], v[8:11], v[24:27]\n"
"s_waitcnt lgkmcnt(1)\n"
"v_mfma_f32_16x16x32_bf16 v[24:27], v[28:31], v[12:15], v[24:27]\n"
"v_add_u32 v47, s29, v1\n"
"ds_read_b128 v[28:31], v47\n"
"s_add_u32 s29, s29, 2048\n"
"s_cmp_eq_u32 s29, s79\n"
"s_cselect_b32 s29, s25, s29\n"
"s_waitcnt lgkmcnt(1)\n"
"v_mfma_f32_16x16x32_bf16 v[24:27], v[36:39], v[16:19], v[24:27]\n"
"ds_read_b128 v[36:39], v47 offset:1024\n"
"v_cndmask_b32_e64 v2, v20, v21, s[34:35]\n"
"v_cndmask_b32_e64 v3, v22, v23, s[34:35]\n"
"v_cndmask_b32_e64 v46, v2, v3, s[36:37]\n"
"v_mfma_f32_16x16x32_bf16 v[20:23], v[56:59], v[4:7], 0\n"
"v_mfma_f32_16x16x32_bf16 v[20:23], v[60:63], v[8:11], v[20:23]\n"
"s_waitcnt lgkmcnt(1)\n"
"v_mfma_f32_16x16x32_bf16 v[20:23], v[28:31], v[12:15], v[20:23]\n"
"v_add_u32 v47, s29, v1\n"
"ds_read_b128 v[28:31], v47\n"
"s_add_u32 s29, s29, 2048\n"
"s_cmp_eq_u32 s29, s79\n"
"s_cselect_b32 s29, s25, s29\n"
"s_waitcnt lgkmcnt(1)\n"
"v_mfma_f32_16x16x32_bf16 v[20:23], v[36:39], v[16:19], v[20:23]\n"
"ds_read_b128 v[36:39], v47 offset:1024\n"
"v_cndmask_b32_e64 v2, v24, v25, s[34:35]\n"
"v_cndmask_b32_e64 v3, v26, v27, s[34:35]\n"
"v_cndmask_b32_e64 v2, v2, v3, s[36:37]\n"
"v_cndmask_b32_e64 v3, v2, v46, s[34:35]\n"
"v_cndmask_b32_e64 v46, v46, v2, s[34:35]\n"
"s_nop 0\n"
"v_add_f32_dpp v46, v3, v46 quad_perm:[1,0,3,2] row_mask:0xf bank_mask:0xf\n"
"v_cndmask_b32_e64 v2, v46, v43, s[36:37]\n"
"v_cndmask_b32_e64 v3, v43, v46, s[36:37]\n"
"s_nop 0\n"
"v_add_f32_dpp v43, v2, v3 quad_perm:[2,3,0,1] row_mask:0xf bank_mask:0xf\n"
"v_mfma_f32_16x16x32_bf16 v[24:27], v[64:67], v[4:7], 0\n"
"v_mfma_f32_16x16x32_bf16 v[24:27], v[68:71], v[8:11], v[24:27]\n"
"s_waitcnt lgkmcnt(1)\n"
"v_mfma_f32_16x16x32_bf16 v[24:27], v[28:31], v[12:15], v[24:27]\n"
"v_add_u32 v47, s29, v1\n"
"ds_read_b128 v[28:31], v47\n"
"s_add_u32 s29, s29, 2048\n"
"s_cmp_eq_u32 s29, s79\n"
"s_cselect_b32 s29, s25, s29\n"
"s_waitcnt lgkmcnt(1)\n"
"v_mfma_f32_16x16x32_bf16 v[24:27], v[36:39], v[16:19], v[24:27]\n"
"ds_read_b128 v[36:39], v47 offset:1024\n"
"v_cndmask_b32_e64 v2, v20, v21, s[34:35]\n"
"v_cndmask_b32_e64 v3, v22, v23, s[34:35]\n"
"v_cndmask_b32_e64 v46, v2, v3, s[36:37]\n"
"v_mfma_f32_16x16x32_bf16 v[20:23], v[72:75], v[4:7], 0\n"
"v_mfma_f32_16x16x32_bf16 v[20:23], v[76:79], v[8:11], v[20:23]\n"
"s_waitcnt lgkmcnt(1)\n"
"v_mfma_f32_16x16x32_bf16 v[20:23], v[28:31], v[12:15], v[20:23]\n"
"v_add_u32 v47, s29, v1\n"
"ds_read_b128 v[28:31], v47\n"
"s_add_u32 s29, s29, 2048\n"
"s_cmp_eq_u32 s29, s79\n"
"s_cselect_b32 s29, s25, s29\n"
"s_waitcnt lgkmcnt(1)\n"
"v_mfma_f32_16x16x32_bf16 v[20:23], v[36:39], v[16:19], v[20:23]\n"
"ds_read_b128 v[36:39], v47 offset:1024\n"
"v_cndmask_b32_e64 v2, v24, v25, s[34:35]\n"
"v_cndmask_b32_e64 v3, v26, v27, s[34:35]\n"
"v_cndmask_b32_e64 v2, v2, v3, s[36:37]\n"
"v_cndmask_b32_e64 v3, v2, v46, s[34:35]\n"
"v_cndmask_b32_e64 v46, v46, v2, s[34:35]\n"
"s_nop 0\n"
"v_add_f32_dpp v44, v3, v46 quad_perm:[1,0,3,2] row_mask:0xf bank_mask:0xf\n"
"v_mfma_f32_16x16x32_bf16 v[24:27], v[80:83], v[4:7], 0\n"
"v_mfma_f32_16x16x32_bf16 v[24:27], v[84:87], v[8:11], v[24:27]\n"
"s_waitcnt lgkmcnt(1)\n"
"v_mfma_f32_16x16x32_bf16 v[24:27], v[28:31], v[12:15], v[24:27]\n"
"s_waitcnt lgkmcnt(0)\n"
"v_mfma_f32_16x16x32_bf16 v[24:27], v[36:39], v[16:19], v[24:27]\n"
"v_cndmask_b32_e64 v2, v20, v21, s[34:35]\n"
"v_cndmask_b32_e64 v3, v22, v23, s[34:35]\n"
"v_cndmask_b32_e64 v46, v2, v3, s[36:37]\n"
"v_mfma_f32_16x16x32_bf16 v[20:23], v[88:91], v[4:7], 0\n"
"v_mfma_f32_16x16x32_bf16 v[20:23], v[92:95], v[8:11], v[20:23]\n"
"v_mfma_f32_16x16x32_bf16 v[20:23], v[96:99], v[12:15], v[20:23]\n"
"v_mfma_f32_16x16x32_bf16 v[20:23], v[100:103], v[16:19], v[20:23]\n"
"s_nop 0\n"
"v_cndmask_b32_e64 v2, v24, v25, s[34:35]\n"
"v_cndmask_b32_e64 v3, v26, v27, s[34:35]\n"
"v_cndmask_b32_e64 v2, v2, v3, s[36:37]\n"
"v_cndmask_b32_e64 v3, v2, v46, s[34:35]\n"
"v_cndmask_b32_e64 v46, v46, v2, s[34:35]\n"
"s_nop 0\n"
"v_add_f32_dpp v46, v3, v46 quad_perm:[1,0,3,2] row_mask:0xf bank_mask:0xf\n"
"v_cndmask_b32_e64 v2, v46, v44, s[36:37]\n"
"v_cndmask_b32_e64 v3, v44, v46, s[36:37]\n"
"s_nop 0\n"
"v_add_f32_dpp v44, v2, v3 quad_perm:[2,3,0,1] row_mask:0xf bank_mask:0xf\n"
"v_mfma_f32_16x16x32_bf16 v[24:27], v[104:107], v[4:7], 0\n"
"v_cndmask_b32_e64 v2, v20, v21, s[34:35]\n"
"v_cndmask_b32_e64 v3, v22, v23, s[34:35]\n"
"v_cndmask_b32_e64 v46, v2, v3, s[36:37]\n"
"s_nop 4\n"
"v_cndmask_b32_e64 v2, v24, v25, s[34:35]\n"
"v_cndmask_b32_e64 v3, v26, v27, s[34:35]\n"
"v_cndmask_b32_e64 v2, v2, v3, s[36:37]\n"
"v_cndmask_b32_e64 v3, v2, v46, s[34:35]\n"
"v_cndmask_b32_e64 v46, v46, v2, s[34:35]\n"
"s_nop 0\n"
"v_add_f32_dpp v45, v3, v46 quad_perm:[1,0,3,2] row_mask:0xf bank_mask:0xf\n"
"v_cndmask_b32_e64 v2, v46, v45, s[36:37]\n"
"v_cndmask_b32_e64 v3, v45, v46, s[36:37]\n"
"s_nop 0\n"
"v_add_f32_dpp v45, v2, v3 quad_perm:[2,3,0,1] row_mask:0xf bank_mask:0xf\n"
"v_mov_b32 v2, 0xff800000\n"
"v_cndmask_b32_e64 v45, v2, v45, s[46:47]\n"
"v_max3_f32 v2, v43, v44, v45\n"
"s_nop 1\n"
"v_max_f32_dpp v2, v2, v2 quad_perm:[1,0,3,2] row_mask:0xf bank_mask:0xf\n"
"s_nop 1\n"
"v_max_f32_dpp v2, v2, v2 quad_perm:[2,3,0,1] row_mask:0xf bank_mask:0xf\n"
"s_nop 1\n"
"v_max_f32_dpp v2, v2, v2 row_half_mirror row_mask:0xf bank_mask:0xf\n"
"s_nop 1\n"
"v_max_f32_dpp v2, v2, v2 row_mirror row_mask:0xf bank_mask:0xf\n"
"v_mov_b32 v3, v2\n"
"s_nop 1\n"
"v_permlane16_swap_b32 v3, v2\n"
"v_max_f32 v2, v2, v3\n"
"v_mov_b32 v3, v2\n"
"s_nop 1\n"
"v_permlane32_swap_b32 v3, v2\n"
"v_max_f32 v2, v2, v3\n"
"v_mul_f32 v3, s64, v2\n"
"v_mul_f32 v2, s65, v2\n"
"v_readfirstlane_b32 s54, v3\n"
"v_fma_f32 v43, v43, s64, v2\n"
"v_fma_f32 v44, v44, s64, v2\n"
"v_fma_f32 v45, v45, s64, v2\n"
"v_exp_f32 v43, v43\n"
"v_exp_f32 v44, v44\n"
"v_exp_f32 v45, v45\n"
"v_mov_b32 v3, 0xffff\n"
"v_not_b32 v47, v3\n"
"v_cndmask_b32_e64 v3, v3, v47, s[34:35]\n"
"v_cndmask_b32_e64 v28, 0, v3, s[80:81]\n"
"v_cndmask_b32_e64 v29, 0, v3, s[82:83]\n"
"v_cndmask_b32_e64 v30, 0, v3, s[84:85]\n"
"v_cndmask_b32_e64 v31, 0, v3, s[86:87]\n"
"v_cvt_pk_bf16_f32 v43, v43, v43\n"
"v_cvt_pk_bf16_f32 v44, v44, v44\n"
"v_cvt_pk_bf16_f32 v45, v45, v45\n"
"v_and_b32 v2, 0xffff0000, v43\n"
"v_and_b32 v3, 0xffff0000, v44\n"
"v_add_f32 v2, v2, v3\n"
"v_and_b32 v3, 0xffff0000, v45\n"
"v_add_f32 v2, v2, v3\n"
"s_nop 1\n"
"v_add_f32_dpp v2, v2, v2 quad_perm:[1,0,3,2] row_mask:0xf bank_mask:0xf\n"
"s_nop 1\n"
"v_add_f32_dpp v2, v2, v2 quad_perm:[2,3,0,1] row_mask:0xf bank_mask:0xf\n"
"s_nop 1\n"
"v_add_f32_dpp v2, v2, v2 row_half_mirror row_mask:0xf bank_mask:0xf\n"
"s_nop 1\n"
"v_add_f32_dpp v2, v2, v2 row_mirror row_mask:0xf bank_mask:0xf\n"
"v_mov_b32 v3, v2\n"
"s_nop 1\n"
"v_permlane16_swap_b32 v3, v2\n"
"v_add_f32 v2, v2, v3\n"
"v_mov_b32 v3, v2\n"
"s_nop 1\n"
"v_permlane32_swap_b32 v3, v2\n"
"v_add_f32 v2, v2, v3\n"
"s_nop 0\n"
"v_readfirstlane_b32 s55, v2\n"
"v_and_b32_dpp v20, v43, v28 row_newbcast:8 row_mask:0xf bank_mask:0xf\n"
"v_and_b32_dpp v21, v43, v29 row_newbcast:8 row_mask:0xf bank_mask:0xf\n"
"v_and_b32_dpp v22, v43, v30 row_newbcast:8 row_mask:0xf bank_mask:0xf\n"
"v_and_b32_dpp v23, v43, v31 row_newbcast:8 row_mask:0xf bank_mask:0xf\n"
"v_and_b32_dpp v24, v43, v28 row_newbcast:12 row_mask:0xf bank_mask:0xf\n"
"v_and_b32_dpp v25, v43, v29 row_newbcast:12 row_mask:0xf bank_mask:0xf\n"
"v_and_b32_dpp v26, v43, v30 row_newbcast:12 row_mask:0xf bank_mask:0xf\n"
"v_and_b32_dpp v27, v43, v31 row_newbcast:12 row_mask:0xf bank_mask:0xf\n"
"v_mfma_f32_16x16x32_bf16 v[36:39], v[108:111], v[20:23], 0\n"
"v_and_b32_dpp v20, v43, v28 row_newbcast:9 row_mask:0xf bank_mask:0xf\n"
"v_and_b32_dpp v21, v43, v29 row_newbcast:9 row_mask:0xf bank_mask:0xf\n"
"v_and_b32_dpp v22, v43, v30 row_newbcast:9 row_mask:0xf bank_mask:0xf\n"
"v_and_b32_dpp v23, v43, v31 row_newbcast:9 row_mask:0xf bank_mask:0xf\n"
"v_mfma_f32_16x16x32_bf16 v[36:39], v[112:115], v[24:27], v[36:39]\n"
"v_and_b32_dpp v24, v43, v28 row_newbcast:13 row_mask:0xf bank_mask:0xf\n"
"v_and_b32_dpp v25, v43, v29 row_newbcast:13 row_mask:0xf bank_mask:0xf\n"
"v_and_b32_dpp v26, v43, v30 row_newbcast:13 row_mask:0xf bank_mask:0xf\n"
"v_and_b32_dpp v27, v43, v31 row_newbcast:13 row_mask:0xf bank_mask:0xf\n"
"v_mfma_f32_16x16x32_bf16 v[36:39], v[116:119], v[20:23], v[36:39]\n"
"v_and_b32_dpp v20, v43, v28 row_newbcast:10 row_mask:0xf bank_mask:0xf\n"
"v_and_b32_dpp v21, v43, v29 row_newbcast:10 row_mask:0xf bank_mask:0xf\n"
"v_and_b32_dpp v22, v43, v30 row_newbcast:10 row_mask:0xf bank_mask:0xf\n"
"v_and_b32_dpp v23, v43, v31 row_newbcast:10 row_mask:0xf bank_mask:0xf\n"
"v_mfma_f32_16x16x32_bf16 v[36:39], v[120:123], v[24:27], v[36:39]\n"
"v_and_b32_dpp v24, v43, v28 row_newbcast:14 row_mask:0xf bank_mask:0xf\n"
"v_and_b32_dpp v25, v43, v29 row_newbcast:14 row_mask:0xf bank_mask:0xf\n"
"v_and_b32_dpp v26, v43, v30 row_newbcast:14 row_mask:0xf bank_mask:0xf\n"
"v_and_b32_dpp v27, v43, v31 row_newbcast:14 row_mask:0xf bank_mask:0xf\n"
"v_mfma_f32_16x16x32_bf16 v[36:39], v[124:127], v[20:23], v[36:39]\n"
"v_and_b32_dpp v20, v43, v28 row_newbcast:11 row_mask:0xf bank_mask:0xf\n"
"v_and_b32_dpp v21, v43, v29 row_newbcast:11 row_mask:0xf bank_mask:0xf\n"
"v_and_b32_dpp v22, v43, v30 row_newbcast:11 row_mask:0xf bank_mask:0xf\n"
"v_and_b32_dpp v23, v43, v31 row_newbcast:11 row_mask:0xf bank_mask:0xf\n"
"v_mfma_f32_16x16x32_bf16 v[36:39], v[128:131], v[24:27], v[36:39]\n"
"v_and_b32_dpp v24, v43, v28 row_newbcast:15 row_mask:0xf bank_mask:0xf\n"
"v_and_b32_dpp v25, v43, v29 row_newbcast:15 row_mask:0xf bank_mask:0xf\n"
"v_and_b32_dpp v26, v43, v30 row_newbcast:15 row_mask:0xf bank_mask:0xf\n"
"v_and_b32_dpp v27, v43, v31 row_newbcast:15 row_mask:0xf bank_mask:0xf\n"
"v_mfma_f32_16x16x32_bf16 v[36:39], v[132:135], v[20:23], v[36:39]\n"
"v_and_b32_dpp v20, v44, v28 row_newbcast:8 row_mask:0xf bank_mask:0xf\n"
"v_and_b32_dpp v21, v44, v29 row_newbcast:8 row_mask:0xf bank_mask:0xf\n"
"v_and_b32_dpp v22, v44, v30 row_newbcast:8 row_mask:0xf bank_mask:0xf\n"
"v_and_b32_dpp v23, v44, v31 row_newbcast:8 row_mask:0xf bank_mask:0xf\n"
"v_mfma_f32_16x16x32_bf16 v[36:39], v[136:139], v[24:27], v[36:39]\n"
"v_and_b32_dpp v24, v44, v28 row_newbcast:12 row_mask:0xf bank_mask:0xf\n"
"v_and_b32_dpp v25, v44, v29 row_newbcast:12 row_mask:0xf bank_mask:0xf\n"
"v_and_b32_dpp v26, v44, v30 row_newbcast:12 row_mask:0xf bank_mask:0xf\n"
"v_and_b32_dpp v27, v44, v31 row_newbcast:12 row_mask:0xf bank_mask:0xf\n"
"v_mfma_f32_16x16x32_bf16 v[36:39], v[140:143], v[20:23], v[36:39]\n"
"v_and_b32_dpp v20, v44, v28 row_newbcast:9 row_mask:0xf bank_mask:0xf\n"
"v_and_b32_dpp v21, v44, v29 row_newbcast:9 row_mask:0xf bank_mask:0xf\n"
"v_and_b32_dpp v22, v44, v30 row_newbcast:9 row_mask:0xf bank_mask:0xf\n"
"v_and_b32_dpp v23, v44, v31 row_newbcast:9 row_mask:0xf bank_mask:0xf\n"
"v_mfma_f32_16x16x32_bf16 v[36:39], v[144:147], v[24:27], v[36:39]\n"
"v_and_b32_dpp v24, v44, v28 row_newbcast:13 row_mask:0xf bank_mask:0xf\n"
"v_and_b32_dpp v25, v44, v29 row_newbcast:13 row_mask:0xf bank_mask:0xf\n"
"v_and_b32_dpp v26, v44, v30 row_newbcast:13 row_mask:0xf bank_mask:0xf\n"
"v_and_b32_dpp v27, v44, v31 row_newbcast:13 row_mask:0xf bank_mask:0xf\n"
"v_mfma_f32_16x16x32_bf16 v[36:39], v[148:151], v[20:23], v[36:39]\n"
"v_and_b32_dpp v20, v44, v28 row_newbcast:10 row_mask:0xf bank_mask:0xf\n"
"v_and_b32_dpp v21, v44, v29 row_newbcast:10 row_mask:0xf bank_mask:0xf\n"
"v_and_b32_dpp v22, v44, v30 row_newbcast:10 row_mask:0xf bank_mask:0xf\n"
"v_and_b32_dpp v23, v44, v31 row_newbcast:10 row_mask:0xf bank_mask:0xf\n"
"v_mfma_f32_16x16x32_bf16 v[36:39], v[152:155], v[24:27], v[36:39]\n"
"v_and_b32_dpp v24, v44, v28 row_newbcast:14 row_mask:0xf bank_mask:0xf\n"
"v_and_b32_dpp v25, v44, v29 row_newbcast:14 row_mask:0xf bank_mask:0xf\n"
"v_and_b32_dpp v26, v44, v30 row_newbcast:14 row_mask:0xf bank_mask:0xf\n"
"v_and_b32_dpp v27, v44, v31 row_newbcast:14 row_mask:0xf bank_mask:0xf\n"
"v_mfma_f32_16x16x32_bf16 v[36:39], v[156:159], v[20:23], v[36:39]\n"
"v_and_b32_dpp v20, v44, v28 row_newbcast:11 row_mask:0xf bank_mask:0xf\n"
"v_and_b32_dpp v21, v44, v29 row_newbcast:11 row_mask:0xf bank_mask:0xf\n"
"v_and_b32_dpp v22, v44, v30 row_newbcast:11 row_mask:0xf bank_mask:0xf\n"
"v_and_b32_dpp v23, v44, v31 row_newbcast:11 row_mask:0xf bank_mask:0xf\n"
"v_mfma_f32_16x16x32_bf16 v[36:39], v[160:163], v[24:27], v[36:39]\n"
"v_and_b32_dpp v24, v44, v28 row_newbcast:15 row_mask:0xf bank_mask:0xf\n"
"v_and_b32_dpp v25, v44, v29 row_newbcast:15 row_mask:0xf bank_mask:0xf\n"
"v_and_b32_dpp v26, v44, v30 row_newbcast:15 row_mask:0xf bank_mask:0xf\n"
"v_and_b32_dpp v27, v44, v31 row_newbcast:15 row_mask:0xf bank_mask:0xf\n"
"v_mfma_f32_16x16x32_bf16 v[36:39], v[164:167], v[20:23], v[36:39]\n"
"v_and_b32_dpp v20, v43, v28 row_newbcast:0 row_mask:0xf bank_mask:0xf\n"
"v_and_b32_dpp v21, v43, v29 row_newbcast:0 row_mask:0xf bank_mask:0xf\n"
"v_and_b32_dpp v22, v43, v30 row_newbcast:0 row_mask:0xf bank_mask:0xf\n"
"v_and_b32_dpp v23, v43, v31 row_newbcast:0 row_mask:0xf bank_mask:0xf\n"
"v_mfma_f32_16x16x32_bf16 v[36:39], v[168:171], v[24:27], v[36:39]\n"
"v_and_b32_dpp v24, v43, v28 row_newbcast:4 row_mask:0xf bank_mask:0xf\n"
"v_and_b32_dpp v25, v43, v29 row_newbcast:4 row_mask:0xf bank_mask:0xf\n"
"v_and_b32_dpp v26, v43, v30 row_newbcast:4 row_mask:0xf bank_mask:0xf\n"
"v_and_b32_dpp v27, v43, v31 row_newbcast:4 row_mask:0xf bank_mask:0xf\n"
"v_mfma_f32_16x16x32_bf16 v[36:39], v[172:175], v[20:23], v[36:39]\n"
"v_and_b32_dpp v20, v43, v28 row_newbcast:1 row_mask:0xf bank_mask:0xf\n"
"v_and_b32_dpp v21, v43, v29 row_newbcast:1 row_mask:0xf bank_mask:0xf\n"
"v_and_b32_dpp v22, v43, v30 row_newbcast:1 row_mask:0xf bank_mask:0xf\n"
"v_and_b32_dpp v23, v43, v31 row_newbcast:1 row_mask:0xf bank_mask:0xf\n"
"v_mfma_f32_16x16x32_bf16 v[36:39], v[176:179], v[24:27], v[36:39]\n"
"v_and_b32_dpp v24, v43, v28 row_newbcast:5 row_mask:0xf bank_mask:0xf\n"
"v_and_b32_dpp v25, v43, v29 row_newbcast:5 row_mask:0xf bank_mask:0xf\n"
"v_and_b32_dpp v26, v43, v30 row_newbcast:5 row_mask:0xf bank_mask:0xf\n"
"v_and_b32_dpp v27, v43, v31 row_newbcast:5 row_mask:0xf bank_mask:0xf\n"
"v_mfma_f32_16x16x32_bf16 v[36:39], v[180:183], v[20:23], v[36:39]\n"
"v_and_b32_dpp v20, v43, v28 row_newbcast:2 row_mask:0xf bank_mask:0xf\n"
"v_and_b32_dpp v21, v43, v29 row_newbcast:2 row_mask:0xf bank_mask:0xf\n"
"v_and_b32_dpp v22, v43, v30 row_newbcast:2 row_mask:0xf bank_mask:0xf\n"
"v_and_b32_dpp v23, v43, v31 row_newbcast:2 row_mask:0xf bank_mask:0xf\n"
"v_mfma_f32_16x16x32_bf16 v[36:39], v[184:187], v[24:27], v[36:39]\n"
"v_and_b32_dpp v24, v43, v28 row_newbcast:6 row_mask:0xf bank_mask:0xf\n"
"v_and_b32_dpp v25, v43, v29 row_newbcast:6 row_mask:0xf bank_mask:0xf\n"
"v_and_b32_dpp v26, v43, v30 row_newbcast:6 row_mask:0xf bank_mask:0xf\n"
"v_and_b32_dpp v27, v43, v31 row_newbcast:6 row_mask:0xf bank_mask:0xf\n"
"v_mfma_f32_16x16x32_bf16 v[36:39], v[188:191], v[20:23], v[36:39]\n"
"v_and_b32_dpp v20, v43, v28 row_newbcast:3 row_mask:0xf bank_mask:0xf\n"
"v_and_b32_dpp v21, v43, v29 row_newbcast:3 row_mask:0xf bank_mask:0xf\n"
"v_and_b32_dpp v22, v43, v30 row_newbcast:3 row_mask:0xf bank_mask:0xf\n"
"v_and_b32_dpp v23, v43, v31 row_newbcast:3 row_mask:0xf bank_mask:0xf\n"
"v_mfma_f32_16x16x32_bf16 v[36:39], v[192:195], v[24:27], v[36:39]\n"
"v_and_b32_dpp v24, v43, v28 row_newbcast:7 row_mask:0xf bank_mask:0xf\n"
"v_and_b32_dpp v25, v43, v29 row_newbcast:7 row_mask:0xf bank_mask:0xf\n"
"v_and_b32_dpp v26, v43, v30 row_newbcast:7 row_mask:0xf bank_mask:0xf\n"
"v_and_b32_dpp v27, v43, v31 row_newbcast:7 row_mask:0xf bank_mask:0xf\n"
"v_mfma_f32_16x16x32_bf16 v[36:39], v[196:199], v[20:23], v[36:39]\n"
"v_and_b32_dpp v20, v44, v28 row_newbcast:0 row_mask:0xf bank_mask:0xf\n"
"v_and_b32_dpp v21, v44, v29 row_newbcast:0 row_mask:0xf bank_mask:0xf\n"
"v_and_b32_dpp v22, v44, v30 row_newbcast:0 row_mask:0xf bank_mask:0xf\n"
"v_and_b32_dpp v23, v44, v31 row_newbcast:0 row_mask:0xf bank_mask:0xf\n"
"v_mfma_f32_16x16x32_bf16 v[36:39], v[200:203], v[24:27], v[36:39]\n"
"v_and_b32_dpp v24, v44, v28 row_newbcast:4 row_mask:0xf bank_mask:0xf\n"
"v_and_b32_dpp v25, v44, v29 row_newbcast:4 row_mask:0xf bank_mask:0xf\n"
"v_and_b32_dpp v26, v44, v30 row_newbcast:4 row_mask:0xf bank_mask:0xf\n"
"v_and_b32_dpp v27, v44, v31 row_newbcast:4 row_mask:0xf bank_mask:0xf\n"
"v_mfma_f32_16x16x32_bf16 v[36:39], v[204:207], v[20:23], v[36:39]\n"
"v_and_b32_dpp v20, v44, v28 row_newbcast:1 row_mask:0xf bank_mask:0xf\n"
"v_and_b32_dpp v21, v44, v29 row_newbcast:1 row_mask:0xf bank_mask:0xf\n"
"v_and_b32_dpp v22, v44, v30 row_newbcast:1 row_mask:0xf bank_mask:0xf\n"
"v_and_b32_dpp v23, v44, v31 row_newbcast:1 row_mask:0xf bank_mask:0xf\n"
"v_mfma_f32_16x16x32_bf16 v[36:39], v[208:211], v[24:27], v[36:39]\n"
"v_and_b32_dpp v24, v44, v28 row_newbcast:5 row_mask:0xf bank_mask:0xf\n"
"v_and_b32_dpp v25, v44, v29 row_newbcast:5 row_mask:0xf bank_mask:0xf\n"
"v_and_b32_dpp v26, v44, v30 row_newbcast:5 row_mask:0xf bank_mask:0xf\n"
"v_and_b32_dpp v27, v44, v31 row_newbcast:5 row_mask:0xf bank_mask:0xf\n"
"v_mfma_f32_16x16x32_bf16 v[36:39], v[212:215], v[20:23], v[36:39]\n"
"v_and_b32_dpp v20, v44, v28 row_newbcast:2 row_mask:0xf bank_mask:0xf\n"
"v_and_b32_dpp v21, v44, v29 row_newbcast:2 row_mask:0xf bank_mask:0xf\n"
"v_and_b32_dpp v22, v44, v30 row_newbcast:2 row_mask:0xf bank_mask:0xf\n"
"v_and_b32_dpp v23, v44, v31 row_newbcast:2 row_mask:0xf bank_mask:0xf\n"
"v_mfma_f32_16x16x32_bf16 v[36:39], v[216:219], v[24:27], v[36:39]\n"
"v_and_b32_dpp v24, v44, v28 row_newbcast:6 row_mask:0xf bank_mask:0xf\n"
"v_and_b32_dpp v25, v44, v29 row_newbcast:6 row_mask:0xf bank_mask:0xf\n"
"v_and_b32_dpp v26, v44, v30 row_newbcast:6 row_mask:0xf bank_mask:0xf\n"
"v_and_b32_dpp v27, v44, v31 row_newbcast:6 row_mask:0xf bank_mask:0xf\n"
"v_mfma_f32_16x16x32_bf16 v[36:39], v[220:223], v[20:23], v[36:39]\n"
"v_and_b32_dpp v20, v44, v28 row_newbcast:3 row_mask:0xf bank_mask:0xf\n"
"v_and_b32_dpp v21, v44, v29 row_newbcast:3 row_mask:0xf bank_mask:0xf\n"
"v_and_b32_dpp v22, v44, v30 row_newbcast:3 row_mask:0xf bank_mask:0xf\n"
"v_and_b32_dpp v23, v44, v31 row_newbcast:3 row_mask:0xf bank_mask:0xf\n"
"v_mfma_f32_16x16x32_bf16 v[36:39], v[224:227], v[24:27], v[36:39]\n"
"v_and_b32_dpp v24, v44, v28 row_newbcast:7 row_mask:0xf bank_mask:0xf\n"
"v_and_b32_dpp v25, v44, v29 row_newbcast:7 row_mask:0xf bank_mask:0xf\n"
"v_and_b32_dpp v26, v44, v30 row_newbcast:7 row_mask:0xf bank_mask:0xf\n"
"v_and_b32_dpp v27, v44, v31 row_newbcast:7 row_mask:0xf bank_mask:0xf\n"
"v_mfma_f32_16x16x32_bf16 v[36:39], v[228:231], v[20:23], v[36:39]\n"
"v_and_b32_dpp v20, v45, v28 row_newbcast:0 row_mask:0xf bank_mask:0xf\n"
"v_and_b32_dpp v21, v45, v29 row_newbcast:0 row_mask:0xf bank_mask:0xf\n"
"v_and_b32_dpp v22, v45, v30 row_newbcast:0 row_mask:0xf bank_mask:0xf\n"
"v_and_b32_dpp v23, v45, v31 row_newbcast:0 row_mask:0xf bank_mask:0xf\n"
"v_mfma_f32_16x16x32_bf16 v[36:39], v[232:235], v[24:27], v[36:39]\n"
"v_and_b32_dpp v24, v45, v28 row_newbcast:4 row_mask:0xf bank_mask:0xf\n"
"v_and_b32_dpp v25, v45, v29 row_newbcast:4 row_mask:0xf bank_mask:0xf\n"
"v_and_b32_dpp v26, v45, v30 row_newbcast:4 row_mask:0xf bank_mask:0xf\n"
"v_and_b32_dpp v27, v45, v31 row_newbcast:4 row_mask:0xf bank_mask:0xf\n"
"v_mfma_f32_16x16x32_bf16 v[36:39], v[236:239], v[20:23], v[36:39]\n"
"v_and_b32_dpp v20, v45, v28 row_newbcast:8 row_mask:0xf bank_mask:0xf\n"
"v_and_b32_dpp v21, v45, v29 row_newbcast:8 row_mask:0xf bank_mask:0xf\n"
"v_and_b32_dpp v22, v45, v30 row_newbcast:8 row_mask:0xf bank_mask:0xf\n"
"v_and_b32_dpp v23, v45, v31 row_newbcast:8 row_mask:0xf bank_mask:0xf\n"
"v_mfma_f32_16x16x32_bf16 v[36:39], v[240:243], v[24:27], v[36:39]\n"
"v_and_b32_dpp v24, v45, v28 row_newbcast:12 row_mask:0xf bank_mask:0xf\n"
"v_and_b32_dpp v25, v45, v29 row_newbcast:12 row_mask:0xf bank_mask:0xf\n"
"v_and_b32_dpp v26, v45, v30 row_newbcast:12 row_mask:0xf bank_mask:0xf\n"
"v_and_b32_dpp v27, v45, v31 row_newbcast:12 row_mask:0xf bank_mask:0xf\n"
"v_mfma_f32_16x16x32_bf16 v[36:39], v[244:247], v[20:23], v[36:39]\n"
"v_and_b32_dpp v20, v45, v28 row_newbcast:1 row_mask:0xf bank_mask:0xf\n"
"v_and_b32_dpp v21, v45, v29 row_newbcast:1 row_mask:0xf bank_mask:0xf\n"
"v_and_b32_dpp v22, v45, v30 row_newbcast:1 row_mask:0xf bank_mask:0xf\n"
"v_and_b32_dpp v23, v45, v31 row_newbcast:1 row_mask:0xf bank_mask:0xf\n"
"v_mfma_f32_16x16x32_bf16 v[36:39], v[248:251], v[24:27], v[36:39]\n"
"s_add_u32 s63, s62, s61\n"
"v_lshrrev_b32 v2, 8, v1\n"
"v_bfe_u32 v3, v1, 4, 4\n"
"v_lshlrev_b32 v2, 5, v2\n"
"v_add_u32 v2, v2, v3\n"
"v_lshl_add_u32 v2, v2, 2, s63\n"
"v_mov_b32 v3, s63\n"
"v_mfma_f32_16x16x32_bf16 v[36:39], v[252:255], v[20:23], v[36:39]\n"
"v_mov_b32 v46, s54\n"
"v_mov_b32 v47, s55\n"
"s_mov_b64 exec, s[48:49]\n"
"ds_write_b64 v3, v[46:47]\n"
"s_nop 3\n"
"ds_write_b32 v2, v36 offset:16\n"
"ds_write_b32 v2, v37 offset:48\n"
"ds_write_b32 v2, v38 offset:80\n"
"ds_write_b32 v2, v39 offset:112\n"
"s_mov_b64 exec, -1\n"
"s_add_u32 s63, s62, s78\n"
"v_mov_b32 v3, s62\n"
"v_bfe_u32 v47, v1, 4, 5\n"
"v_lshl_add_u32 v2, v47, 2, s63\n"
"s_waitcnt lgkmcnt(0)\n"
"s_barrier\n"
"ds_read_b64 v[20:21], v3 offset:0\n"
"ds_read_b64 v[22:23], v3 offset:528\n"
"ds_read_b64 v[24:25], v3 offset:1056\n"
"ds_read_b64 v[26:27], v3 offset:1584\n"
"ds_read_b32 v28, v2 offset:0\n"
"ds_read_b32 v29, v2 offset:528\n"
"ds_read_b32 v30, v2 offset:1056\n"
"ds_read_b32 v31, v2 offset:1584\n"
"s_xor_b32 s62, s62, 2112\n"
"s_add_u32 s63, s59, s72\n"
"s_sub_u32 s70, s73, s72\n"
"v_lshl_add_u32 v47, v47, 2, s63\n"
"v_mov_b32 v46, s70\n"
"v_cndmask_b32_e64 v46, 0, v46, s[66:67]\n"
"v_add_u32 v47, v47, v46\n"
"s_waitcnt lgkmcnt(4)\n"
"v_max3_f32 v2, v20, v22, v24\n"
"v_max_f32 v2, v2, v26\n"
"v_sub_f32 v20, v20, v2\n"
"v_sub_f32 v22, v22, v2\n"
"v_sub_f32 v24, v24, v2\n"
"v_sub_f32 v26, v26, v2\n"
"v_exp_f32 v20, v20\n"
"v_exp_f32 v22, v22\n"
"v_exp_f32 v24, v24\n"
"v_exp_f32 v26, v26\n"
"v_mul_f32 v3, v20, v21\n"
"v_fmac_f32 v3, v22, v23\n"
"v_fmac_f32 v3, v24, v25\n"
"v_fmac_f32 v3, v26, v27\n"
"s_waitcnt lgkmcnt(0)\n"
"v_mul_f32 v21, v20, v28\n"
"v_fmac_f32 v21, v22, v29\n"
"v_fmac_f32 v21, v24, v30\n"
"v_fmac_f32 v21, v26, v31\n"
"v_cndmask_b32_e64 v2, v2, v3, s[34:35]\n"
"v_cndmask_b32_e64 v21, v21, v2, s[66:67]\n"
"v_or_b32 v21, 1, v21\n"
"s_mov_b64 exec, s[50:51]\n"
"global_store_dword v47, v21, s[22:23] sc1\n"
"s_mov_b64 exec, -1\n"
"s_branch .Lst_end\n"
"s_nop 7\n"
"s_nop 3\n"
".Lst_qslow_1:\n"
"s_mov_b32 s60, 0\n"
".Lst_qloop_3:\n"
"v_and_b32 v47, 0xff, v0\n"
"v_lshl_add_u32 v47, v47, 1, s56\n"
"global_load_dwordx4 v[4:7], v47, s[22:23] sc1\n"
"global_load_dwordx4 v[8:11], v47, s[22:23] offset:16 sc1\n"
"s_waitcnt vmcnt(0)\n"
"v_bitop3_b32 v2, v4, v5, v6 bitop3:0x80\n"
"v_bitop3_b32 v3, v7, v8, v9 bitop3:0x80\n"
"v_bitop3_b32 v2, v2, v10, v11 bitop3:0x80\n"
"v_bitop3_b32 v2, v2, 1, v3 bitop3:0x80\n"
"v_cmp_ne_u32 vcc, 0, v2\n"
"s_nop 1\n"
"s_cmp_eq_u64 vcc, -1\n"
"s_cbranch_scc1 .Lst_qback_4\n"
"s_add_u32 s60, s60, 1\n"
"s_cmp_lt_u32 s60, 1000000\n"
"s_cbranch_scc1 .Lst_qloop_3\n"
"s_branch .Lst_abort\n"
".Lst_qback_4:\n"
"s_branch .Lst_qok_2\n"
"s_nop 7\n"
"s_nop 3\n"
".Lst_qslow_5:\n"
"s_mov_b32 s60, 0\n"
".Lst_qloop_7:\n"
"v_and_b32 v47, 0xff, v0\n"
"v_lshl_add_u32 v47, v47, 1, s56\n"
"global_load_dwordx4 v[4:7], v47, s[22:23] sc1\n"
"global_load_dwordx4 v[8:11], v47, s[22:23] offset:16 sc1\n"
"s_waitcnt vmcnt(0)\n"
"v_bitop3_b32 v2, v4, v5, v6 bitop3:0x80\n"
"v_bitop3_b32 v3, v7, v8, v9 bitop3:0x80\n"
"v_bitop3_b32 v2, v2, v10, v11 bitop3:0x80\n"
"v_bitop3_b32 v2, v2, 1, v3 bitop3:0x80\n"
"v_cmp_ne_u32 vcc, 0, v2\n"
"s_nop 1\n"
"s_cmp_eq_u64 vcc, -1\n"
"s_cbranch_scc1 .Lst_qback_8\n"
"s_add_u32 s60, s60, 1\n"
"s_cmp_lt_u32 s60, 1000000\n"
"s_cbranch_scc1 .Lst_qloop_7\n"
"s_branch .Lst_abort\n"
".Lst_qback_8:\n"
"s_branch .Lst_qok_6\n"
"s_nop 7\n"
"s_nop 3\n"
".Lst_qslow_66:\n"
"s_mov_b32 s60, 0\n"
".Lst_qloop_68:\n"
"v_and_b32 v47, 0xff, v0\n"
"v_lshl_add_u32 v47, v47, 1, s57\n"
"global_load_dwordx4 v[4:7], v47, s[22:23] sc1\n"
"global_load_dwordx4 v[8:11], v47, s[22:23] offset:16 sc1\n"
"s_waitcnt vmcnt(0)\n"
"v_bitop3_b32 v2, v4, v5, v6 bitop3:0x80\n"
"v_bitop3_b32 v3, v7, v8, v9 bitop3:0x80\n"
"v_bitop3_b32 v2, v2, v10, v11 bitop3:0x80\n"
"v_bitop3_b32 v2, v2, 1, v3 bitop3:0x80\n"
"v_cmp_ne_u32 vcc, 0, v2\n"
"s_nop 1\n"
"s_cmp_eq_u64 vcc, -1\n"
"s_cbranch_scc1 .Lst_qback_69\n"
"s_add_u32 s60, s60, 1\n"
"s_cmp_lt_u32 s60, 1000000\n"
"s_cbranch_scc1 .Lst_qloop_68\n"
"s_branch .Lst_abort\n"
".Lst_qback_69:\n"
"s_branch .Lst_qok_67\n"
"s_nop 7\n"
"s_nop 3\n"
".Lst_qslow_70:\n"
"s_mov_b32 s60, 0\n"
".Lst_qloop_72:\n"
"v_and_b32 v47, 0xff, v0\n"
"v_lshl_add_u32 v47, v47, 1, s56\n"
"global_load_dwordx4 v[4:7], v47, s[22:23] sc1\n"
"global_load_dwordx4 v[8:11], v47, s[22:23] offset:16 sc1\n"
"s_waitcnt vmcnt(0)\n"
"v_bitop3_b32 v2, v4, v5, v6 bitop3:0x80\n"
"v_bitop3_b32 v3, v7, v8, v9 bitop3:0x80\n"
"v_bitop3_b32 v2, v2, v10, v11 bitop3:0x80\n"
"v_bitop3_b32 v2, v2, 1, v3 bitop3:0x80\n"
"v_cmp_ne_u32 vcc, 0, v2\n"
"s_nop 1\n"
"s_cmp_eq_u64 vcc, -1\n"
"s_cbranch_scc1 .Lst_qback_73\n"
"s_add_u32 s60, s60, 1\n"
"s_cmp_lt_u32 s60, 1000000\n"
"s_cbranch_scc1 .Lst_qloop_72\n"
"s_branch .Lst_abort\n"
".Lst_qback_73:\n"
"s_branch .Lst_qok_71\n"
"s_nop 7\n"
"s_nop 3\n"
".Lst_qslow_131:\n"
"s_mov_b32 s60, 0\n"
".Lst_qloop_133:\n"
"v_and_b32 v47, 0xff, v0\n"
"v_lshl_add_u32 v47, v47, 1, s57\n"
"global_load_dwordx4 v[4:7], v47, s[22:23] sc1\n"
"global_load_dwordx4 v[8:11], v47, s[22:23] offset:16 sc1\n"
"s_waitcnt vmcnt(0)\n"
"v_bitop3_b32 v2, v4, v5, v6 bitop3:0x80\n"
"v_bitop3_b32 v3, v7, v8, v9 bitop3:0x80\n"
"v_bitop3_b32 v2, v2, v10, v11 bitop3:0x80\n"
"v_bitop3_b32 v2, v2, 1, v3 bitop3:0x80\n"
"v_cmp_ne_u32 vcc, 0, v2\n"
"s_nop 1\n"
"s_cmp_eq_u64 vcc, -1\n"
"s_cbranch_scc1 .Lst_qback_134\n"
"s_add_u32 s60, s60, 1\n"
"s_cmp_lt_u32 s60, 1000000\n"
"s_cbranch_scc1 .Lst_qloop_133\n"
"s_branch .Lst_abort\n"
".Lst_qback_134:\n"
"s_branch .Lst_qok_132\n"
"s_nop 7\n"
"s_nop 3\n"
".Lst_qslow_135:\n"
"s_mov_b32 s60, 0\n"
".Lst_qloop_137:\n"
"v_and_b32 v47, 0xff, v0\n"
"v_lshl_add_u32 v47, v47, 1, s57\n"
"global_load_dwordx4 v[4:7], v47, s[22:23] sc1\n"
"global_load_dwordx4 v[8:11], v47, s[22:23] offset:16 sc1\n"
"s_waitcnt vmcnt(0)\n"
"v_bitop3_b32 v2, v4, v5, v6 bitop3:0x80\n"
"v_bitop3_b32 v3, v7, v8, v9 bitop3:0x80\n"
"v_bitop3_b32 v2, v2, v10, v11 bitop3:0x80\n"
"v_bitop3_b32 v2, v2, 1, v3 bitop3:0x80\n"
"v_cmp_ne_u32 vcc, 0, v2\n"
"s_nop 1\n"
"s_cmp_eq_u64 vcc, -1\n"
"s_cbranch_scc1 .Lst_qback_138\n"
"s_add_u32 s60, s60, 1\n"
"s_cmp_lt_u32 s60, 1000000\n"
"s_cbranch_scc1 .Lst_qloop_137\n"
"s_branch .Lst_abort\n"
".Lst_qback_138:\n"
"s_branch .Lst_qok_136\n"
".Lst_abort:\n"
"v_mov_b32 v2, 262144\n"
"v_mov_b32 v3, 1\n"
"global_store_dword v2, v3, s[22:23] sc1\n"
".Lst_end:\n"
"s_waitcnt vmcnt(0)\n"
"s_endpgm\n"

    :: "s"(kb), "s"(vb), "s"(p.ws), "s"(dbg), "s"(flags), "s"(q0off), "s"(p0off), "s"(pf0), "s"(pf1), "s"(pf2), "s"(pf3), "v"(koff), "v"(voff)
    : "memory", "vcc", "scc", "v8", "v9", "v10", "v11", "v12", "v13", "v14", "v15", "v16", "v17", "v18", "v19", "v20", "v21", "v22", "v23", "v24", "v25", "v26", "v27", "v28", "v29", "v30", "v31", "v32", "v33", "v34", "v35", "v36", "v37", "v38", "v39", "v40", "v41", "v42", "v43", "v44", "v45", "v46", "v47", "v48", "v49", "v50", "v51", "v52", "v53", "v54", "v55", "v56", "v57", "v58", "v59", "v60", "v61", "v62", "v63", "v64", "v65", "v66", "v67", "v68", "v69", "v70", "v71", "v72", "v73", "v74", "v75", "v76", "v77", "v78", "v79", "v80", "v81", "v82", "v83", "v84", "v85", "v86", "v87", "v88", "v89", "v90", "v91", "v92", "v93", "v94", "v95", "v96", "v97", "v98", "v99", "v100", "v101", "v102", "v103", "v104", "v105", "v106", "v107", "v108", "v109", "v110", "v111", "v112", "v113", "v114", "v115", "v116", "v117", "v118", "v119", "v120", "v121", "v122", "v123", "v124", "v125", "v126", "v127", "v128", "v129", "v130", "v131", "v132", "v133", "v134", "v135", "v136", "v137", "v138", "v139", "v140", "v141", "v142", "v143", "v144", "v145", "v146", "v147", "v148", "v149", "v150", "v151", "v152", "v153", "v154", "v155", "v156", "v157", "v158", "v159", "v160", "v161", "v162", "v163", "v164", "v165", "v166", "v167", "v168", "v169", "v170", "v171", "v172", "v173", "v174", "v175", "v176", "v177", "v178", "v179", "v180", "v181", "v182", "v183", "v184", "v185", "v186", "v187", "v188", "v189", "v190", "v191", "v192", "v193", "v194", "v195", "v196", "v197", "v198", "v199", "v200", "v201", "v202", "v203", "v204", "v205", "v206", "v207", "v208", "v209", "v210", "v211", "v212", "v213", "v214", "v215", "v216", "v217", "v218", "v219", "v220", "v221", "v222", "v223", "v224", "v225", "v226", "v227", "v228", "v229", "v230", "v231", "v232", "v233", "v234", "v235", "v236", "v237", "v238", "v239", "v240", "v241", "v242", "v243", "v244", "v245", "v246", "v247", "v248", "v249", "v250", "v251", "v252", "v253", "v254", "v255", "a0", "a1", "a2", "a3", "a4", "a5", "a6", "a7", "a8", "a9", "a10", "a11", "a12", "a13", "a14", "a15", "a16", "a17", "a18", "a19", "a20", "a21", "a22", "a23", "a24", "a25", "a26", "a27", "a28", "a29", "a30", "a31", "a32", "a33", "a34", "a35", "a36", "a37", "a38", "a39", "a40", "a41", "a42", "a43", "a44", "a45", "a46", "a47", "a48", "a49", "a50", "a51", "a52", "a53", "a54", "a55", "a56", "a57", "a58", "a59", "a60", "a61", "a62", "a63", "a64", "a65", "a66", "a67", "a68", "a69", "a70", "a71", "a72", "a73", "a74", "a75", "a76", "a77", "a78", "a79", "a80", "a81", "a82", "a83", "a84", "a85", "a86", "a87", "a88", "a89", "a90", "a91", "a92", "a93", "a94", "a95", "a96", "a97", "a98", "a99", "a100", "a101", "a102", "a103", "a104", "a105", "a106", "a107", "a108", "a109", "a110", "a111", "a112", "a113", "a114", "a115", "a116", "a117", "a118", "a119", "a120", "a121", "a122", "a123", "a124", "a125", "a126", "a127", "a128", "a129", "a130", "a131", "a132", "a133", "a134", "a135", "a136", "a137", "a138", "a139", "a140", "a141", "a142", "a143", "a144", "a145", "a146", "a147", "a148", "a149", "a150", "a151", "a152", "a153", "a154", "a155", "a156", "a157", "a158", "a159", "a160", "a161", "a162", "a163", "a164", "a165", "a166", "a167", "a168", "a169", "a170", "a171", "a172", "a173", "a174", "a175", "a176", "a177", "a178", "a179", "a180", "a181", "a182", "a183", "a184", "a185", "a186", "a187", "a188", "a189", "a190", "a191", "a192", "a193", "a194", "a195", "a196", "a197", "a198", "a199", "a200", "a201", "a202", "a203", "a204", "a205", "a206", "a207", "a208", "a209", "a210", "a211", "a212", "a213", "a214", "a215", "a216", "a217", "a218", "a219", "a220", "a221", "a222", "a223", "a224", "a225", "a226", "a227", "a228", "a229", "a230", "a231", "a232", "a233", "a234", "a235", "a236", "a237", "a238", "a239", "a240", "a241", "a242", "a243", "a244", "a245", "a246", "a247", "a248", "a249", "a250", "a251", "a252", "a253", "a254", "a255", "s16", "s17", "s18", "s19", "s20", "s21", "s22", "s23", "s24", "s25", "s26", "s27", "s28", "s29", "s30", "s31", "s32", "s33", "s34", "s35", "s36", "s37", "s38", "s39", "s40", "s41", "s42", "s43", "s44", "s45", "s46", "s47", "s48", "s49", "s50", "s51", "s52", "s53", "s54", "s55", "s56", "s57", "s58", "s59", "s60", "s61", "s62", "s63", "s64", "s65", "s66", "s67", "s68", "s69", "s70", "s71", "s72", "s73", "s74", "s75", "s76", "s77", "s78", "s79", "s80", "s81", "s82", "s83", "s84", "s85", "s86", "s87", "s88", "s89", "s90", "s91", "s92", "s93");
}

__global__ __launch_bounds__(256, 1) void fused_kernel(Params p) {
  extern __shared__ __attribute__((aligned(16))) char lds_raw[];
  LAS char* L = (LAS char*)lds_raw;
  asm volatile("s_waitcnt lgkmcnt(0)" :: "s"(p.x), "s"(p.K), "s"(p.V), "s"(p.wq), "s"(p.wo), "s"(p.w1), "s"(p.w2), "s"(p.out), "s"(p.ws) : "memory");
  if (blockIdx.x < NCHAIN) chain_role(p, L); else stream_role(p);
}

extern "C" void kernel_launch(void* const* d_in, const int* in_sizes, int n_in, void* d_out, int out_size, void* d_ws, size_t ws_size, hipStream_t stream) {
  Params p;
  p.x = (const u16*)d_in[0]; p.K = (const u16*)d_in[1]; p.V = (const u16*)d_in[2]; p.wq = (const u16*)d_in[3]; p.wo = (const u16*)d_in[4];
  p.w1 = (const u16*)d_in[6]; p.w2 = (const u16*)d_in[7]; p.out = (u16*)d_out; p.ws = (unsigned char*)d_ws;
  constexpr int LDS_BYTES = 160 * 1024;
  (void)hipFuncSetAttribute((const void*)fused_kernel, hipFuncAttributeMaxDynamicSharedMemorySize, LDS_BYTES);
  (void)hipMemsetAsync((char*)d_ws + WS_EARLY, 0, WS_EARLY_END - WS_EARLY, stream);
  hipLaunchKernelGGL(fused_kernel, dim3(256), dim3(256), LDS_BYTES, stream, p);
}
```
